```python
import torch

def _softmax(x, axis=-1):
    return torch.softmax(x, dim=axis)
def _gelu(x):
    return torch.nn.functional.gelu(x, approximate="tanh")
def _silu(x):
    return torch.nn.functional.silu(x)
def _sigmoid(x):
    return torch.sigmoid(x)
def _layernorm(x, axis=-1, eps=1e-5):
    m = x.mean(dim=axis, keepdim=True)
    v = x.var(dim=axis, keepdim=True, unbiased=False)
    return (x - m) / torch.sqrt(v + eps)
def _scatter_add(tgt, idx, src, axis=0):
    return tgt.clone().index_add_(axis, idx, src)
def _segment_sum(src, seg, n):
    out = torch.zeros((n,) + src.shape[1:], dtype=src.dtype)
    return out.index_add_(0, seg, src)
def _pad(x, pw):
    flat = tuple(v for pair in reversed(pw) for v in pair)
    return torch.nn.functional.pad(x, flat)

kernel_name = 'interleave_layers__linear_attn_block__swiglu_ffn__prompt'

def setup_inputs():

    _w = (lambda a: (lambda t: t.to(torch.bfloat16) if t.is_floating_point() else t)(torch.as_tensor(a)))
    def _A_setup():
        B, S, D = 1, 2048, 640
        return {
            "x": _w((lambda *s: torch.randn(*s, dtype=torch.bfloat16) * 0.05)(B, S, D)),
            "wqkv": _w((lambda *s: torch.randn(*s, dtype=torch.bfloat16) * 0.05)(D, 3 * D)),
            "wo": _w((lambda *s: torch.randn(*s, dtype=torch.bfloat16) * 0.05)(D, D)),
        }
    _A_in = _A_setup()

    def _B_setup():
        B, S, D, F = 1, 2048, 640, 1728
        return {
            "x": _w((lambda *s: torch.randn(*s, dtype=torch.bfloat16) * 0.05)(B, S, D)),
            "w_gate": _w((lambda *s: torch.randn(*s, dtype=torch.bfloat16) * 0.05)(D, F)),
            "w_up": _w((lambda *s: torch.randn(*s, dtype=torch.bfloat16) * 0.05)(D, F)),
            "w_down": _w((lambda *s: torch.randn(*s, dtype=torch.bfloat16) * 0.05)(F, D)),
        }
    _B_in = _B_setup()

    return {
        "A_x": _A_in["x"],
        "A_wqkv": _A_in["wqkv"],
        "A_wo": _A_in["wo"],
        "B_x": _B_in["x"],
        "B_w_gate": _B_in["w_gate"],
        "B_w_up": _B_in["w_up"],
        "B_w_down": _B_in["w_down"],
    }

def reference(A_x, A_wqkv, A_wo, B_x, B_w_gate, B_w_up, B_w_down):
    def _A_ref(x, wqkv, wo):
        B, S, D = x.shape
        H = 32; Dh = D // H
        qkv = (lambda x, *d: x.permute(*d))((x @ wqkv).reshape(B, S, 3, H, Dh), 2, 0, 3, 1, 4)
        q, k, v = qkv[0], qkv[1], qkv[2]
        phi = lambda t: torch.where(t > 0, t + 1, torch.exp(t)) + 1e-6
        pq, pk = phi(q), phi(k)
        kv = torch.einsum("bhsd,bhse->bhde", pk, v)
        z = torch.einsum("bhsd->bhd", pk)
        num = torch.einsum("bhqd,bhde->bhqe", pq, kv)
        den = torch.einsum("bhqd,bhd->bhq", pq, z)[..., None] + 1e-6
        o = num / den
        return (lambda x, *d: x.permute(*d))(o, 0, 2, 1, 3).reshape(B, S, D) @ wo

    def _B_ref(x, w_gate, w_up, w_down):
        g = _silu(x @ w_gate)
        u = x @ w_up
        return (g * u) @ w_down

    x = A_x
    x = x + _A_ref(_layernorm(x, axis=-1), A_wqkv, A_wo)
    x = x + _B_ref(_layernorm(x, axis=-1), B_w_gate, B_w_up, B_w_down)
    x = x + _A_ref(_layernorm(x, axis=-1), A_wqkv, A_wo)
    x = x + _B_ref(_layernorm(x, axis=-1), B_w_gate, B_w_up, B_w_down)
    return x
```

```cpp
#include <hip/hip_runtime.h>
#include <hip/hip_bf16.h>
#include <stdint.h>
namespace naive {
constexpr int S = 2048, D = 640, H = 32, DH = 20, F = 1728;
typedef unsigned short u16;
__device__ __forceinline__ float b2f(u16 b) { return __builtin_bit_cast(float, (unsigned)b << 16); }
__device__ __forceinline__ u16 f2b(float f) { __hip_bfloat16 h = __float2bfloat16(f); return __builtin_bit_cast(u16, h); }

__global__ void k_ln(const u16* x, u16* y) {
  int r = blockIdx.x, t = threadIdx.x;
  float v[10]; float s = 0;
  for (int i = 0; i < 10; i++) { v[i] = b2f(x[r * D + t + 64 * i]); s += v[i]; }
  for (int o = 32; o; o >>= 1) s += __shfl_xor(s, o);
  float m = s / D, q = 0;
  for (int i = 0; i < 10; i++) { float d = v[i] - m; q += d * d; }
  for (int o = 32; o; o >>= 1) q += __shfl_xor(q, o);
  float rs = 1.0f / sqrtf(q / D + 1e-5f);
  for (int i = 0; i < 10; i++) y[r * D + t + 64 * i] = f2b((v[i] - m) * rs);
}
__global__ void k_gemm(const u16* A, const u16* B, const u16* R, u16* C, int N, int K) {
  int n = blockIdx.x * 64 + threadIdx.x, m0 = blockIdx.y * 8;
  float acc[8] = {0,0,0,0,0,0,0,0};
  for (int k = 0; k < K; k++) { float b = b2f(B[k * N + n]);
#pragma unroll
    for (int i = 0; i < 8; i++) acc[i] += b2f(A[(m0 + i) * K + k]) * b; }
#pragma unroll
  for (int i = 0; i < 8; i++) { float v = acc[i]; if (R) v = b2f(f2b(v)) + b2f(R[(m0 + i) * N + n]); C[(m0 + i) * N + n] = f2b(v); }
}
__device__ __forceinline__ float phi(float t) { return b2f(f2b((t > 0 ? t + 1.0f : expf(t)) + 1e-6f)); }
__global__ void k_kv(const u16* qkv, float* kv) {
  int h = blockIdx.x, t = threadIdx.x; if (t >= 420) return;
  int d = t / 21, e = t % 21; float a = 0;
  for (int s = 0; s < S; s++) { float pk = phi(b2f(qkv[s * 1920 + 640 + h * 20 + d])); float v = e < 20 ? b2f(qkv[s * 1920 + 1280 + h * 20 + e]) : 1.0f; a += pk * v; }
  kv[h * 420 + t] = a;
}
__global__ void k_o(const u16* qkv, const float* kv, u16* o) {
  int s = blockIdx.x, c = threadIdx.x, h = c / 20, e = c % 20; float num = 0, den = 0;
  for (int d = 0; d < 20; d++) { float pq = phi(b2f(qkv[s * 1920 + h * 20 + d])); num += pq * kv[h * 420 + d * 21 + e]; den += pq * kv[h * 420 + d * 21 + 20]; }
  o[s * D + c] = f2b(num / (den + 1e-6f));
}
__global__ void k_act(const u16* g, const u16* u, u16* h) { int i = blockIdx.x * 256 + threadIdx.x; float a = b2f(g[i]); h[i] = f2b(b2f(f2b(a / (1.0f + expf(-a)))) * b2f(u[i])); }

inline void run(void* const* d_in, void* d_out, void* d_ws, hipStream_t st) {
  const u16 *x0 = (const u16*)d_in[0], *wqkv = (const u16*)d_in[1], *wo = (const u16*)d_in[2], *wg = (const u16*)d_in[4], *wu = (const u16*)d_in[5], *wd = (const u16*)d_in[6];
  char* w = (char*)d_ws;
  u16 *xn = (u16*)w, *qkv = (u16*)(w + (4 << 20)), *o = (u16*)(w + (16 << 20)), *x1 = (u16*)(w + (20 << 20)), *g = (u16*)(w + (24 << 20)), *u = (u16*)(w + (32 << 20)), *h = (u16*)(w + (40 << 20)), *x2 = (u16*)(w + (48 << 20));
  float* kv = (float*)(w + (52 << 20));
  const u16* x = x0;
  for (int l = 0; l < 2; l++) {
    k_ln<<<S, 64, 0, st>>>(x, xn);
    k_gemm<<<dim3(1920 / 64, S / 8), 64, 0, st>>>(xn, wqkv, nullptr, qkv, 1920, 640);
    k_kv<<<32, 448, 0, st>>>(qkv, kv);
    k_o<<<S, 640, 0, st>>>(qkv, kv, o);
    k_gemm<<<dim3(10, S / 8), 64, 0, st>>>(o, wo, x, x1, 640, 640);
    k_ln<<<S, 64, 0, st>>>(x1, xn);
    k_gemm<<<dim3(27, S / 8), 64, 0, st>>>(xn, wg, nullptr, g, F, 640);
    k_gemm<<<dim3(27, S / 8), 64, 0, st>>>(xn, wu, nullptr, u, F, 640);
    k_act<<<S * F / 256, 256, 0, st>>>(g, u, h);
    u16* dst = l == 0 ? x2 : (u16*)d_out;
    k_gemm<<<dim3(10, S / 8), 64, 0, st>>>(h, wd, x1, dst, 640, F);
    x = x2;
  }
}
}
extern "C" void kernel_launch(void* const* d_in, const int* in_sizes, int n_in, void* d_out, int out_size, void* d_ws, size_t ws_size, hipStream_t stream) {
  naive::run(d_in, d_out, d_ws, stream);
}
```

```cpp
#include <hip/hip_runtime.h>
#include <hip/hip_bf16.h>

__global__ void __launch_bounds__(256, 1)
fused_block_kernel(const void* x, const void* wq, const void* wo, const void* wg, const void* wu, const void* wd, void* out, void* ws) {
  register int tid asm("v0") = threadIdx.x;
  asm volatile(
"; set-up\n"
"s_load_dwordx16 s[80:95], %[ka], 0\n"
"s_lshr_b32 s60, %[b], 3\n"
"s_getpc_b64 s[64:65]\n"
"Lpc_%=:\n"
"v_and_b32 v220, 31, v0\n"
"v_lshl_add_u32 v220, s60, 5, v220\n"
"v_lshlrev_b32 v220, 7, v220\n"
"v_min_u32 v220, Lend_%=-Lpc_%=, v220\n"
"global_load_dword a255, v220, s[64:65]\n"
"s_and_b32 s60, s60, 3\n"
"s_lshl_b32 s56, %[b], 1\n"
"s_and_b32 s56, s56, 12\n"
"s_or_b32 s56, s56, s60\n"
"s_lshl_b32 s57, %[b], 3\n"
"s_and_b32 s57, s57, 8\n"
"s_lshr_b32 s60, %[b], 5\n"
"s_or_b32 s57, s57, s60\n"
"v_lshrrev_b32 v246, 6, v0\n"
"v_and_b32 v198, 15, v0\n"
"v_readfirstlane_b32 s58, v246\n"
"v_cmp_gt_u32 s[74:75], 2, v246\n"
"v_mov_b32 v247, 1073741824\n"
"v_cmp_gt_u32 vcc, 15, v198\n"
"v_lshrrev_b32 v54, 4, v0\n"
"v_mad_i32_i24 v56, v198, 52, 0\n"
"v_lshrrev_b32 v55, 8, v56\n"
"s_mul_i32 s4, s56, 1920\n"
"s_add_u32 s5, s4, s57\n"
"s_mul_i32 s6, s5, 80\n"
"v_mul_u32_u24 v218, 3840, v54\n"
"v_mul_u32_u24 v57, 1200, v55\n"
"v_add_u32 v218, v218, v57\n"
"v_lshl_add_u32 v218, v198, 4, v218\n"
"v_add_u32 v218, s6, v218\n"
"v_cndmask_b32 v218, v247, v218, vcc\n"
"s_lshl_b32 s59, s58, 10\n"
"v_and_b32 v200, 63, v0\n"
"v_bfe_u32 v199, v0, 4, 2\n"
"v_cndmask_b32 v219, v247, v218, s[74:75]\n"
"s_mov_b32 s30, 2457600\n"
"s_mov_b32 s31, 131072\n"
"s_mov_b32 s22, 46137344\n"
"s_mov_b32 s23, 131072\n"
"s_mov_b32 s26, 2621440\n"
"s_mov_b32 s27, 131072\n"
"v_mov_b32 v245, 2097152\n"
"v_lshrrev_b32 v58, 3, v200\n"
"v_and_b32 v60, 7, v200\n"
"v_xor_b32 v59, v199, v60\n"
"s_lshl2_add_u32 s7, s56, s58\n"
"s_mul_i32 s8, s7, 40960\n"
"v_mul_u32_u24 v220, 1280, v58\n"
"v_lshl_add_u32 v220, v59, 4, v220\n"
"v_add_u32 v220, s8, v220\n"
"v_xor_b32 v61, 64, v220\n"
"v_add_u32 v221, 10240, v61\n"
"v_cmp_gt_u32 vcc, 40, v200\n"
"s_and_b32 s9, s57, 7\n"
"s_lshl3_add_u32 s10, s56, s9\n"
"s_lshl2_add_u32 s11, s10, s58\n"
"s_mul_i32 s12, s11, 5120\n"
"v_lshl_add_u32 v244, v200, 7, s12\n"
"v_cndmask_b32 v244, v247, v244, vcc\n"
"s_waitcnt lgkmcnt(0)\n"
"s_mov_b64 s[28:29], s[82:83]\n"
"s_add_u32 m0, s59, 143360\n"
"s_mov_b32 s73, 0\n"
"buffer_load_dwordx4 v218, s[28:31], s73 offen lds\n"
"s_add_u32 m0, s59, 147456\n"
"s_mov_b32 s73, 61440\n"
"buffer_load_dwordx4 v218, s[28:31], s73 offen lds\n"
"s_add_u32 m0, s59, 151552\n"
"s_mov_b32 s73, 122880\n"
"buffer_load_dwordx4 v219, s[28:31], s73 offen lds\n"
"s_mov_b64 s[20:21], s[94:95]\n"
"s_mov_b64 s[24:25], s[80:81]\n"
"buffer_load_dword a254, v244, s[24:27], 0 offen\n"
"buffer_load_dwordx2 v[8:9], v245, s[20:23], 0 offen sc1\n"
"s_add_u32 m0, s59, 56448\n"
"s_nop 0\n"
"buffer_load_dwordx4 v220, s[24:27], 0 offen offset:896 lds\n"
"s_mov_b64 s[36:37], s[84:85]\n"
"s_mov_b64 s[40:41], s[86:87]\n"
"s_mov_b64 s[44:45], s[88:89]\n"
"s_mov_b64 s[48:49], s[90:91]\n"
"s_mov_b64 s[52:53], s[92:93]\n"
"s_mov_b32 s84, 20480\n"
"s_mul_i32 s13, s57, 163840\n"
"s_add_u32 s64, s13, 2101248\n"
"s_cmp_eq_u32 s58, 2\n"
"s_cselect_b32 s15, 40, 8\n"
"v_and_b32 v63, 3, v198\n"
"v_mul_u32_u24 v62, s15, v63\n"
"v_lshrrev_b32 v64, 2, v198\n"
"s_add_u32 m0, s59, 60544\n"
"s_nop 0\n"
"buffer_load_dwordx4 v221, s[24:27], 0 offen offset:896 lds\n"
"s_lshl_b32 s16, s58, 3\n"
"s_lshr_b32 s17, 678469712, s16\n"
"s_and_b32 s4, s17, 255\n"
"s_add_u32 s5, s4, 143360\n"
"v_add_u32 v222, s5, v62\n"
"v_lshl_add_u32 v222, v64, 8, v222\n"
"s_lshl_b32 s6, s58, 1\n"
"s_cmp_lt_u32 s6, 5\n"
"s_cselect_b32 s7, 5, 3\n"
"s_mul_i32 s8, s56, 5\n"
"v_add_u32 v67, s8, v199\n"
"v_lshrrev_b32 v66, 2, v67\n"
"v_mul_u32_u24 v65, s7, v66\n"
"v_and_b32 v68, 3, v67\n"
"s_add_u32 m0, s59, 64512\n"
"s_nop 0\n"
"buffer_load_dwordx4 v220, s[24:27], 0 offen offset:1024 lds\n"
"s_cmp_lt_u32 s6, 5\n"
"s_cselect_b32 s9, 0, 95\n"
"s_lshl1_add_u32 s10, s58, s9\n"
"s_lshl_b32 s11, s10, 10\n"
"s_add_u32 s12, s11, s64\n"
"v_lshl_add_u32 v226, v65, 10, s12\n"
"v_lshl_add_u32 v226, v68, 8, v226\n"
"v_lshl_add_u32 v226, v198, 4, v226\n"
"s_add_u32 s13, s8, 4\n"
"s_and_b32 s14, s13, 3\n"
"s_lshr_b32 s15, s13, 2\n"
"s_mul_i32 s16, s15, s7\n"
"s_lshl1_add_u32 s17, s58, s16\n"
"s_add_u32 s4, s17, s9\n"
"s_add_u32 m0, s59, 68608\n"
"s_nop 0\n"
"buffer_load_dwordx4 v221, s[24:27], 0 offen offset:1024 lds\n"
"s_lshl2_add_u32 s5, s4, s14\n"
"s_lshl_b32 s6, s5, 8\n"
"s_add_u32 s10, s6, s64\n"
"v_lshl_add_u32 v228, v198, 4, s10\n"
"s_cmp_eq_u32 s58, 3\n"
"s_cselect_b32 s11, 40, 8\n"
"v_mul_u32_u24 v69, s11, v63\n"
"s_lshl_b32 s12, s58, 3\n"
"s_lshr_b32 s8, 536922232, s12\n"
"s_and_b32 s14, s8, 255\n"
"s_add_u32 s9, s14, 143360\n"
"v_add_u32 v223, s9, v69\n"
"v_lshl_add_u32 v223, v64, 8, v223\n"
"s_lshl_b32 s13, s58, 1\n"
"s_add_u32 m0, s59, 72576\n"
"s_nop 0\n"
"buffer_load_dwordx4 v220, s[24:27], 0 offen offset:1152 lds\n"
"s_add_u32 s15, s13, 1\n"
"s_cmp_lt_u32 s15, 5\n"
"s_cselect_b32 s7, 5, 3\n"
"v_mul_u32_u24 v70, s7, v66\n"
"s_cmp_lt_u32 s15, 5\n"
"s_cselect_b32 s16, 0, 95\n"
"s_lshl1_add_u32 s17, s58, s16\n"
"s_lshl_b32 s4, s17, 10\n"
"s_add_u32 s5, s4, s64\n"
"s_add_u32 s6, s5, 1024\n"
"v_lshl_add_u32 v227, v70, 10, s6\n"
"v_lshl_add_u32 v227, v68, 8, v227\n"
"v_lshl_add_u32 v227, v198, 4, v227\n"
"s_mul_i32 s10, s56, 5\n"
"s_add_u32 s11, s10, 4\n"
"s_add_u32 m0, s59, 76672\n"
"s_nop 0\n"
"buffer_load_dwordx4 v221, s[24:27], 0 offen offset:1152 lds\n"
"s_and_b32 s12, s11, 3\n"
"s_lshr_b32 s8, s11, 2\n"
"s_mul_i32 s14, s8, s7\n"
"s_lshl1_add_u32 s9, s58, s14\n"
"s_add_u32 s13, s9, s16\n"
"s_lshl2_add_u32 s15, s13, s12\n"
"s_lshl_b32 s17, s15, 8\n"
"s_add_u32 s4, s17, s64\n"
"s_add_u32 s5, s4, 1024\n"
"v_lshl_add_u32 v229, v198, 4, s5\n"
"v_cmp_eq_u32 vcc, 1, v199\n"
"s_nop 1\n"
"v_cndmask_b32 v71, v222, v223, vcc\n"
"s_add_u32 m0, s59, 0\n"
"s_nop 0\n"
"buffer_load_dwordx4 v220, s[24:27], 0 offen lds\n"
"v_add_u32 v224, 8192, v71\n"
"v_cndmask_b32 v230, v228, v229, vcc\n"
"v_cmp_gt_u32 vcc, 2, v199\n"
"s_nop 1\n"
"v_cndmask_b32 v230, v247, v230, vcc\n"
"s_lshl4_add_u32 s6, s57, s56\n"
"s_mul_i32 s10, s6, 12\n"
"s_add_u32 s12, s10, s58\n"
"s_lshl_b32 s16, s12, 7\n"
"s_add_u32 s11, s16, 12357632\n"
"v_lshl_add_u32 v231, v200, 2, s11\n"
"v_cndmask_b32 v231, v247, v231, vcc\n"
"v_lshl_add_u32 v222, v199, 11, v222\n"
"v_lshl_add_u32 v223, v199, 11, v223\n"
"s_add_u32 m0, s59, 4096\n"
"s_nop 0\n"
"buffer_load_dwordx4 v221, s[24:27], 0 offen lds\n"
"v_cmp_eq_u32 vcc, 0, v64\n"
"v_mov_b32 v72, 1065369472\n"
"s_nop 0\n"
"v_cndmask_b32 v86, 0, v72, vcc\n"
"v_mov_b32 v87, v86\n"
"v_mov_b32 v88, v86\n"
"v_mov_b32 v89, v86\n"
"v_cmp_eq_u32 vcc, 1, v64\n"
"s_nop 1\n"
"v_cndmask_b32 v90, 0, v72, vcc\n"
"v_mov_b32 v91, v90\n"
"v_mov_b32 v92, v90\n"
"v_mov_b32 v93, v90\n"
"v_cmp_eq_u32 vcc, v199, v64\n"
"s_nop 1\n"
"v_cndmask_b32 v102, 0, v72, vcc\n"
"s_add_u32 m0, s59, 8064\n"
"s_nop 0\n"
"buffer_load_dwordx4 v220, s[24:27], 0 offen offset:128 lds\n"
"v_mov_b32 v103, v102\n"
"v_mov_b32 v104, v102\n"
"v_mov_b32 v105, v102\n"
"v_lshrrev_b32 v73, 3, v198\n"
"v_and_b32 v74, 7, v198\n"
"v_lshrrev_b32 v76, 1, v198\n"
"v_xor_b32 v75, v76, v199\n"
"v_lshl_add_u32 v238, v73, 12, s59\n"
"v_lshl_add_u32 v238, v74, 7, v238\n"
"v_lshl_add_u32 v238, v75, 4, v238\n"
"v_xor_b32 v239, 64, v238\n"
"v_add_u32 v240, 65536, v238\n"
"v_add_u32 v241, 65536, v239\n"
"s_add_u32 m0, s59, 12160\n"
"s_nop 0\n"
"buffer_load_dwordx4 v221, s[24:27], 0 offen offset:128 lds\n"
"v_add_u32 v242, 65536, v240\n"
"v_add_u32 v243, 65536, v241\n"
"v_cmp_gt_u32 vcc, 16, v200\n"
"s_lshl4_add_u32 s8, s56, s57\n"
"s_lshl2_add_u32 s7, s8, s58\n"
"s_lshl_b32 s14, s7, 7\n"
"v_lshl_add_u32 v14, v200, 3, s14\n"
"v_cndmask_b32 v14, v247, v14, vcc\n"
"v_cmp_eq_u32 vcc, 0, v199\n"
"s_lshl2_add_u32 s9, s57, s58\n"
"s_lshl_b32 s13, s9, 7\n"
"v_lshl_add_u32 v10, v198, 13, s13\n"
"v_cndmask_b32 v10, v247, v10, vcc\n"
"s_waitcnt vmcnt(12)\n"
"s_barrier\n"
"ds_read_b64_tr_b16 v[110:111], v222\n"
"ds_read_b64_tr_b16 v[112:113], v222 offset:1024\n"
"ds_read_b64_tr_b16 v[114:115], v223\n"
"ds_read_b64_tr_b16 v[116:117], v223 offset:1024\n"
"ds_read_b64_tr_b16 v[118:119], v224\n"
"ds_read_b64_tr_b16 v[120:121], v224 offset:1024\n"
"s_waitcnt lgkmcnt(4)\n"
"buffer_store_dwordx4 v[110:113], v226, s[20:23], 0 offen sc1\n"
"v_mfma_f32_16x16x32_bf16 v[122:125], v[86:89], v[110:113], 0\n"
"s_waitcnt lgkmcnt(2)\n"
"buffer_store_dwordx4 v[114:117], v227, s[20:23], 0 offen sc1\n"
"v_mfma_f32_16x16x32_bf16 v[122:125], v[90:93], v[114:117], v[122:125]\n"
"s_waitcnt lgkmcnt(0)\n"
"buffer_store_dwordx4 v[118:121], v230, s[20:23], 0 offen sc1\n"
"v_mfma_f32_16x16x32_bf16 v[122:125], v[102:105], v[118:121], v[122:125]\n"
"s_waitcnt vmcnt(13)\n"
"v_mov_b32 v30, v8\n"
"v_mov_b32 v31, v9\n"
"v_mov_b32 v32, v8\n"
"v_mov_b32 v33, v9\n"
"v_add_co_u32 v8, vcc, 1, v8\n"
"s_nop 1\n"
"v_addc_co_u32 v9, vcc, 0, v9, vcc\n"
"buffer_store_dword v122, v231, s[20:23], 0 offen sc1\n"
"v_cmp_gt_u32 vcc, 8, v198\n"
"s_nop 1\n"
"v_cndmask_b32 v254, v30, v8, vcc\n"
"v_cndmask_b32 v255, v31, v9, vcc\n"
"s_waitcnt vmcnt(12)\n"
"ds_read_b128 a[112:115], v238 offset:57344\n"
"ds_read_b128 a[120:123], v239 offset:57344\n"
"s_waitcnt vmcnt(10)\n"
"ds_read_b128 a[128:131], v240\n"
"ds_read_b128 a[136:139], v241\n"
"s_waitcnt vmcnt(8)\n"
"ds_read_b128 a[144:147], v240 offset:8192\n"
"ds_read_b128 a[152:155], v241 offset:8192\n"
"s_add_u32 m0, s59, 16128\n"
"s_nop 0\n"
"buffer_load_dwordx4 v220, s[24:27], 0 offen offset:256 lds\n"
"s_mov_b32 s42, 2211840\n"
"s_mov_b32 s43, 131072\n"
"s_mov_b32 s46, 2211840\n"
"s_mov_b32 s47, 131072\n"
"s_mov_b32 s38, 819200\n"
"s_mov_b32 s39, 131072\n"
"s_mov_b32 s50, 2211840\n"
"s_mov_b32 s51, 131072\n"
"v_lshlrev_b32 v78, 1, v199\n"
"v_xor_b32 v77, v78, v198\n"
"s_and_b32 s15, s57, 3\n"
"s_cmp_eq_u32 s15, 3\n"
"s_cselect_b32 s17, 12, 14\n"
"v_cmp_gt_u32 vcc, s17, v77\n"
"s_add_u32 m0, s59, 20224\n"
"s_nop 0\n"
"buffer_load_dwordx4 v221, s[24:27], 0 offen offset:256 lds\n"
"s_lshr_b32 s4, s57, 2\n"
"s_mul_i32 s5, s57, 7\n"
"s_mul_i32 s6, s56, 4320\n"
"s_add_u32 s10, s6, s5\n"
"s_sub_u32 s12, s10, s4\n"
"s_lshl_b32 s16, s12, 5\n"
"v_mul_u32_u24 v184, 3456, v54\n"
"v_lshl_add_u32 v184, v77, 4, v184\n"
"v_add_u32 v184, s16, v184\n"
"v_cndmask_b32 v184, v247, v184, vcc\n"
"v_cndmask_b32 v185, v247, v184, s[74:75]\n"
"v_cmp_gt_u32 vcc, 10, v77\n"
"s_lshr_b32 s11, s57, 1\n"
"s_mul_i32 s8, s56, 320\n"
"s_add_u32 m0, s59, 24192\n"
"s_nop 0\n"
"buffer_load_dwordx4 v220, s[24:27], 0 offen offset:384 lds\n"
"s_add_u32 s7, s8, s11\n"
"s_mul_i32 s14, s7, 160\n"
"v_mul_u32_u24 v186, 1280, v54\n"
"v_lshl_add_u32 v186, v77, 4, v186\n"
"v_add_u32 v186, s14, v186\n"
"v_cndmask_b32 v186, v247, v186, vcc\n"
"v_cndmask_b32 v187, v247, v186, s[74:75]\n"
"v_lshrrev_b32 v79, 3, v54\n"
"v_bfe_u32 v80, v0, 4, 3\n"
"s_lshr_b32 s9, s56, 2\n"
"s_lshl_b32 s13, s9, 7\n"
"s_and_b32 s15, s57, 1\n"
"s_mul_i32 s17, s56, 14\n"
"s_add_u32 s4, s17, s15\n"
"s_add_u32 m0, s59, 28288\n"
"s_nop 0\n"
"buffer_load_dwordx4 v221, s[24:27], 0 offen offset:384 lds\n"
"s_lshl_b32 s5, s4, 6\n"
"s_add_u32 s6, s5, s11\n"
"s_sub_u32 s10, s6, s13\n"
"s_mul_i32 s12, s10, 160\n"
"v_mul_u32_u24 v188, 20480, v79\n"
"v_mul_u32_u24 v81, 1280, v80\n"
"v_add_u32 v188, v188, v81\n"
"v_lshl_add_u32 v188, v77, 4, v188\n"
"v_add_u32 v188, s12, v188\n"
"v_cndmask_b32 v188, v247, v188, vcc\n"
"v_cndmask_b32 v189, v247, v188, s[74:75]\n"
"v_mov_b32 v106, 1065369472\n"
"v_mov_b32 v107, v106\n"
"v_mov_b32 v108, v106\n"
"s_add_u32 m0, s59, 32256\n"
"s_nop 0\n"
"buffer_load_dwordx4 v220, s[24:27], 0 offen offset:512 lds\n"
"v_mov_b32 v109, v106\n"
"v_cmp_eq_u32 vcc, 2, v64\n"
"s_nop 1\n"
"v_cndmask_b32 v94, 0, v72, vcc\n"
"v_mov_b32 v95, v94\n"
"v_mov_b32 v96, v94\n"
"v_mov_b32 v97, v94\n"
"v_cmp_eq_u32 vcc, 3, v64\n"
"s_nop 1\n"
"v_cndmask_b32 v98, 0, v72, vcc\n"
"v_mov_b32 v99, v98\n"
"v_mov_b32 v100, v98\n"
"v_mov_b32 v101, v98\n"
"s_mov_b32 s82, 12288\n"
"s_mul_i32 s16, s57, 286720\n"
"s_add_u32 s66, s16, 5541888\n"
"s_add_u32 m0, s59, 36352\n"
"s_nop 0\n"
"buffer_load_dwordx4 v221, s[24:27], 0 offen offset:512 lds\n"
"s_mul_i32 s7, s11, 102400\n"
"s_add_u32 s65, s7, 4722688\n"
"s_mul_i32 s9, s11, 278528\n"
"s_add_u32 s67, s9, 10129408\n"
"s_add_u32 s96, s20, 13631488\n"
"s_addc_u32 s97, s21, 0\n"
"s_mov_b32 s98, 32505856\n"
"s_mov_b32 s99, 131072\n"
"v_lshlrev_b32 v1, 4, v0\n"
"v_lshlrev_b32 v2, 4, v200\n"
"v_add_u32 v3, 65536, v2\n"
"v_add_u32 v4, 131072, v2\n"
"v_cmp_gt_u32 s[74:75], 8, v198\n"
"v_cmp_gt_u32 vcc, 2, v199\n"
"s_and_b32 s15, s57, 14\n"
"s_add_u32 m0, s59, 40320\n"
"s_nop 0\n"
"buffer_load_dwordx4 v220, s[24:27], 0 offen offset:640 lds\n"
"s_lshl2_add_u32 s17, s15, s58\n"
"s_lshl_b32 s4, s17, 7\n"
"v_lshl_add_u32 v13, v198, 13, s4\n"
"v_lshl_add_u32 v13, v199, 9, v13\n"
"v_cndmask_b32 v13, v247, v13, vcc\n"
"s_lshl_b32 s5, s58, 12\n"
"s_add_u32 s6, s5, 135168\n"
"v_mul_u32_u24 v15, 80, v198\n"
"v_lshl_add_u32 v15, v199, 4, v15\n"
"v_add_u32 v15, s6, v15\n"
"v_cmp_eq_u32 vcc, 2, v64\n"
"v_mov_b32 v83, 99\n"
"v_add_u32 v82, 16, v63\n"
"v_cndmask_b32 v82, v83, v82, vcc\n"
"s_add_u32 m0, s59, 44416\n"
"s_nop 0\n"
"buffer_load_dwordx4 v221, s[24:27], 0 offen offset:640 lds\n"
"v_mov_b32 v84, 20\n"
"v_cmp_eq_u32 vcc, 0, v198\n"
"s_nop 1\n"
"v_cndmask_b32 v248, v82, v84, vcc\n"
"v_cmp_gt_u32 vcc, 99, v248\n"
"s_add_u32 s10, s5, 138752\n"
"v_lshl_add_u32 v85, v198, 4, s10\n"
"v_mul_u32_u24 v16, 80, v248\n"
"v_lshl_add_u32 v16, v199, 4, v16\n"
"v_add_u32 v16, s6, v16\n"
"v_cndmask_b32 v16, v85, v16, vcc\n"
"v_cmp_eq_u32 vcc, 3, v64\n"
"v_add_u32 v55, 16, v63\n"
"s_nop 0\n"
"v_cndmask_b32 v55, v83, v55, vcc\n"
"v_cmp_eq_u32 vcc, 0, v198\n"
"s_add_u32 m0, s59, 48384\n"
"s_nop 0\n"
"buffer_load_dwordx4 v220, s[24:27], 0 offen offset:768 lds\n"
"v_cndmask_b32 v248, v55, v84, vcc\n"
"v_cmp_gt_u32 vcc, 99, v248\n"
"s_add_u32 s12, s5, 136848\n"
"v_mul_u32_u24 v17, 80, v248\n"
"v_lshl_add_u32 v17, v199, 4, v17\n"
"v_add_u32 v17, s12, v17\n"
"v_cndmask_b32 v17, v85, v17, vcc\n"
"v_cmp_eq_u32 vcc, 0, v199\n"
"v_add_u32 v18, 64, v15\n"
"s_nop 0\n"
"v_cndmask_b32 v18, v85, v18, vcc\n"
"v_cmp_eq_u32 vcc, 1, v199\n"
"v_add_u32 v19, 1728, v15\n"
"s_nop 0\n"
"v_cndmask_b32 v19, v85, v19, vcc\n"
"v_add_u32 v57, 2, v199\n"
"s_add_u32 m0, s59, 52480\n"
"s_nop 0\n"
"buffer_load_dwordx4 v221, s[24:27], 0 offen offset:768 lds\n"
"v_cmp_eq_u32 vcc, v57, v64\n"
"v_add_u32 v56, 16, v63\n"
"s_nop 0\n"
"v_cndmask_b32 v56, v83, v56, vcc\n"
"v_cmp_eq_u32 vcc, 0, v198\n"
"s_nop 1\n"
"v_cndmask_b32 v248, v56, v84, vcc\n"
"v_cmp_gt_u32 vcc, 2, v199\n"
"s_nop 1\n"
"v_cndmask_b32 v248, v83, v248, vcc\n"
"v_cmp_gt_u32 vcc, 99, v248\n"
"s_add_u32 s16, s5, 135232\n"
"v_mul_u32_u24 v20, 80, v248\n"
"v_mul_u32_u24 v58, 1680, v199\n"
"v_add_u32 v20, v20, v58\n"
"v_add_u32 v20, s16, v20\n"
"v_cndmask_b32 v20, v85, v20, vcc\n"
"s_add_u32 m0, s59, 81920\n"
"s_nop 0\n"
"buffer_load_dwordx4 v220, s[24:27], s84 offen lds\n"
"v_add_u32 v45, 135168, v1\n"
"v_cmp_gt_u32 vcc, 224, v0\n"
"s_lshl_b32 s8, s57, 16\n"
"s_add_u32 s7, s8, 2752512\n"
"v_lshl_add_u32 v21, v0, 3, s7\n"
"v_cndmask_b32 v21, v247, v21, vcc\n"
"s_lshl_b32 s14, s58, 8\n"
"s_add_u32 s11, s14, 157184\n"
"v_lshl_add_u32 v47, v199, 4, s11\n"
"v_lshl_add_u32 v48, v198, 2, s11\n"
"v_lshlrev_b32 v49, 2, v198\n"
"v_add_u32 v49, 155648, v49\n"
"v_lshlrev_b32 v50, 4, v199\n"
"v_add_u32 v50, 155648, v50\n"
"s_add_u32 m0, s59, 86016\n"
"s_nop 0\n"
"buffer_load_dwordx4 v221, s[24:27], s84 offen lds\n"
"s_lshr_b32 s9, s57, 1\n"
"s_and_b32 s13, s57, 1\n"
"s_lshl1_add_u32 s15, s56, s13\n"
"s_lshl2_add_u32 s17, s15, s58\n"
"s_lshl_b32 s4, s17, 7\n"
"s_add_u32 s10, s4, s9\n"
"s_mul_i32 s6, s10, 160\n"
"v_mul_u32_u24 v51, 1280, v198\n"
"v_lshl_add_u32 v51, v199, 4, v51\n"
"v_add_u32 v51, s6, v51\n"
"v_add_u32 v52, 128, v51\n"
"v_mad_i32_i24 v52, v199, -8, v52\n"
"s_lshl_b32 s12, s58, 7\n"
"s_add_u32 s5, s12, 155648\n"
"s_mov_b32 s3, 0\n"
"s_waitcnt vmcnt(12)\n"
"buffer_store_dwordx2 v[254:255], v14, s[20:23], s3 offen sc1\n"
"s_add_u32 m0, s59, 89984\n"
"s_nop 0\n"
"buffer_load_dwordx4 v220, s[24:27], s84 offen offset:128 lds\n"
"ds_read_b128 a[0:3], v238\n"
"ds_read_b128 a[8:11], v239\n"
"ds_read_b128 a[16:19], v238 offset:8192\n"
"ds_read_b128 a[24:27], v239 offset:8192\n"
"s_waitcnt vmcnt(12)\n"
"ds_read_b128 a[32:35], v238 offset:16384\n"
"ds_read_b128 a[40:43], v239 offset:16384\n"
"s_waitcnt vmcnt(10)\n"
"ds_read_b128 a[48:51], v238 offset:24576\n"
"ds_read_b128 a[56:59], v239 offset:24576\n"
"s_add_u32 m0, s59, 94080\n"
"s_nop 0\n"
"buffer_load_dwordx4 v221, s[24:27], s84 offen offset:128 lds\n"
"s_waitcnt vmcnt(9)\n"
"ds_read_b128 a[64:67], v238 offset:32768\n"
"s_waitcnt lgkmcnt(14)\n"
"ds_read_b128 a[72:75], v239 offset:32768\n"
"s_waitcnt vmcnt(7)\n"
"s_waitcnt lgkmcnt(14)\n"
"ds_read_b128 a[80:83], v238 offset:40960\n"
"s_waitcnt lgkmcnt(14)\n"
"ds_read_b128 a[88:91], v239 offset:40960\n"
"s_waitcnt vmcnt(5)\n"
"s_waitcnt lgkmcnt(14)\n"
"ds_read_b128 a[96:99], v238 offset:49152\n"
"s_waitcnt lgkmcnt(14)\n"
"ds_read_b128 a[104:107], v239 offset:49152\n"
"s_waitcnt lgkmcnt(0)\n"
"v_lshl_add_u32 v245, v60, 4, s5\n"
"s_add_u32 m0, s59, 98048\n"
"s_nop 0\n"
"buffer_load_dwordx4 v220, s[24:27], s84 offen offset:256 lds\n"
"v_lshrrev_b32 v59, 3, v200\n"
"s_mul_i32 s16, s57, 24576\n"
"s_add_u32 s8, s16, 12201984\n"
"v_mul_u32_u24 v244, 1536, v59\n"
"v_add3_u32 v244, v245, s8, v244\n"
"v_cmp_eq_u32 vcc, v199, v64\n"
"v_mov_b32 v61, 161216\n"
"s_nop 0\n"
"v_cndmask_b32 v246, v61, v48, vcc\n"
"s_add_u32 m0, s59, 102144\n"
"s_nop 0\n"
"buffer_load_dwordx4 v221, s[24:27], s84 offen offset:256 lds\n"
"v_add_u32 v249, 0, v63\n"
"s_lshl4_add_u32 s7, s56, s57\n"
"s_lshl2_add_u32 s14, s7, s58\n"
"s_lshl_b32 s11, s14, 7\n"
"s_add_u32 s9, s11, 131072\n"
"v_lshl_add_u32 v43, v59, 17, s9\n"
"v_lshl_add_u32 v43, v60, 4, v43\n"
"v_add_u32 v46, 917504, v43\n"
"s_add_u32 m0, s59, 106112\n"
"s_nop 0\n"
"buffer_load_dwordx4 v220, s[24:27], s84 offen offset:384 lds\n"
"v_cmp_gt_u32 vcc, 24, v200\n"
"v_mov_b32 v69, 25296896\n"
"s_nop 0\n"
"v_cndmask_b32 v67, v247, v69, vcc\n"
"v_mov_b32 v70, 18874368\n"
"v_cmp_gt_u32 vcc, 16, v200\n"
"s_nop 1\n"
"v_cndmask_b32 v65, v67, v70, vcc\n"
"v_mov_b32 v66, 9043968\n"
"v_cmp_gt_u32 vcc, 8, v200\n"
"s_add_u32 m0, s59, 110208\n"
"s_nop 0\n"
"buffer_load_dwordx4 v221, s[24:27], s84 offen offset:384 lds\n"
"v_cndmask_b32 v62, v65, v66, vcc\n"
"v_add_u32 v44, s11, v62\n"
"v_lshl_add_u32 v44, v60, 4, v44\n"
"s_and_b32 s13, s58, 1\n"
"s_lshr_b32 s15, s58, 1\n"
"s_mul_i32 s17, s15, 96\n"
"s_add_u32 s4, s17, s13\n"
"s_lshl_b32 s10, s4, 7\n"
"s_add_u32 m0, s59, 114176\n"
"s_nop 0\n"
"buffer_load_dwordx4 v220, s[24:27], s84 offen offset:512 lds\n"
"v_lshl_add_u32 v218, v199, 11, s10\n"
"v_lshl_add_u32 v218, v64, 8, v218\n"
"v_lshl_add_u32 v218, v198, 3, v218\n"
"v_xor_b32 v68, v64, v199\n"
"s_add_u32 s6, s10, 8192\n"
"v_lshl_add_u32 v219, v64, 8, s6\n"
"v_lshl_add_u32 v219, v68, 5, v219\n"
"v_lshl_add_u32 v219, v63, 3, v219\n"
"s_add_u32 m0, s59, 118272\n"
"s_nop 0\n"
"buffer_load_dwordx4 v221, s[24:27], s84 offen offset:512 lds\n"
"v_xor_b32 v190, 32, v218\n"
"v_xor_b32 v191, 64, v218\n"
"v_xor_b32 v192, 96, v218\n"
"s_lshl_b32 s12, s13, 2\n"
"s_cmp_lt_u32 s12, 2\n"
"s_cselect_b32 s5, 2, 1\n"
"s_mul_i32 s16, s56, 5\n"
"v_add_u32 v74, s16, v199\n"
"v_lshrrev_b32 v73, 2, v74\n"
"s_add_u32 m0, s59, 122240\n"
"s_nop 0\n"
"buffer_load_dwordx4 v220, s[24:27], s84 offen offset:640 lds\n"
"v_mul_u32_u24 v71, s5, v73\n"
"v_and_b32 v75, 3, v74\n"
"s_cmp_lt_u32 s12, 2\n"
"s_cselect_b32 s8, 1, 20\n"
"s_mul_i32 s7, s12, s8\n"
"s_lshl1_add_u32 s14, s7, s15\n"
"s_lshl_b32 s9, s14, 10\n"
"s_add_u32 s11, s9, s66\n"
"v_lshl_add_u32 v222, v71, 11, s11\n"
"s_add_u32 m0, s59, 126336\n"
"s_nop 0\n"
"buffer_load_dwordx4 v221, s[24:27], s84 offen offset:640 lds\n"
"v_lshl_add_u32 v222, v75, 8, v222\n"
"v_lshl_add_u32 v222, v198, 4, v222\n"
"s_add_u32 s17, s12, 1\n"
"s_cmp_lt_u32 s17, 2\n"
"s_cselect_b32 s4, 2, 1\n"
"v_mul_u32_u24 v76, s4, v73\n"
"s_cmp_lt_u32 s17, 2\n"
"s_cselect_b32 s10, 1, 20\n"
"s_mul_i32 s6, s17, s10\n"
"s_lshl1_add_u32 s13, s6, s15\n"
"s_add_u32 m0, s59, 130304\n"
"s_nop 0\n"
"buffer_load_dwordx4 v220, s[24:27], s84 offen offset:768 lds\n"
"s_lshl_b32 s5, s13, 10\n"
"s_add_u32 s16, s5, s66\n"
"v_lshl_add_u32 v223, v76, 11, s16\n"
"v_lshl_add_u32 v223, v75, 8, v223\n"
"v_lshl_add_u32 v223, v198, 4, v223\n"
"s_add_u32 s8, s12, 2\n"
"s_cmp_lt_u32 s8, 2\n"
"s_cselect_b32 s7, 2, 1\n"
"v_mul_u32_u24 v78, s7, v73\n"
"s_add_u32 m0, s59, 134400\n"
"s_nop 0\n"
"buffer_load_dwordx4 v221, s[24:27], s84 offen offset:768 lds\n"
"s_cmp_lt_u32 s8, 2\n"
"s_cselect_b32 s14, 1, 20\n"
"s_mul_i32 s9, s8, s14\n"
"s_lshl1_add_u32 s11, s9, s15\n"
"s_lshl_b32 s4, s11, 10\n"
"s_add_u32 s17, s4, s66\n"
"v_lshl_add_u32 v224, v78, 11, s17\n"
"v_lshl_add_u32 v224, v75, 8, v224\n"
"v_lshl_add_u32 v224, v198, 4, v224\n"
"s_add_u32 m0, s59, 56448\n"
"s_nop 0\n"
"buffer_load_dwordx4 v220, s[24:27], s84 offen offset:896 lds\n"
"s_add_u32 s10, s12, 3\n"
"s_cmp_lt_u32 s10, 2\n"
"s_cselect_b32 s6, 2, 1\n"
"v_mul_u32_u24 v79, s6, v73\n"
"s_cmp_lt_u32 s10, 2\n"
"s_cselect_b32 s13, 1, 20\n"
"s_mul_i32 s5, s10, s13\n"
"s_lshl1_add_u32 s16, s5, s15\n"
"s_lshl_b32 s7, s16, 10\n"
"s_add_u32 s8, s7, s66\n"
"s_add_u32 m0, s59, 60544\n"
"s_nop 0\n"
"buffer_load_dwordx4 v221, s[24:27], s84 offen offset:896 lds\n"
"v_lshl_add_u32 v225, v79, 11, s8\n"
"v_lshl_add_u32 v225, v75, 8, v225\n"
"v_lshl_add_u32 v225, v198, 4, v225\n"
"v_mov_b32 v54, s10\n"
"v_cmp_gt_u32 vcc, 7, v54\n"
"s_nop 1\n"
"v_cndmask_b32 v225, v247, v225, vcc\n"
"s_mul_i32 s14, s56, 5\n"
"s_add_u32 s9, s14, 4\n"
"s_add_u32 m0, s59, 64512\n"
"s_nop 0\n"
"buffer_load_dwordx4 v220, s[24:27], s84 offen offset:1024 lds\n"
"s_mov_b32 s63, 0\n"
"buffer_load_dwordx2 v[254:255], v10, s[20:23], s63 offen sc1\n"
"s_lshr_b32 s11, s9, 2\n"
"v_add_u32 v81, s12, v199\n"
"v_cmp_gt_u32 vcc, 2, v81\n"
"v_mov_b32 v72, 2\n"
"s_nop 0\n"
"v_cndmask_b32 v77, 1, v72, vcc\n"
"v_mul_u32_u24 v80, s11, v77\n"
"v_mov_b32 v56, 1\n"
"v_cndmask_b32 v55, 20, v56, vcc\n"
"s_add_u32 m0, s59, 68608\n"
"s_nop 0\n"
"buffer_load_dwordx4 v221, s[24:27], s84 offen offset:1024 lds\n"
"v_mul_u32_u24 v82, v55, v81\n"
"s_and_b32 s4, s9, 3\n"
"s_lshl2_add_u32 s17, s15, s4\n"
"s_lshl_b32 s6, s17, 8\n"
"s_add_u32 s13, s6, s66\n"
"v_lshl_add_u32 v226, v80, 11, s13\n"
"v_lshl_add_u32 v226, v82, 11, v226\n"
"v_lshl_add_u32 v226, v198, 4, v226\n"
"s_add_u32 m0, s59, 72576\n"
"s_nop 0\n"
"buffer_load_dwordx4 v220, s[24:27], s84 offen offset:1152 lds\n"
"v_cmp_gt_u32 vcc, 7, v81\n"
"s_nop 1\n"
"v_cndmask_b32 v226, v247, v226, vcc\n"
"s_lshl4_add_u32 s5, s57, s56\n"
"s_mul_i32 s16, s5, 1536\n"
"s_add_u32 s7, s16, 12358144\n"
"v_lshl_add_u32 v227, v0, 2, s7\n"
"s_and_b32 s8, s57, 1\n"
"s_lshl_b32 s10, s8, 6\n"
"s_add_u32 m0, s59, 76672\n"
"s_nop 0\n"
"buffer_load_dwordx4 v221, s[24:27], s84 offen offset:1152 lds\n"
"v_bfe_u32 v83, v0, 4, 3\n"
"v_lshlrev_b32 v85, 1, v64\n"
"v_xor_b32 v84, v85, v63\n"
"s_lshl_b32 s14, s15, 3\n"
"s_add_u32 s11, s14, 24576\n"
"v_lshl_add_u32 v57, v83, 10, s11\n"
"v_lshl_add_u32 v57, v64, 8, v57\n"
"v_lshl_add_u32 v57, v84, 4, v57\n"
"v_xor_b32 v228, s10, v57\n"
"v_mfma_f32_16x16x32_bf16 v[158:161], a[0:3], a[0:3], 0\n"
"s_add_u32 m0, s59, 0\n"
"s_mov_b32 s73, 0\n"
"buffer_load_dwordx4 v184, s[40:43], s73 offen lds\n"
"v_lshlrev_b32 v229, 10, v83\n"
"v_lshl_add_u32 v229, v64, 8, v229\n"
"v_mfma_f32_16x16x32_bf16 v[162:165], v[106:109], a[0:3], 0\n"
"v_lshl_add_u32 v229, v198, 3, v229\n"
"v_add_u32 v229, 24704, v229\n"
"v_mfma_f32_16x16x32_bf16 v[158:161], a[8:11], a[8:11], v[158:161]\n"
"s_mul_i32 s12, s56, 5\n"
"v_add_u32 v233, s12, v83\n"
"v_mfma_f32_16x16x32_bf16 v[162:165], v[106:109], a[8:11], v[162:165]\n"
"v_cmp_gt_u32 vcc, 5, v83\n"
"v_and_b32 v58, 7, v233\n"
"v_mfma_f32_16x16x32_bf16 v[158:161], a[16:19], a[16:19], v[158:161]\n"
"v_lshl_add_u32 v232, v58, 8, s65\n"
"v_lshl_add_u32 v232, v198, 4, v232\n"
"v_mfma_f32_16x16x32_bf16 v[162:165], v[106:109], a[16:19], v[162:165]\n"
"v_cndmask_b32 v232, v247, v232, vcc\n"
"s_sub_u32 s9, 3, s8\n"
"v_mfma_f32_16x16x32_bf16 v[158:161], a[24:27], a[24:27], v[158:161]\n"
"s_add_u32 m0, s59, 4096\n"
"s_mov_b32 s73, 55296\n"
"buffer_load_dwordx4 v184, s[40:43], s73 offen lds\n"
"v_lshrrev_b32 v59, 3, v233\n"
"v_mul_u32_u24 v61, s9, v59\n"
"v_mfma_f32_16x16x32_bf16 v[162:165], v[106:109], a[24:27], v[162:165]\n"
"s_mul_i32 s4, s8, 30\n"
"v_mfma_f32_16x16x32_bf16 v[158:161], a[32:35], a[32:35], v[158:161]\n"
"s_add_u32 s17, s4, s15\n"
"s_lshl_b32 s6, s17, 11\n"
"v_mfma_f32_16x16x32_bf16 v[162:165], v[106:109], a[32:35], v[162:165]\n"
"v_add_u32 v230, s6, v232\n"
"v_lshl_add_u32 v230, v61, 11, v230\n"
"v_mfma_f32_16x16x32_bf16 v[158:161], a[40:43], a[40:43], v[158:161]\n"
"s_add_u32 s13, s8, s15\n"
"s_lshl_b32 s5, s13, 30\n"
"v_mfma_f32_16x16x32_bf16 v[162:165], v[106:109], a[40:43], v[162:165]\n"
"s_add_u32 s16, s5, 4096\n"
"v_mul_u32_u24 v231, 6144, v59\n"
"v_mfma_f32_16x16x32_bf16 v[158:161], a[48:51], a[48:51], v[158:161]\n"
"v_add3_u32 v231, v232, s16, v231\n"
"s_add_u32 s7, s14, 36864\n"
"v_mfma_f32_16x16x32_bf16 v[162:165], v[106:109], a[48:51], v[162:165]\n"
"s_add_u32 m0, s59, 8192\n"
"s_mov_b32 s73, 110592\n"
"buffer_load_dwordx4 v185, s[40:43], s73 offen lds\n"
"v_lshl_add_u32 v234, v83, 11, s7\n"
"v_lshl_add_u32 v234, v64, 8, v234\n"
"v_mfma_f32_16x16x32_bf16 v[158:161], a[56:59], a[56:59], v[158:161]\n"
"v_lshl_add_u32 v234, v84, 4, v234\n"
"v_xor_b32 v193, 64, v234\n"
"v_mfma_f32_16x16x32_bf16 v[162:165], v[106:109], a[56:59], v[162:165]\n"
"v_lshlrev_b32 v235, 11, v83\n"
"v_mfma_f32_16x16x32_bf16 v[158:161], a[64:67], a[64:67], v[158:161]\n"
"v_lshl_add_u32 v235, v64, 8, v235\n"
"v_lshl_add_u32 v235, v198, 3, v235\n"
"v_mfma_f32_16x16x32_bf16 v[162:165], v[106:109], a[64:67], v[162:165]\n"
"v_add_u32 v235, 36992, v235\n"
"v_cmp_gt_u32 vcc, 6, v83\n"
"v_mfma_f32_16x16x32_bf16 v[158:161], a[72:75], a[72:75], v[158:161]\n"
"s_lshr_b32 s10, s56, 2\n"
"s_sub_u32 s11, s56, s10\n"
"v_mfma_f32_16x16x32_bf16 v[162:165], v[106:109], a[72:75], v[162:165]\n"
"s_lshl_b32 s12, s11, 1\n"
"v_mov_b32 v65, s12\n"
"v_mfma_f32_16x16x32_bf16 v[158:161], a[80:83], a[80:83], v[158:161]\n"
"s_add_u32 m0, s59, 12288\n"
"s_mov_b32 s73, 0\n"
"buffer_load_dwordx4 v184, s[44:47], s73 offen lds\n"
"s_lshl_b32 s9, s56, 2\n"
"v_mov_b32 v67, s9\n"
"v_mfma_f32_16x16x32_bf16 v[162:165], v[106:109], a[80:83], v[162:165]\n"
"v_cndmask_b32 v62, v65, v67, vcc\n"
"v_lshrrev_b32 v69, 1, v83\n"
"v_mfma_f32_16x16x32_bf16 v[158:161], a[88:91], a[88:91], v[158:161]\n"
"v_add_u32 v233, s8, v62\n"
"v_mad_i32_i24 v233, v69, 60, v233\n"
"v_mfma_f32_16x16x32_bf16 v[162:165], v[106:109], a[88:91], v[162:165]\n"
"v_lshl_add_u32 v233, v83, 1, v233\n"
"v_mfma_f32_16x16x32_bf16 v[158:161], a[96:99], a[96:99], v[158:161]\n"
"s_and_b32 s4, s56, 3\n"
"s_add_u32 s17, s4, 1\n"
"v_mfma_f32_16x16x32_bf16 v[162:165], v[106:109], a[96:99], v[162:165]\n"
"s_lshr_b32 s6, s17, 2\n"
"s_sub_u32 s15, 7, s6\n"
"v_mfma_f32_16x16x32_bf16 v[158:161], a[104:107], a[104:107], v[158:161]\n"
"s_add_u32 m0, s59, 16384\n"
"s_mov_b32 s73, 55296\n"
"buffer_load_dwordx4 v184, s[44:47], s73 offen lds\n"
"v_cmp_gt_u32 vcc, s15, v83\n"
"v_and_b32 v70, 7, v233\n"
"v_mfma_f32_16x16x32_bf16 v[162:165], v[106:109], a[104:107], v[162:165]\n"
"v_lshl_add_u32 v232, v70, 8, s67\n"
"v_lshl_add_u32 v232, v198, 4, v232\n"
"v_mfma_f32_16x16x32_bf16 v[158:161], a[112:115], a[112:115], v[158:161]\n"
"v_cndmask_b32 v232, v247, v232, vcc\n"
"v_lshrrev_b32 v60, 3, v233\n"
"v_mfma_f32_16x16x32_bf16 v[162:165], v[106:109], a[112:115], v[162:165]\n"
"v_cmp_gt_u32 vcc, 18, v60\n"
"v_mad_i32_i24 v68, v60, 3, 36\n"
"v_mfma_f32_16x16x32_bf16 v[158:161], a[120:123], a[120:123], v[158:161]\n"
"v_mad_i32_i24 v66, v60, 5, 0\n"
"v_cndmask_b32 v66, v68, v66, vcc\n"
"v_mfma_f32_16x16x32_bf16 v[162:165], v[106:109], a[120:123], v[162:165]\n"
"s_lshr_b32 s13, s58, 1\n"
"v_mfma_f32_16x16x32_bf16 v[158:161], a[128:131], a[128:131], v[158:161]\n"
"s_lshl_b32 s5, s13, 11\n"
"v_add_u32 v236, s5, v232\n"
"v_mfma_f32_16x16x32_bf16 v[162:165], v[106:109], a[128:131], v[162:165]\n"
"s_add_u32 m0, s59, 20480\n"
"s_mov_b32 s73, 110592\n"
"buffer_load_dwordx4 v185, s[44:47], s73 offen lds\n"
"v_lshl_add_u32 v236, v66, 11, v236\n"
"v_lshlrev_b32 v74, 1, v60\n"
"v_mfma_f32_16x16x32_bf16 v[158:161], a[136:139], a[136:139], v[158:161]\n"
"v_add_u32 v74, 81, v74\n"
"v_mad_i32_i24 v71, v60, 5, 2\n"
"v_mfma_f32_16x16x32_bf16 v[162:165], v[106:109], a[136:139], v[162:165]\n"
"v_cndmask_b32 v71, v74, v71, vcc\n"
"v_add_u32 v250, s5, v232\n"
"v_mfma_f32_16x16x32_bf16 v[158:161], a[144:147], a[144:147], v[158:161]\n"
"v_lshl_add_u32 v250, v71, 11, v250\n"
"v_mad_i32_i24 v78, v60, 3, 38\n"
"v_mfma_f32_16x16x32_bf16 v[162:165], v[106:109], a[144:147], v[162:165]\n"
"v_mad_i32_i24 v76, v60, 5, 4\n"
"v_cndmask_b32 v76, v78, v76, vcc\n"
"v_mfma_f32_16x16x32_bf16 v[158:161], a[152:155], a[152:155], v[158:161]\n"
"s_lshl_b32 s16, s13, 30\n"
"v_add_u32 v237, s16, v232\n"
"v_mfma_f32_16x16x32_bf16 v[162:165], v[106:109], a[152:155], v[162:165]\n"
"v_lshl_add_u32 v237, v76, 11, v237\n"
"s_waitcnt vmcnt(9)\n"
"v_cmp_ne_u64 s[18:19], v[254:255], v[8:9]\n"
"s_nop 0\n"
"s_and_b32 s18, s18, 65535\n"
"s_cbranch_scc0 L1_%=\n"
"s_mov_b32 s72, 0\n"
"L2_%=:\n"
"buffer_load_dwordx2 v[254:255], v10, s[20:23], s63 offen sc1\n"
"s_add_u32 s72, s72, 1\n"
"s_waitcnt vmcnt(0)\n"
"v_cmp_ne_u64 s[18:19], v[254:255], v[8:9]\n"
"s_nop 0\n"
"s_and_b32 s18, s18, 65535\n"
"s_cbranch_scc0 L1_%=\n"
"s_cmp_lt_u32 s72, 4194304\n"
"s_cbranch_scc1 L2_%=\n"
"L1_%=:\n"
"s_barrier\n"
"; P1 layer 0\n"
"s_add_u32 m0, s59, 139264\n"
"s_add_u32 s60, s64, 4294828032\n"
"v_add_u32 v1, s60, v2\n"
"buffer_load_dwordx4 v1, s[20:23], m0 offen sc1 lds\n"
"ds_read_b128 a[4:7], v240 offset:16384\n"
"ds_read_b128 a[12:15], v241 offset:16384\n"
"ds_read_b128 a[20:23], v240 offset:24576\n"
"ds_read_b128 a[28:31], v241 offset:24576\n"
"s_add_u32 m0, s59, 143360\n"
"s_nop 0\n"
"buffer_load_dwordx4 v1, s[20:23], m0 offen sc1 lds\n"
"ds_read_b128 a[36:39], v240 offset:32768\n"
"ds_read_b128 a[44:47], v241 offset:32768\n"
"ds_read_b128 a[52:55], v240 offset:40960\n"
"ds_read_b128 a[60:63], v241 offset:40960\n"
"s_add_u32 m0, s59, 147456\n"
"s_nop 0\n"
"buffer_load_dwordx4 v1, s[20:23], m0 offen sc1 lds\n"
"ds_read_b128 a[68:71], v240 offset:49152\n"
"ds_read_b128 a[76:79], v241 offset:49152\n"
"ds_read_b128 a[84:87], v240 offset:57344\n"
"ds_read_b128 a[92:95], v241 offset:57344\n"
"s_add_u32 m0, s59, 151552\n"
"s_nop 0\n"
"buffer_load_dwordx4 v1, s[20:23], m0 offen sc1 lds\n"
"ds_read_b128 a[100:103], v242\n"
"ds_read_b128 a[108:111], v243\n"
"ds_read_b128 a[116:119], v238 offset:57344\n"
"s_waitcnt lgkmcnt(14)\n"
"ds_read_b128 a[124:127], v239 offset:57344\n"
"s_add_u32 m0, s59, 53248\n"
"v_add_u32 v1, 102400, v1\n"
"buffer_load_dwordx4 v1, s[20:23], m0 offen sc1 lds\n"
"s_waitcnt vmcnt(13)\n"
"s_waitcnt lgkmcnt(14)\n"
"ds_read_b128 a[132:135], v240\n"
"s_waitcnt lgkmcnt(14)\n"
"ds_read_b128 a[140:143], v241\n"
"s_waitcnt vmcnt(11)\n"
"s_waitcnt lgkmcnt(14)\n"
"ds_read_b128 a[148:151], v240 offset:8192\n"
"s_waitcnt lgkmcnt(14)\n"
"ds_read_b128 a[156:159], v241 offset:8192\n"
"s_waitcnt lgkmcnt(0)\n"
"v_mfma_f32_16x16x32_bf16 v[166:169], a[4:7], a[4:7], 0\n"
"s_add_u32 m0, s59, 135168\n"
"v_add_u32 v1, 4294889472, v1\n"
"buffer_load_dwordx4 v1, s[20:23], m0 offen sc1 lds\n"
"v_mfma_f32_16x16x32_bf16 v[170:173], v[106:109], a[4:7], 0\n"
"v_mfma_f32_16x16x32_bf16 v[166:169], a[12:15], a[12:15], v[166:169]\n"
"v_mfma_f32_16x16x32_bf16 v[170:173], v[106:109], a[12:15], v[170:173]\n"
"s_add_u32 m0, s59, 57344\n"
"v_add_u32 v1, 81920, v1\n"
"buffer_load_dwordx4 v1, s[20:23], m0 offen sc1 lds\n"
"v_mfma_f32_16x16x32_bf16 v[166:169], a[20:23], a[20:23], v[166:169]\n"
"v_mfma_f32_16x16x32_bf16 v[170:173], v[106:109], a[20:23], v[170:173]\n"
"v_mfma_f32_16x16x32_bf16 v[166:169], a[28:31], a[28:31], v[166:169]\n"
"v_mfma_f32_16x16x32_bf16 v[170:173], v[106:109], a[28:31], v[170:173]\n"
"s_add_u32 m0, s59, 61440\n"
"s_nop 0\n"
"buffer_load_dwordx4 v1, s[20:23], m0 offen sc1 lds\n"
"v_mfma_f32_16x16x32_bf16 v[166:169], a[36:39], a[36:39], v[166:169]\n"
"v_mfma_f32_16x16x32_bf16 v[170:173], v[106:109], a[36:39], v[170:173]\n"
"v_mfma_f32_16x16x32_bf16 v[166:169], a[44:47], a[44:47], v[166:169]\n"
"s_add_u32 m0, s59, 65536\n"
"s_nop 0\n"
"buffer_load_dwordx4 v1, s[20:23], m0 offen sc1 lds\n"
"v_mfma_f32_16x16x32_bf16 v[170:173], v[106:109], a[44:47], v[170:173]\n"
"v_mfma_f32_16x16x32_bf16 v[166:169], a[52:55], a[52:55], v[166:169]\n"
"v_mfma_f32_16x16x32_bf16 v[170:173], v[106:109], a[52:55], v[170:173]\n"
"v_mfma_f32_16x16x32_bf16 v[166:169], a[60:63], a[60:63], v[166:169]\n"
"s_add_u32 m0, s59, 69632\n"
"s_nop 0\n"
"buffer_load_dwordx4 v1, s[20:23], m0 offen sc1 lds\n"
"v_mfma_f32_16x16x32_bf16 v[170:173], v[106:109], a[60:63], v[170:173]\n"
"v_mfma_f32_16x16x32_bf16 v[166:169], a[68:71], a[68:71], v[166:169]\n"
"v_mfma_f32_16x16x32_bf16 v[170:173], v[106:109], a[68:71], v[170:173]\n"
"v_mfma_f32_16x16x32_bf16 v[166:169], a[76:79], a[76:79], v[166:169]\n"
"s_add_u32 m0, s59, 73728\n"
"s_nop 0\n"
"buffer_load_dwordx4 v1, s[20:23], m0 offen sc1 lds\n"
"v_mfma_f32_16x16x32_bf16 v[170:173], v[106:109], a[76:79], v[170:173]\n"
"v_mfma_f32_16x16x32_bf16 v[166:169], a[84:87], a[84:87], v[166:169]\n"
"v_mfma_f32_16x16x32_bf16 v[170:173], v[106:109], a[84:87], v[170:173]\n"
"s_add_u32 m0, s59, 77824\n"
"s_nop 0\n"
"buffer_load_dwordx4 v1, s[20:23], m0 offen sc1 lds\n"
"v_mfma_f32_16x16x32_bf16 v[166:169], a[92:95], a[92:95], v[166:169]\n"
"v_mfma_f32_16x16x32_bf16 v[170:173], v[106:109], a[92:95], v[170:173]\n"
"v_mfma_f32_16x16x32_bf16 v[166:169], a[100:103], a[100:103], v[166:169]\n"
"v_mfma_f32_16x16x32_bf16 v[170:173], v[106:109], a[100:103], v[170:173]\n"
"s_add_u32 m0, s59, 24576\n"
"s_mov_b32 s73, 0\n"
"buffer_load_dwordx4 v186, s[36:39], s73 offen lds\n"
"v_mfma_f32_16x16x32_bf16 v[166:169], a[108:111], a[108:111], v[166:169]\n"
"v_mfma_f32_16x16x32_bf16 v[170:173], v[106:109], a[108:111], v[170:173]\n"
"v_mfma_f32_16x16x32_bf16 v[166:169], a[116:119], a[116:119], v[166:169]\n"
"v_mfma_f32_16x16x32_bf16 v[170:173], v[106:109], a[116:119], v[170:173]\n"
"s_add_u32 m0, s59, 28672\n"
"s_mov_b32 s73, 20480\n"
"buffer_load_dwordx4 v186, s[36:39], s73 offen lds\n"
"v_mfma_f32_16x16x32_bf16 v[166:169], a[124:127], a[124:127], v[166:169]\n"
"v_mfma_f32_16x16x32_bf16 v[170:173], v[106:109], a[124:127], v[170:173]\n"
"v_mfma_f32_16x16x32_bf16 v[166:169], a[132:135], a[132:135], v[166:169]\n"
"s_add_u32 m0, s59, 32768\n"
"s_mov_b32 s73, 40960\n"
"buffer_load_dwordx4 v187, s[36:39], s73 offen lds\n"
"v_mfma_f32_16x16x32_bf16 v[170:173], v[106:109], a[132:135], v[170:173]\n"
"v_mfma_f32_16x16x32_bf16 v[166:169], a[140:143], a[140:143], v[166:169]\n"
"v_mfma_f32_16x16x32_bf16 v[170:173], v[106:109], a[140:143], v[170:173]\n"
"v_mfma_f32_16x16x32_bf16 v[166:169], a[148:151], a[148:151], v[166:169]\n"
"s_add_u32 m0, s59, 36864\n"
"s_mov_b32 s73, 0\n"
"buffer_load_dwordx4 v188, s[48:51], s73 offen lds\n"
"v_mfma_f32_16x16x32_bf16 v[170:173], v[106:109], a[148:151], v[170:173]\n"
"v_mfma_f32_16x16x32_bf16 v[166:169], a[156:159], a[156:159], v[166:169]\n"
"v_mfma_f32_16x16x32_bf16 v[170:173], v[106:109], a[156:159], v[170:173]\n"
"v_cmp_eq_u32 vcc, 1, v249\n"
"s_nop 1\n"
"v_cndmask_b32 v158, v158, v159, vcc\n"
"s_nop 2\n"
"v_cndmask_b32 v166, v166, v167, vcc\n"
"v_cmp_eq_u32 vcc, 2, v249\n"
"s_nop 1\n"
"v_cndmask_b32 v158, v158, v160, vcc\n"
"v_cndmask_b32 v166, v166, v168, vcc\n"
"v_cmp_eq_u32 vcc, 3, v249\n"
"s_nop 1\n"
"v_cndmask_b32 v158, v158, v161, vcc\n"
"v_cndmask_b32 v166, v166, v169, vcc\n"
"v_mul_f32 v162, 986500301, v162\n"
"v_mul_f32 v158, 986500301, v158\n"
"v_fma_f32 v158, -v162, v162, v158\n"
"v_max_f32 v158, 0, v158\n"
"v_add_f32 v158, 925353388, v158\n"
"v_rsq_f32 v158, v158\n"
"ds_write_b32 v246, v162\n"
"ds_write_b32 v246, v158 offset:128\n"
"v_mul_f32 v170, 986500301, v170\n"
"v_mul_f32 v166, 986500301, v166\n"
"v_fma_f32 v166, -v170, v170, v166\n"
"v_max_f32 v166, 0, v166\n"
"v_add_f32 v166, 925353388, v166\n"
"v_rsq_f32 v166, v166\n"
"ds_write_b32 v246, v170 offset:64\n"
"ds_write_b32 v246, v166 offset:192\n"
"s_waitcnt vmcnt(13) lgkmcnt(0)\n"
"s_barrier\n"
"ds_read_b128 v[54:57], v4 offset:8192\n"
"ds_read_b128 v[58:61], v4 offset:9216\n"
"ds_read_b128 v[62:65], v4 offset:10240\n"
"ds_read_b128 v[66:69], v4 offset:11264\n"
"ds_read_b128 v[70:73], v4 offset:12288\n"
"ds_read_b128 v[74:77], v4 offset:13312\n"
"s_waitcnt lgkmcnt(5)\n"
"v_mfma_f32_16x16x32_bf16 v[110:113], a[0:3], v[54:57], 0\n"
"s_waitcnt lgkmcnt(4)\n"
"ds_read_b128 v[78:81], v4 offset:14336\n"
"s_mov_b32 s80, 4096\n"
"v_mfma_f32_16x16x32_bf16 v[114:117], a[4:7], v[54:57], 0\n"
"s_add_u32 m0, s59, 40960\n"
"s_mov_b32 s81, 8192\n"
"s_mov_b32 s83, 16384\n"
"s_mov_b32 s85, 24576\n"
"v_mfma_f32_16x16x32_bf16 v[118:121], a[0:3], v[58:61], 0\n"
"s_waitcnt lgkmcnt(4)\n"
"ds_read_b128 v[82:85], v4 offset:15360\n"
"s_mov_b32 s73, 40960\n"
"v_mfma_f32_16x16x32_bf16 v[122:125], a[4:7], v[58:61], 0\n"
"buffer_load_dwordx4 v188, s[48:51], s73 offen lds\n"
"v_mfma_f32_16x16x32_bf16 v[126:129], a[0:3], v[62:65], 0\n"
"s_waitcnt lgkmcnt(4)\n"
"ds_read_b128 v[54:57], v4 offset:16384\n"
"s_mov_b32 s86, 28672\n"
"v_mfma_f32_16x16x32_bf16 v[130:133], a[4:7], v[62:65], 0\n"
"s_add_u32 m0, s59, 45056\n"
"s_mov_b32 s87, 32768\n"
"s_mov_b32 s88, 36864\n"
"s_mov_b32 s89, 40960\n"
"v_mfma_f32_16x16x32_bf16 v[134:137], a[0:3], v[66:69], 0\n"
"s_waitcnt lgkmcnt(4)\n"
"ds_read_b128 v[58:61], v4 offset:17408\n"
"s_mov_b32 s73, 81920\n"
"v_mfma_f32_16x16x32_bf16 v[138:141], a[4:7], v[66:69], 0\n"
"buffer_load_dwordx4 v188, s[48:51], s73 offen lds\n"
"v_mfma_f32_16x16x32_bf16 v[142:145], a[0:3], v[70:73], 0\n"
"s_waitcnt lgkmcnt(4)\n"
"ds_read_b128 v[62:65], v4 offset:18432\n"
"s_mov_b32 s90, 45056\n"
"v_mfma_f32_16x16x32_bf16 v[146:149], a[4:7], v[70:73], 0\n"
"s_add_u32 m0, s59, 49152\n"
"s_mov_b32 s91, 49152\n"
"s_mov_b32 s92, 53248\n"
"s_mov_b32 s93, 57344\n"
"v_mfma_f32_16x16x32_bf16 v[110:113], a[8:11], v[74:77], v[110:113]\n"
"ds_read_b128 v[66:69], v4 offset:19456\n"
"s_mov_b32 s73, 122880\n"
"v_mfma_f32_16x16x32_bf16 v[114:117], a[12:15], v[74:77], v[114:117]\n"
"s_waitcnt vmcnt(11)\n"
"s_barrier\n"
"s_waitcnt lgkmcnt(5)\n"
"v_mfma_f32_16x16x32_bf16 v[118:121], a[8:11], v[78:81], v[118:121]\n"
"s_waitcnt lgkmcnt(4)\n"
"ds_read_b128 v[70:73], v4 offset:20480\n"
"s_mov_b32 s94, 61440\n"
"v_mfma_f32_16x16x32_bf16 v[122:125], a[12:15], v[78:81], v[122:125]\n"
"buffer_load_dwordx4 v189, s[48:51], s73 offen lds\n"
"v_mfma_f32_16x16x32_bf16 v[126:129], a[8:11], v[82:85], v[126:129]\n"
"s_waitcnt lgkmcnt(4)\n"
"ds_read_b128 v[74:77], v4 offset:21504\n"
"s_add_u32 m0, s59, 139264\n"
"v_mfma_f32_16x16x32_bf16 v[130:133], a[12:15], v[82:85], v[130:133]\n"
"v_add_u32 v1, 4294909952, v1\n"
"s_lshl_b32 s78, s56, 12\n"
"v_mfma_f32_16x16x32_bf16 v[134:137], a[8:11], v[54:57], v[134:137]\n"
"s_waitcnt lgkmcnt(4)\n"
"ds_read_b128 v[78:81], v4 offset:22528\n"
"s_mov_b32 s68, 897988541\n"
"v_mfma_f32_16x16x32_bf16 v[138:141], a[12:15], v[54:57], v[138:141]\n"
"buffer_load_dwordx4 v1, s[20:23], m0 offen sc1 lds\n"
"v_mfma_f32_16x16x32_bf16 v[142:145], a[8:11], v[58:61], v[142:145]\n"
"s_waitcnt lgkmcnt(4)\n"
"ds_read_b128 v[82:85], v4 offset:23552\n"
"s_add_u32 m0, s59, 81920\n"
"v_mfma_f32_16x16x32_bf16 v[146:149], a[12:15], v[58:61], v[146:149]\n"
"v_add_u32 v1, 61440, v1\n"
"s_mov_b32 s69, 897988541\n"
"v_mfma_f32_16x16x32_bf16 v[110:113], a[16:19], v[62:65], v[110:113]\n"
"s_waitcnt lgkmcnt(4)\n"
"ds_read_b128 v[54:57], v2 offset:53248\n"
"s_mov_b32 s70, 1069066811\n"
"v_mfma_f32_16x16x32_bf16 v[114:117], a[20:23], v[62:65], v[114:117]\n"
"buffer_load_dwordx4 v1, s[20:23], m0 offen sc1 lds\n"
"v_mfma_f32_16x16x32_bf16 v[118:121], a[16:19], v[66:69], v[118:121]\n"
"s_waitcnt lgkmcnt(4)\n"
"ds_read_b128 v[58:61], v2 offset:54272\n"
"s_add_u32 m0, s59, 86016\n"
"v_mfma_f32_16x16x32_bf16 v[122:125], a[20:23], v[66:69], v[122:125]\n"
"buffer_load_dwordx4 v1, s[20:23], m0 offen sc1 lds\n"
"v_mfma_f32_16x16x32_bf16 v[126:129], a[16:19], v[70:73], v[126:129]\n"
"s_waitcnt lgkmcnt(4)\n"
"ds_read_b128 v[62:65], v2 offset:55296\n"
"s_mov_b32 s71, 1069066811\n"
"v_mfma_f32_16x16x32_bf16 v[130:133], a[20:23], v[70:73], v[130:133]\n"
"s_add_u32 m0, s59, 90112\n"
"v_mfma_f32_16x16x32_bf16 v[134:137], a[16:19], v[74:77], v[134:137]\n"
"s_waitcnt lgkmcnt(4)\n"
"ds_read_b128 v[66:69], v2 offset:56320\n"
"v_mfma_f32_16x16x32_bf16 v[138:141], a[20:23], v[74:77], v[138:141]\n"
"buffer_load_dwordx4 v1, s[20:23], m0 offen sc1 lds\n"
"v_mfma_f32_16x16x32_bf16 v[142:145], a[16:19], v[78:81], v[142:145]\n"
"s_waitcnt lgkmcnt(4)\n"
"ds_read_b128 v[70:73], v4 offset:4096\n"
"v_mfma_f32_16x16x32_bf16 v[146:149], a[20:23], v[78:81], v[146:149]\n"
"s_add_u32 m0, s59, 94208\n"
"v_mfma_f32_16x16x32_bf16 v[110:113], a[24:27], v[82:85], v[110:113]\n"
"s_waitcnt lgkmcnt(4)\n"
"ds_read_b128 v[74:77], v4 offset:5120\n"
"v_mfma_f32_16x16x32_bf16 v[114:117], a[28:31], v[82:85], v[114:117]\n"
"buffer_load_dwordx4 v1, s[20:23], m0 offen sc1 lds\n"
"v_mfma_f32_16x16x32_bf16 v[118:121], a[24:27], v[54:57], v[118:121]\n"
"s_waitcnt lgkmcnt(4)\n"
"ds_read_b128 v[78:81], v4 offset:6144\n"
"v_mfma_f32_16x16x32_bf16 v[122:125], a[28:31], v[54:57], v[122:125]\n"
"s_add_u32 m0, s59, 98304\n"
"v_mfma_f32_16x16x32_bf16 v[126:129], a[24:27], v[58:61], v[126:129]\n"
"s_waitcnt lgkmcnt(4)\n"
"ds_read_b128 v[82:85], v4 offset:7168\n"
"v_mfma_f32_16x16x32_bf16 v[130:133], a[28:31], v[58:61], v[130:133]\n"
"buffer_load_dwordx4 v1, s[20:23], m0 offen sc1 lds\n"
"v_mfma_f32_16x16x32_bf16 v[134:137], a[24:27], v[62:65], v[134:137]\n"
"s_waitcnt lgkmcnt(4)\n"
"ds_read_b128 v[54:57], v2 offset:57344\n"
"v_mfma_f32_16x16x32_bf16 v[138:141], a[28:31], v[62:65], v[138:141]\n"
"s_add_u32 m0, s59, 102400\n"
"v_mfma_f32_16x16x32_bf16 v[142:145], a[24:27], v[66:69], v[142:145]\n"
"s_waitcnt lgkmcnt(4)\n"
"ds_read_b128 v[58:61], v2 offset:58368\n"
"v_mfma_f32_16x16x32_bf16 v[146:149], a[28:31], v[66:69], v[146:149]\n"
"buffer_load_dwordx4 v1, s[20:23], m0 offen sc1 lds\n"
"v_mfma_f32_16x16x32_bf16 v[110:113], a[32:35], v[70:73], v[110:113]\n"
"s_waitcnt lgkmcnt(4)\n"
"ds_read_b128 v[62:65], v2 offset:59392\n"
"v_mfma_f32_16x16x32_bf16 v[114:117], a[36:39], v[70:73], v[114:117]\n"
"s_add_u32 m0, s59, 106496\n"
"v_mfma_f32_16x16x32_bf16 v[118:121], a[32:35], v[74:77], v[118:121]\n"
"ds_read_b128 v[66:69], v2 offset:60416\n"
"v_mfma_f32_16x16x32_bf16 v[122:125], a[36:39], v[74:77], v[122:125]\n"
"s_waitcnt vmcnt(16)\n"
"s_barrier\n"
"s_waitcnt lgkmcnt(5)\n"
"v_mfma_f32_16x16x32_bf16 v[126:129], a[32:35], v[78:81], v[126:129]\n"
"s_waitcnt lgkmcnt(4)\n"
"ds_read_b128 v[70:73], v2 offset:61440\n"
"v_mfma_f32_16x16x32_bf16 v[130:133], a[36:39], v[78:81], v[130:133]\n"
"buffer_load_dwordx4 v1, s[20:23], m0 offen sc1 lds\n"
"v_mfma_f32_16x16x32_bf16 v[134:137], a[32:35], v[82:85], v[134:137]\n"
"s_waitcnt lgkmcnt(4)\n"
"ds_read_b128 v[74:77], v2 offset:62464\n"
"s_add_u32 m0, s59, 110592\n"
"v_mfma_f32_16x16x32_bf16 v[138:141], a[36:39], v[82:85], v[138:141]\n"
"buffer_load_dwordx4 v1, s[20:23], m0 offen sc1 lds\n"
"v_mfma_f32_16x16x32_bf16 v[142:145], a[32:35], v[54:57], v[142:145]\n"
"s_waitcnt lgkmcnt(4)\n"
"ds_read_b128 v[78:81], v2 offset:63488\n"
"v_mfma_f32_16x16x32_bf16 v[146:149], a[36:39], v[54:57], v[146:149]\n"
"s_add_u32 m0, s59, 114688\n"
"v_mfma_f32_16x16x32_bf16 v[110:113], a[40:43], v[58:61], v[110:113]\n"
"s_waitcnt lgkmcnt(4)\n"
"ds_read_b128 v[82:85], v2 offset:64512\n"
"v_mfma_f32_16x16x32_bf16 v[114:117], a[44:47], v[58:61], v[114:117]\n"
"buffer_load_dwordx4 v1, s[20:23], m0 offen sc1 lds\n"
"v_mfma_f32_16x16x32_bf16 v[118:121], a[40:43], v[62:65], v[118:121]\n"
"s_waitcnt lgkmcnt(4)\n"
"ds_read_b128 v[54:57], v3\n"
"v_mfma_f32_16x16x32_bf16 v[122:125], a[44:47], v[62:65], v[122:125]\n"
"s_add_u32 m0, s59, 118784\n"
"v_mfma_f32_16x16x32_bf16 v[126:129], a[40:43], v[66:69], v[126:129]\n"
"s_waitcnt lgkmcnt(4)\n"
"ds_read_b128 v[58:61], v3 offset:1024\n"
"v_mfma_f32_16x16x32_bf16 v[130:133], a[44:47], v[66:69], v[130:133]\n"
"buffer_load_dwordx4 v1, s[20:23], m0 offen sc1 lds\n"
"v_mfma_f32_16x16x32_bf16 v[134:137], a[40:43], v[70:73], v[134:137]\n"
"s_waitcnt lgkmcnt(4)\n"
"ds_read_b128 v[62:65], v3 offset:2048\n"
"v_mfma_f32_16x16x32_bf16 v[138:141], a[44:47], v[70:73], v[138:141]\n"
"s_add_u32 m0, s59, 122880\n"
"v_mfma_f32_16x16x32_bf16 v[142:145], a[40:43], v[74:77], v[142:145]\n"
"s_waitcnt lgkmcnt(4)\n"
"ds_read_b128 v[66:69], v3 offset:3072\n"
"v_mfma_f32_16x16x32_bf16 v[146:149], a[44:47], v[74:77], v[146:149]\n"
"buffer_load_dwordx4 v1, s[20:23], m0 offen sc1 lds\n"
"v_mfma_f32_16x16x32_bf16 v[110:113], a[48:51], v[78:81], v[110:113]\n"
"s_waitcnt lgkmcnt(4)\n"
"ds_read_b128 v[70:73], v3 offset:4096\n"
"v_mfma_f32_16x16x32_bf16 v[114:117], a[52:55], v[78:81], v[114:117]\n"
"s_add_u32 m0, s59, 126976\n"
"v_mfma_f32_16x16x32_bf16 v[118:121], a[48:51], v[82:85], v[118:121]\n"
"s_waitcnt lgkmcnt(4)\n"
"ds_read_b128 v[74:77], v3 offset:5120\n"
"v_mfma_f32_16x16x32_bf16 v[122:125], a[52:55], v[82:85], v[122:125]\n"
"buffer_load_dwordx4 v1, s[20:23], m0 offen sc1 lds\n"
"v_mfma_f32_16x16x32_bf16 v[126:129], a[48:51], v[54:57], v[126:129]\n"
"s_waitcnt lgkmcnt(4)\n"
"ds_read_b128 v[78:81], v3 offset:6144\n"
"v_mfma_f32_16x16x32_bf16 v[130:133], a[52:55], v[54:57], v[130:133]\n"
"buffer_load_dwordx4 v[150:153], v244, s[20:23], 0 offen sc1\n"
"v_mfma_f32_16x16x32_bf16 v[134:137], a[48:51], v[58:61], v[134:137]\n"
"ds_read_b128 v[82:85], v3 offset:7168\n"
"v_mfma_f32_16x16x32_bf16 v[138:141], a[52:55], v[58:61], v[138:141]\n"
"s_waitcnt vmcnt(14)\n"
"s_barrier\n"
"s_waitcnt lgkmcnt(5)\n"
"v_mfma_f32_16x16x32_bf16 v[142:145], a[48:51], v[62:65], v[142:145]\n"
"s_waitcnt lgkmcnt(4)\n"
"ds_read_b128 v[54:57], v3 offset:8192\n"
"ds_read_b64_tr_b16 a[180:181], v228\n"
"v_mfma_f32_16x16x32_bf16 v[146:149], a[52:55], v[62:65], v[146:149]\n"
"buffer_load_dwordx4 v[154:157], v244, s[20:23], s82 offen sc1\n"
"v_mfma_f32_16x16x32_bf16 v[110:113], a[56:59], v[66:69], v[110:113]\n"
"s_waitcnt lgkmcnt(5)\n"
"ds_read_b128 v[58:61], v3 offset:9216\n"
"ds_read_b64_tr_b16 a[182:183], v228 offset:5120\n"
"v_mfma_f32_16x16x32_bf16 v[114:117], a[60:63], v[66:69], v[114:117]\n"
"ds_read_b64_tr_b16 a[184:185], v229\n"
"v_mfma_f32_16x16x32_bf16 v[118:121], a[56:59], v[70:73], v[118:121]\n"
"s_waitcnt lgkmcnt(7)\n"
"ds_read_b128 v[62:65], v3 offset:10240\n"
"v_mfma_f32_16x16x32_bf16 v[122:125], a[60:63], v[70:73], v[122:125]\n"
"buffer_load_dwordx4 v[174:177], v51, s[24:27], 0 offen\n"
"v_mfma_f32_16x16x32_bf16 v[126:129], a[56:59], v[74:77], v[126:129]\n"
"s_waitcnt lgkmcnt(7)\n"
"ds_read_b128 v[66:69], v3 offset:11264\n"
"ds_read_b64_tr_b16 a[186:187], v229 offset:5120\n"
"v_mfma_f32_16x16x32_bf16 v[130:133], a[60:63], v[74:77], v[130:133]\n"
"ds_read_b64_tr_b16 a[160:161], v218\n"
"v_mfma_f32_16x16x32_bf16 v[134:137], a[56:59], v[78:81], v[134:137]\n"
"s_waitcnt lgkmcnt(9)\n"
"ds_read_b128 v[70:73], v3 offset:12288\n"
"ds_read_b64_tr_b16 a[162:163], v218 offset:1024\n"
"v_mfma_f32_16x16x32_bf16 v[138:141], a[60:63], v[78:81], v[138:141]\n"
"buffer_load_dwordx4 v[178:181], v51, s[24:27], 0 offen offset:64\n"
"v_mfma_f32_16x16x32_bf16 v[142:145], a[56:59], v[82:85], v[142:145]\n"
"s_waitcnt lgkmcnt(10)\n"
"ds_read_b128 v[74:77], v3 offset:13312\n"
"ds_read_b64_tr_b16 a[164:165], v190\n"
"v_mfma_f32_16x16x32_bf16 v[146:149], a[60:63], v[82:85], v[146:149]\n"
"v_mfma_f32_16x16x32_bf16 v[110:113], a[64:67], v[54:57], v[110:113]\n"
"s_waitcnt lgkmcnt(10)\n"
"ds_read_b128 v[78:81], v3 offset:14336\n"
"ds_read_b64_tr_b16 a[166:167], v190 offset:1024\n"
"v_mfma_f32_16x16x32_bf16 v[114:117], a[68:71], v[54:57], v[114:117]\n"
"buffer_load_dwordx2 v[182:183], v52, s[24:27], 0 offen\n"
"v_mfma_f32_16x16x32_bf16 v[118:121], a[64:67], v[58:61], v[118:121]\n"
"ds_read_b128 v[82:85], v3 offset:15360\n"
"ds_read_b64_tr_b16 a[168:169], v191\n"
"v_mfma_f32_16x16x32_bf16 v[122:125], a[68:71], v[58:61], v[122:125]\n"
"s_waitcnt vmcnt(14) lgkmcnt(0)\n"
"s_barrier\n"
"v_mfma_f32_16x16x32_bf16 v[126:129], a[64:67], v[62:65], v[126:129]\n"
"ds_read_b128 v[54:57], v4 offset:8192\n"
"ds_read_b64_tr_b16 a[170:171], v191 offset:1024\n"
"v_mfma_f32_16x16x32_bf16 v[130:133], a[68:71], v[62:65], v[130:133]\n"
"ds_read_b64_tr_b16 a[172:173], v192\n"
"buffer_store_dwordx4 a[180:183], v230, s[20:23], 0 offen sc1\n"
"v_mfma_f32_16x16x32_bf16 v[134:137], a[64:67], v[66:69], v[134:137]\n"
"ds_read_b128 v[58:61], v4 offset:9216\n"
"s_add_u32 m0, s59, 131072\n"
"v_mfma_f32_16x16x32_bf16 v[138:141], a[68:71], v[66:69], v[138:141]\n"
"ds_read_b64_tr_b16 a[174:175], v192 offset:1024\n"
"v_mfma_f32_16x16x32_bf16 v[142:145], a[64:67], v[70:73], v[142:145]\n"
"ds_read_b128 v[62:65], v4 offset:10240\n"
"v_mfma_f32_16x16x32_bf16 v[146:149], a[68:71], v[70:73], v[146:149]\n"
"ds_read_b64_tr_b16 a[176:177], v219\n"
"v_mfma_f32_16x16x32_bf16 v[110:113], a[72:75], v[74:77], v[110:113]\n"
"ds_read_b128 v[66:69], v4 offset:11264\n"
"ds_read_b64_tr_b16 a[178:179], v219 offset:1024\n"
"v_mfma_f32_16x16x32_bf16 v[114:117], a[76:79], v[74:77], v[114:117]\n"
"buffer_load_dwordx4 v1, s[20:23], m0 offen sc1 lds\n"
"v_mfma_f32_16x16x32_bf16 v[118:121], a[72:75], v[78:81], v[118:121]\n"
"ds_read_b128 v[70:73], v3 offset:16384\n"
"ds_read_b64_tr_b16 a[188:189], v234\n"
"v_mfma_f32_16x16x32_bf16 v[122:125], a[76:79], v[78:81], v[122:125]\n"
"v_mfma_f32_16x16x32_bf16 v[126:129], a[72:75], v[82:85], v[126:129]\n"
"s_waitcnt lgkmcnt(10)\n"
"ds_read_b128 v[74:77], v3 offset:17408\n"
"ds_read_b64_tr_b16 a[190:191], v234 offset:1024\n"
"v_mfma_f32_16x16x32_bf16 v[130:133], a[76:79], v[82:85], v[130:133]\n"
"buffer_store_dwordx4 a[184:187], v231, s[20:23], 0 offen sc1\n"
"v_mfma_f32_16x16x32_bf16 v[134:137], a[72:75], v[54:57], v[134:137]\n"
"s_waitcnt lgkmcnt(9)\n"
"ds_read_b128 v[78:81], v3 offset:18432\n"
"ds_read_b64_tr_b16 a[192:193], v193\n"
"v_mfma_f32_16x16x32_bf16 v[138:141], a[76:79], v[54:57], v[138:141]\n"
"ds_read_b64_tr_b16 a[194:195], v193 offset:1024\n"
"s_add_u32 m0, s59, 53248\n"
"v_mfma_f32_16x16x32_bf16 v[142:145], a[72:75], v[58:61], v[142:145]\n"
"s_waitcnt lgkmcnt(10)\n"
"ds_read_b128 v[82:85], v3 offset:19456\n"
"v_add_u32 v1, 81920, v1\n"
"v_mfma_f32_16x16x32_bf16 v[146:149], a[76:79], v[58:61], v[146:149]\n"
"ds_read_b64_tr_b16 a[196:197], v235\n"
"v_mfma_f32_16x16x32_bf16 v[110:113], a[80:83], v[62:65], v[110:113]\n"
"s_waitcnt lgkmcnt(10)\n"
"ds_read_b128 v[54:57], v3 offset:20480\n"
"buffer_load_dwordx4 v1, s[20:23], m0 offen sc1 lds\n"
"v_mfma_f32_16x16x32_bf16 v[114:117], a[84:87], v[62:65], v[114:117]\n"
"ds_read_b64_tr_b16 a[198:199], v235 offset:1024\n"
"v_mfma_f32_16x16x32_bf16 v[118:121], a[80:83], v[66:69], v[118:121]\n"
"ds_read_b128 v[58:61], v3 offset:21504\n"
"v_mfma_f32_16x16x32_bf16 v[122:125], a[84:87], v[66:69], v[122:125]\n"
"v_mfma_f32_16x16x32_bf16 v[194:197], v[86:89], a[160:163], 0\n"
"s_waitcnt lgkmcnt(11)\n"
"v_mfma_f32_16x16x32_bf16 v[126:129], a[80:83], v[70:73], v[126:129]\n"
"ds_read_b128 v[62:65], v3 offset:22528\n"
"v_mfma_f32_16x16x32_bf16 v[130:133], a[84:87], v[70:73], v[130:133]\n"
"v_mfma_f32_16x16x32_bf16 v[194:197], v[90:93], a[164:167], v[194:197]\n"
"s_waitcnt lgkmcnt(10)\n"
"buffer_store_dwordx4 a[160:163], v222, s[20:23], 0 offen sc1\n"
"v_mfma_f32_16x16x32_bf16 v[134:137], a[80:83], v[74:77], v[134:137]\n"
"ds_read_b128 v[66:69], v3 offset:23552\n"
"s_add_u32 m0, s59, 57344\n"
"v_mfma_f32_16x16x32_bf16 v[138:141], a[84:87], v[74:77], v[138:141]\n"
"v_mfma_f32_16x16x32_bf16 v[194:197], v[94:97], a[168:171], v[194:197]\n"
"s_waitcnt lgkmcnt(9)\n"
"v_mfma_f32_16x16x32_bf16 v[142:145], a[80:83], v[78:81], v[142:145]\n"
"ds_read_b128 v[70:73], v3 offset:24576\n"
"v_mfma_f32_16x16x32_bf16 v[146:149], a[84:87], v[78:81], v[146:149]\n"
"v_mfma_f32_16x16x32_bf16 v[194:197], v[98:101], a[172:175], v[194:197]\n"
"s_waitcnt lgkmcnt(7)\n"
"v_mfma_f32_16x16x32_bf16 v[110:113], a[88:91], v[82:85], v[110:113]\n"
"ds_read_b128 v[74:77], v3 offset:25600\n"
"v_mfma_f32_16x16x32_bf16 v[114:117], a[92:95], v[82:85], v[114:117]\n"
"v_mfma_f32_16x16x32_bf16 v[194:197], v[102:105], a[176:179], v[194:197]\n"
"s_waitcnt lgkmcnt(6)\n"
"v_mfma_f32_16x16x32_bf16 v[118:121], a[88:91], v[54:57], v[118:121]\n"
"s_waitcnt lgkmcnt(4)\n"
"ds_read_b128 v[78:81], v3 offset:26624\n"
"v_mfma_f32_16x16x32_bf16 v[122:125], a[92:95], v[54:57], v[122:125]\n"
"buffer_load_dwordx4 v1, s[20:23], m0 offen sc1 lds\n"
"v_mfma_f32_16x16x32_bf16 v[126:129], a[88:91], v[58:61], v[126:129]\n"
"s_waitcnt vmcnt(9)\n"
"ds_read_b128 v[82:85], v3 offset:27648\n"
"v_add_f32 v150, v150, v154\n"
"v_mfma_f32_16x16x32_bf16 v[130:133], a[92:95], v[58:61], v[130:133]\n"
"s_barrier\n"
"s_waitcnt lgkmcnt(5)\n"
"v_mfma_f32_16x16x32_bf16 v[134:137], a[88:91], v[62:65], v[134:137]\n"
"s_waitcnt lgkmcnt(4)\n"
"ds_read_b128 v[54:57], v3 offset:28672\n"
"v_add_f32 v151, v151, v155\n"
"v_mfma_f32_16x16x32_bf16 v[138:141], a[92:95], v[62:65], v[138:141]\n"
"buffer_store_dwordx4 a[164:167], v223, s[20:23], 0 offen sc1\n"
"v_mfma_f32_16x16x32_bf16 v[142:145], a[88:91], v[66:69], v[142:145]\n"
"s_waitcnt lgkmcnt(4)\n"
"ds_read_b128 v[58:61], v3 offset:29696\n"
"v_add_f32 v152, v152, v156\n"
"v_mfma_f32_16x16x32_bf16 v[146:149], a[92:95], v[66:69], v[146:149]\n"
"s_add_u32 m0, s59, 61440\n"
"v_add_f32 v153, v153, v157\n"
"v_add_f32_dpp v150, v150, v150 row_ror:8 row_mask:0xf bank_mask:0xf\n"
"v_mfma_f32_16x16x32_bf16 v[110:113], a[96:99], v[70:73], v[110:113]\n"
"s_waitcnt lgkmcnt(4)\n"
"ds_read_b128 v[62:65], v3 offset:30720\n"
"v_add_f32_dpp v151, v151, v151 row_ror:8 row_mask:0xf bank_mask:0xf\n"
"v_mfma_f32_16x16x32_bf16 v[114:117], a[100:103], v[70:73], v[114:117]\n"
"buffer_load_dwordx4 v1, s[20:23], m0 offen sc1 lds\n"
"v_mfma_f32_16x16x32_bf16 v[118:121], a[96:99], v[74:77], v[118:121]\n"
"s_waitcnt lgkmcnt(4)\n"
"ds_read_b128 v[66:69], v3 offset:31744\n"
"v_add_f32_dpp v152, v152, v152 row_ror:8 row_mask:0xf bank_mask:0xf\n"
"v_mfma_f32_16x16x32_bf16 v[122:125], a[100:103], v[74:77], v[122:125]\n"
"v_add_f32_dpp v153, v153, v153 row_ror:8 row_mask:0xf bank_mask:0xf\n"
"v_mov_b32 v154, v150\n"
"v_mfma_f32_16x16x32_bf16 v[126:129], a[96:99], v[78:81], v[126:129]\n"
"s_waitcnt lgkmcnt(4)\n"
"ds_read_b128 v[70:73], v3 offset:32768\n"
"v_mov_b32 v155, v151\n"
"v_mfma_f32_16x16x32_bf16 v[130:133], a[100:103], v[78:81], v[130:133]\n"
"buffer_store_dwordx4 a[168:171], v224, s[20:23], 0 offen sc1\n"
"v_mfma_f32_16x16x32_bf16 v[134:137], a[96:99], v[82:85], v[134:137]\n"
"s_waitcnt vmcnt(11) lgkmcnt(4)\n"
"ds_read_b128 v[74:77], v3 offset:33792\n"
"v_mov_b32 v156, v152\n"
"v_mfma_f32_16x16x32_bf16 v[138:141], a[100:103], v[82:85], v[138:141]\n"
"s_add_u32 m0, s59, 65536\n"
"v_mov_b32 v157, v153\n"
"v_lshlrev_b32 v198, 16, v174\n"
"v_mfma_f32_16x16x32_bf16 v[142:145], a[96:99], v[54:57], v[142:145]\n"
"s_waitcnt lgkmcnt(4)\n"
"ds_read_b128 v[78:81], v3 offset:34816\n"
"v_and_b32 v199, 4294901760, v174\n"
"v_mfma_f32_16x16x32_bf16 v[146:149], a[100:103], v[54:57], v[146:149]\n"
"v_permlane16_swap_b32 v150, v154\n"
"v_mfma_f32_16x16x32_bf16 v[110:113], a[104:107], v[58:61], v[110:113]\n"
"s_waitcnt lgkmcnt(4)\n"
"ds_read_b128 v[82:85], v3 offset:35840\n"
"v_add_f32 v150, v150, v154\n"
"v_mfma_f32_16x16x32_bf16 v[114:117], a[108:111], v[58:61], v[114:117]\n"
"buffer_load_dwordx4 v1, s[20:23], m0 offen sc1 lds\n"
"v_mfma_f32_16x16x32_bf16 v[118:121], a[104:107], v[62:65], v[118:121]\n"
"s_waitcnt lgkmcnt(4)\n"
"ds_read_b128 v[54:57], v3 offset:36864\n"
"v_mov_b32 v154, v150\n"
"v_mfma_f32_16x16x32_bf16 v[122:125], a[108:111], v[62:65], v[122:125]\n"
"v_permlane16_swap_b32 v151, v155\n"
"v_mfma_f32_16x16x32_bf16 v[126:129], a[104:107], v[66:69], v[126:129]\n"
"s_waitcnt lgkmcnt(4)\n"
"ds_read_b128 v[58:61], v3 offset:37888\n"
"v_add_f32 v151, v151, v155\n"
"v_mfma_f32_16x16x32_bf16 v[130:133], a[108:111], v[66:69], v[130:133]\n"
"buffer_store_dwordx4 a[172:175], v225, s[20:23], 0 offen sc1\n"
"v_mfma_f32_16x16x32_bf16 v[134:137], a[104:107], v[70:73], v[134:137]\n"
"s_waitcnt lgkmcnt(4)\n"
"ds_read_b128 v[62:65], v3 offset:38912\n"
"v_mov_b32 v155, v151\n"
"v_mfma_f32_16x16x32_bf16 v[138:141], a[108:111], v[70:73], v[138:141]\n"
"s_add_u32 m0, s59, 69632\n"
"v_lshlrev_b32 v200, 16, v175\n"
"v_and_b32 v201, 4294901760, v175\n"
"v_mfma_f32_16x16x32_bf16 v[142:145], a[104:107], v[74:77], v[142:145]\n"
"s_waitcnt lgkmcnt(4)\n"
"ds_read_b128 v[66:69], v3 offset:39936\n"
"v_lshlrev_b32 v202, 16, v176\n"
"v_mfma_f32_16x16x32_bf16 v[146:149], a[108:111], v[74:77], v[146:149]\n"
"v_permlane16_swap_b32 v152, v156\n"
"v_mfma_f32_16x16x32_bf16 v[110:113], a[112:115], v[78:81], v[110:113]\n"
"s_waitcnt lgkmcnt(4)\n"
"ds_read_b128 v[70:73], v3 offset:40960\n"
"v_add_f32 v152, v152, v156\n"
"v_mov_b32 v156, v152\n"
"v_mfma_f32_16x16x32_bf16 v[114:117], a[116:119], v[78:81], v[114:117]\n"
"buffer_load_dwordx4 v1, s[20:23], m0 offen sc1 lds\n"
"v_mfma_f32_16x16x32_bf16 v[118:121], a[112:115], v[82:85], v[118:121]\n"
"s_waitcnt lgkmcnt(4)\n"
"ds_read_b128 v[74:77], v3 offset:41984\n"
"v_and_b32 v203, 4294901760, v176\n"
"v_mfma_f32_16x16x32_bf16 v[122:125], a[116:119], v[82:85], v[122:125]\n"
"v_permlane16_swap_b32 v153, v157\n"
"v_mfma_f32_16x16x32_bf16 v[126:129], a[112:115], v[54:57], v[126:129]\n"
"s_waitcnt lgkmcnt(4)\n"
"ds_read_b128 v[78:81], v3 offset:43008\n"
"v_add_f32 v153, v153, v157\n"
"v_mfma_f32_16x16x32_bf16 v[130:133], a[116:119], v[54:57], v[130:133]\n"
"v_permlane32_swap_b32 v150, v154\n"
"v_mfma_f32_16x16x32_bf16 v[134:137], a[112:115], v[58:61], v[134:137]\n"
"ds_read_b128 v[82:85], v3 offset:44032\n"
"v_mov_b32 v157, v153\n"
"v_mfma_f32_16x16x32_bf16 v[138:141], a[116:119], v[58:61], v[138:141]\n"
"s_barrier\n"
"s_waitcnt lgkmcnt(5)\n"
"v_mfma_f32_16x16x32_bf16 v[142:145], a[112:115], v[62:65], v[142:145]\n"
"s_waitcnt lgkmcnt(4)\n"
"ds_read_b128 v[54:57], v3 offset:45056\n"
"v_add_f32 v150, v150, v154\n"
"v_mfma_f32_16x16x32_bf16 v[146:149], a[116:119], v[62:65], v[146:149]\n"
"buffer_store_dwordx4 a[176:179], v226, s[20:23], 0 offen sc1\n"
"v_mfma_f32_16x16x32_bf16 v[110:113], a[120:123], v[66:69], v[110:113]\n"
"ds_read_b128 v[58:61], v3 offset:46080\n"
"s_mov_b32 s3, 0\n"
"s_waitcnt vmcnt(10)\n"
"v_mfma_f32_16x16x32_bf16 v[114:117], a[124:127], v[66:69], v[114:117]\n"
"s_waitcnt lgkmcnt(5)\n"
"s_add_u32 m0, s59, 73728\n"
"v_lshlrev_b32 v204, 16, v177\n"
"v_and_b32 v205, 4294901760, v177\n"
"v_mfma_f32_16x16x32_bf16 v[118:121], a[120:123], v[70:73], v[118:121]\n"
"s_waitcnt lgkmcnt(4)\n"
"ds_read_b128 v[62:65], v3 offset:47104\n"
"buffer_store_dwordx2 v[8:9], v14, s[20:23], s3 offen sc1\n"
"v_mfma_f32_16x16x32_bf16 v[122:125], a[124:127], v[70:73], v[122:125]\n"
"buffer_load_dwordx4 v1, s[20:23], m0 offen sc1 lds\n"
"v_mfma_f32_16x16x32_bf16 v[126:129], a[120:123], v[74:77], v[126:129]\n"
"s_waitcnt lgkmcnt(4)\n"
"ds_read_b128 v[66:69], v3 offset:48128\n"
"v_lshlrev_b32 v206, 16, v178\n"
"v_mfma_f32_16x16x32_bf16 v[130:133], a[124:127], v[74:77], v[130:133]\n"
"v_permlane32_swap_b32 v151, v155\n"
"v_mfma_f32_16x16x32_bf16 v[134:137], a[120:123], v[78:81], v[134:137]\n"
"s_waitcnt lgkmcnt(4)\n"
"ds_read_b128 v[70:73], v3 offset:49152\n"
"v_add_f32 v151, v151, v155\n"
"v_mfma_f32_16x16x32_bf16 v[138:141], a[124:127], v[78:81], v[138:141]\n"
"buffer_store_dwordx4 a[188:191], v236, s[20:23], 0 offen sc1\n"
"v_mfma_f32_16x16x32_bf16 v[142:145], a[120:123], v[82:85], v[142:145]\n"
"s_waitcnt lgkmcnt(4)\n"
"ds_read_b128 v[74:77], v3 offset:50176\n"
"v_and_b32 v207, 4294901760, v178\n"
"v_mfma_f32_16x16x32_bf16 v[146:149], a[124:127], v[82:85], v[146:149]\n"
"s_add_u32 m0, s59, 77824\n"
"v_lshlrev_b32 v208, 16, v179\n"
"v_and_b32 v209, 4294901760, v179\n"
"v_mfma_f32_16x16x32_bf16 v[110:113], a[128:131], v[54:57], v[110:113]\n"
"s_waitcnt lgkmcnt(4)\n"
"ds_read_b128 v[78:81], v3 offset:51200\n"
"v_lshlrev_b32 v210, 16, v180\n"
"v_mfma_f32_16x16x32_bf16 v[114:117], a[132:135], v[54:57], v[114:117]\n"
"buffer_load_dwordx4 v1, s[20:23], m0 offen sc1 lds\n"
"v_mfma_f32_16x16x32_bf16 v[118:121], a[128:131], v[58:61], v[118:121]\n"
"s_waitcnt lgkmcnt(4)\n"
"ds_read_b128 v[82:85], v3 offset:52224\n"
"v_and_b32 v211, 4294901760, v180\n"
"v_mfma_f32_16x16x32_bf16 v[122:125], a[132:135], v[58:61], v[122:125]\n"
"v_permlane32_swap_b32 v152, v156\n"
"v_mfma_f32_16x16x32_bf16 v[126:129], a[128:131], v[62:65], v[126:129]\n"
"s_waitcnt lgkmcnt(4)\n"
"ds_read_b128 v[54:57], v3 offset:53248\n"
"v_add_f32 v152, v152, v156\n"
"v_mfma_f32_16x16x32_bf16 v[130:133], a[132:135], v[62:65], v[130:133]\n"
"v_permlane32_swap_b32 v153, v157\n"
"v_mfma_f32_16x16x32_bf16 v[134:137], a[128:131], v[66:69], v[134:137]\n"
"s_waitcnt lgkmcnt(4)\n"
"ds_read_b128 v[58:61], v3 offset:54272\n"
"v_add_f32 v153, v153, v157\n"
"v_mfma_f32_16x16x32_bf16 v[138:141], a[132:135], v[66:69], v[138:141]\n"
"buffer_store_dwordx4 a[192:195], v250, s[20:23], 0 offen sc1\n"
"v_mfma_f32_16x16x32_bf16 v[142:145], a[128:131], v[70:73], v[142:145]\n"
"s_waitcnt lgkmcnt(4)\n"
"ds_read_b128 v[62:65], v3 offset:55296\n"
"v_lshlrev_b32 v212, 16, v181\n"
"v_mfma_f32_16x16x32_bf16 v[146:149], a[132:135], v[70:73], v[146:149]\n"
"s_add_u32 m0, s59, 81920\n"
"v_and_b32 v213, 4294901760, v181\n"
"v_lshlrev_b32 v214, 16, v182\n"
"v_mfma_f32_16x16x32_bf16 v[110:113], a[136:139], v[74:77], v[110:113]\n"
"s_waitcnt lgkmcnt(4)\n"
"ds_read_b128 v[66:69], v3 offset:56320\n"
"v_and_b32 v215, 4294901760, v182\n"
"v_mfma_f32_16x16x32_bf16 v[114:117], a[140:143], v[74:77], v[114:117]\n"
"buffer_load_dwordx4 v1, s[20:23], m0 offen sc1 lds\n"
"v_mfma_f32_16x16x32_bf16 v[118:121], a[136:139], v[78:81], v[118:121]\n"
"s_waitcnt lgkmcnt(4)\n"
"ds_read_b128 v[70:73], v3 offset:57344\n"
"v_lshlrev_b32 v216, 16, v183\n"
"v_mfma_f32_16x16x32_bf16 v[122:125], a[140:143], v[78:81], v[122:125]\n"
"ds_write_b128 v245, v[150:153]\n"
"v_mfma_f32_16x16x32_bf16 v[126:129], a[136:139], v[82:85], v[126:129]\n"
"s_waitcnt lgkmcnt(5)\n"
"ds_read_b128 v[74:77], v3 offset:58368\n"
"v_and_b32 v217, 4294901760, v183\n"
"v_mfma_f32_16x16x32_bf16 v[130:133], a[140:143], v[82:85], v[130:133]\n"
"v_mfma_f32_16x16x32_bf16 v[134:137], a[136:139], v[54:57], v[134:137]\n"
"s_waitcnt lgkmcnt(5)\n"
"ds_read_b128 v[78:81], v3 offset:59392\n"
"v_mfma_f32_16x16x32_bf16 v[138:141], a[140:143], v[54:57], v[138:141]\n"
"buffer_store_dwordx4 a[196:199], v237, s[20:23], 0 offen sc1\n"
"v_mfma_f32_16x16x32_bf16 v[142:145], a[136:139], v[58:61], v[142:145]\n"
"ds_read_b128 v[82:85], v3 offset:60416\n"
"v_mfma_f32_16x16x32_bf16 v[146:149], a[140:143], v[58:61], v[146:149]\n"
"s_waitcnt lgkmcnt(3)\n"
"s_barrier\n"
"v_mfma_f32_16x16x32_bf16 v[110:113], a[144:147], v[62:65], v[110:113]\n"
"ds_read_b128 v[54:57], v3 offset:61440\n"
"v_mfma_f32_16x16x32_bf16 v[114:117], a[148:151], v[62:65], v[114:117]\n"
"s_add_u32 m0, s59, 86016\n"
"v_mfma_f32_16x16x32_bf16 v[118:121], a[144:147], v[66:69], v[118:121]\n"
"ds_read_b128 v[58:61], v3 offset:62464\n"
"v_mfma_f32_16x16x32_bf16 v[122:125], a[148:151], v[66:69], v[122:125]\n"
"buffer_load_dwordx4 v1, s[20:23], m0 offen sc1 lds\n"
"v_mfma_f32_16x16x32_bf16 v[126:129], a[144:147], v[70:73], v[126:129]\n"
"s_waitcnt lgkmcnt(4)\n"
"ds_read_b128 v[62:65], v3 offset:63488\n"
"v_mfma_f32_16x16x32_bf16 v[130:133], a[148:151], v[70:73], v[130:133]\n"
"v_mfma_f32_16x16x32_bf16 v[134:137], a[144:147], v[74:77], v[134:137]\n"
"s_waitcnt lgkmcnt(4)\n"
"ds_read_b128 v[66:69], v3 offset:64512\n"
"v_mfma_f32_16x16x32_bf16 v[138:141], a[148:151], v[74:77], v[138:141]\n"
"buffer_store_dword v194, v227, s[20:23], 0 offen sc1\n"
"v_mfma_f32_16x16x32_bf16 v[142:145], a[144:147], v[78:81], v[142:145]\n"
"s_add_u32 m0, s59, 90112\n"
"v_mfma_f32_16x16x32_bf16 v[146:149], a[148:151], v[78:81], v[146:149]\n"
"s_waitcnt lgkmcnt(4)\n"
"ds_read_b128 v[158:161], v47 offset:128\n"
"ds_read_b128 v[162:165], v47 offset:192\n"
"v_mfma_f32_16x16x32_bf16 v[110:113], a[152:155], v[82:85], v[110:113]\n"
"ds_read_b128 v[150:153], v47\n"
"v_mfma_f32_16x16x32_bf16 v[114:117], a[156:159], v[82:85], v[114:117]\n"
"s_waitcnt lgkmcnt(6)\n"
"buffer_load_dwordx4 v1, s[20:23], m0 offen sc1 lds\n"
"v_mfma_f32_16x16x32_bf16 v[118:121], a[152:155], v[54:57], v[118:121]\n"
"s_add_u32 m0, s59, 94208\n"
"v_mfma_f32_16x16x32_bf16 v[122:125], a[156:159], v[54:57], v[122:125]\n"
"s_waitcnt lgkmcnt(5)\n"
"ds_read_b128 v[154:157], v47 offset:64\n"
"ds_read_b32 v178, v49 offset:256\n"
"v_mfma_f32_16x16x32_bf16 v[126:129], a[152:155], v[58:61], v[126:129]\n"
"ds_read_b32 v176, v49 offset:128\n"
"ds_read_b32 v177, v49 offset:192\n"
"v_mfma_f32_16x16x32_bf16 v[130:133], a[156:159], v[58:61], v[130:133]\n"
"s_waitcnt lgkmcnt(8)\n"
"v_mfma_f32_16x16x32_bf16 v[134:137], a[152:155], v[62:65], v[134:137]\n"
"buffer_load_dwordx4 v1, s[20:23], m0 offen sc1 lds\n"
"v_mfma_f32_16x16x32_bf16 v[138:141], a[156:159], v[62:65], v[138:141]\n"
"s_waitcnt lgkmcnt(7)\n"
"ds_read_b32 v174, v49\n"
"ds_read_b32 v175, v49 offset:64\n"
"v_mfma_f32_16x16x32_bf16 v[142:145], a[152:155], v[66:69], v[142:145]\n"
"v_mfma_f32_16x16x32_bf16 v[146:149], a[156:159], v[66:69], v[146:149]\n"
"s_waitcnt lgkmcnt(8)\n"
"v_pk_mul_f32 v[166:167], v[158:159], s[70:71]\n"
"v_pk_mul_f32 v[168:169], v[160:161], s[70:71]\n"
"s_waitcnt lgkmcnt(7)\n"
"v_pk_mul_f32 v[170:171], v[162:163], s[70:71]\n"
"v_pk_mul_f32 v[172:173], v[164:165], s[70:71]\n"
"s_waitcnt lgkmcnt(4)\n"
"v_pk_fma_f32 v[142:143], v[150:151], v[178:179], v[142:143] op_sel_hi:[1,0,1] neg_lo:[1,0,0] neg_hi:[1,0,0]\n"
"v_pk_mul_f32 v[184:185], v[142:143], v[158:159]\n"
"v_pk_mul_f32 v[180:181], v[142:143], v[166:167]\n"
"v_pk_fma_f32 v[142:143], v[142:143], v[158:159], s[68:69]\n"
"v_exp_f32_e64 v180, v180 clamp\n"
"v_exp_f32_e64 v181, v181 clamp\n"
"v_max_f32 v142, s68, v142\n"
"v_max_f32 v143, s68, v143\n"
"v_pk_add_f32 v[142:143], v[142:143], v[180:181]\n"
"v_cvt_pk_bf16_f32 v234, v142, v143\n"
"v_pk_fma_f32 v[144:145], v[152:153], v[178:179], v[144:145] op_sel_hi:[1,0,1] neg_lo:[1,0,0] neg_hi:[1,0,0]\n"
"v_pk_mul_f32 v[186:187], v[144:145], v[160:161]\n"
"v_pk_mul_f32 v[182:183], v[144:145], v[168:169]\n"
"v_pk_fma_f32 v[144:145], v[144:145], v[160:161], s[68:69]\n"
"v_exp_f32_e64 v182, v182 clamp\n"
"v_exp_f32_e64 v183, v183 clamp\n"
"v_max_f32 v144, s68, v144\n"
"v_max_f32 v145, s68, v145\n"
"v_pk_add_f32 v[144:145], v[144:145], v[182:183]\n"
"v_cvt_pk_bf16_f32 v235, v144, v145\n"
"v_pk_fma_f32 v[146:147], v[154:155], v[178:179], v[146:147] op_sel_hi:[1,0,1] neg_lo:[1,0,0] neg_hi:[1,0,0]\n"
"v_pk_mul_f32 v[188:189], v[146:147], v[162:163]\n"
"v_pk_mul_f32 v[180:181], v[146:147], v[170:171]\n"
"v_pk_fma_f32 v[146:147], v[146:147], v[162:163], s[68:69]\n"
"v_exp_f32_e64 v180, v180 clamp\n"
"v_exp_f32_e64 v181, v181 clamp\n"
"v_max_f32 v146, s68, v146\n"
"v_max_f32 v147, s68, v147\n"
"v_pk_add_f32 v[146:147], v[146:147], v[180:181]\n"
"v_cvt_pk_bf16_f32 v236, v146, v147\n"
"v_pk_fma_f32 v[148:149], v[156:157], v[178:179], v[148:149] op_sel_hi:[1,0,1] neg_lo:[1,0,0] neg_hi:[1,0,0]\n"
"v_pk_mul_f32 v[190:191], v[148:149], v[164:165]\n"
"v_pk_mul_f32 v[182:183], v[148:149], v[172:173]\n"
"v_pk_fma_f32 v[148:149], v[148:149], v[164:165], s[68:69]\n"
"v_exp_f32_e64 v182, v182 clamp\n"
"v_exp_f32_e64 v183, v183 clamp\n"
"v_max_f32 v148, s68, v148\n"
"v_max_f32 v149, s68, v149\n"
"v_pk_add_f32 v[148:149], v[148:149], v[182:183]\n"
"v_cvt_pk_bf16_f32 v237, v148, v149\n"
"v_mov_b32 v192, 1065369472\n"
"v_cvt_pk_bf16_f32 v238, v184, v185\n"
"v_cndmask_b32 v238, v238, v192, s[74:75]\n"
"v_cvt_pk_bf16_f32 v239, v186, v187\n"
"v_cndmask_b32 v239, v239, v192, s[74:75]\n"
"v_cvt_pk_bf16_f32 v240, v188, v189\n"
"v_cndmask_b32 v240, v240, v192, s[74:75]\n"
"v_cvt_pk_bf16_f32 v241, v190, v191\n"
"v_cndmask_b32 v241, v241, v192, s[74:75]\n"
"buffer_store_dwordx4 v[30:33], v43, s[20:23], 0 offen sc1\n"
"s_waitcnt lgkmcnt(2)\n"
"v_pk_fma_f32 v[126:127], v[150:151], v[176:177], v[126:127] op_sel_hi:[1,0,1] neg_lo:[1,0,0] neg_hi:[1,0,0]\n"
"v_pk_mul_f32 v[126:127], v[126:127], v[158:159]\n"
"v_cvt_pk_bf16_f32 v226, v126, v127\n"
"v_pk_fma_f32 v[128:129], v[152:153], v[176:177], v[128:129] op_sel_hi:[1,0,1] neg_lo:[1,0,0] neg_hi:[1,0,0]\n"
"v_pk_mul_f32 v[128:129], v[128:129], v[160:161]\n"
"v_cvt_pk_bf16_f32 v227, v128, v129\n"
"v_pk_fma_f32 v[130:131], v[154:155], v[176:177], v[130:131] op_sel_hi:[1,0,1] neg_lo:[1,0,0] neg_hi:[1,0,0]\n"
"v_pk_mul_f32 v[130:131], v[130:131], v[162:163]\n"
"v_cvt_pk_bf16_f32 v228, v130, v131\n"
"v_pk_fma_f32 v[132:133], v[156:157], v[176:177], v[132:133] op_sel_hi:[1,0,1] neg_lo:[1,0,0] neg_hi:[1,0,0]\n"
"v_pk_mul_f32 v[132:133], v[132:133], v[164:165]\n"
"v_cvt_pk_bf16_f32 v229, v132, v133\n"
"v_pk_fma_f32 v[134:135], v[150:151], v[176:177], v[134:135] op_sel:[0,1,0] neg_lo:[1,0,0] neg_hi:[1,0,0]\n"
"v_pk_mul_f32 v[134:135], v[134:135], v[158:159]\n"
"v_cvt_pk_bf16_f32 v230, v134, v135\n"
"v_pk_fma_f32 v[136:137], v[152:153], v[176:177], v[136:137] op_sel:[0,1,0] neg_lo:[1,0,0] neg_hi:[1,0,0]\n"
"v_pk_mul_f32 v[136:137], v[136:137], v[160:161]\n"
"v_cvt_pk_bf16_f32 v231, v136, v137\n"
"v_pk_fma_f32 v[138:139], v[154:155], v[176:177], v[138:139] op_sel:[0,1,0] neg_lo:[1,0,0] neg_hi:[1,0,0]\n"
"v_pk_mul_f32 v[138:139], v[138:139], v[162:163]\n"
"v_cvt_pk_bf16_f32 v232, v138, v139\n"
"v_pk_fma_f32 v[140:141], v[156:157], v[176:177], v[140:141] op_sel:[0,1,0] neg_lo:[1,0,0] neg_hi:[1,0,0]\n"
"v_pk_mul_f32 v[140:141], v[140:141], v[164:165]\n"
"v_cvt_pk_bf16_f32 v233, v140, v141\n"
"buffer_store_dwordx4 v[30:33], v46, s[20:23], 0 offen sc1\n"
"v_mfma_f32_16x16x32_bf16 v[142:145], v[234:237], v[226:229], 0\n"
"v_mfma_f32_16x16x32_bf16 v[146:149], v[234:237], v[230:233], 0\n"
"v_mfma_f32_16x16x32_bf16 v[126:129], v[234:237], v[238:241], 0\n"
"s_waitcnt lgkmcnt(0)\n"
"v_pk_fma_f32 v[110:111], v[150:151], v[174:175], v[110:111] op_sel_hi:[1,0,1] neg_lo:[1,0,0] neg_hi:[1,0,0]\n"
"v_pk_mul_f32 v[180:181], v[110:111], v[166:167]\n"
"v_pk_fma_f32 v[110:111], v[110:111], v[158:159], s[68:69]\n"
"v_exp_f32_e64 v180, v180 clamp\n"
"v_exp_f32_e64 v181, v181 clamp\n"
"v_max_f32 v110, s68, v110\n"
"v_max_f32 v111, s68, v111\n"
"v_pk_add_f32 v[110:111], v[110:111], v[180:181]\n"
"v_cvt_pk_bf16_f32 v218, v110, v111\n"
"v_pk_fma_f32 v[112:113], v[152:153], v[174:175], v[112:113] op_sel_hi:[1,0,1] neg_lo:[1,0,0] neg_hi:[1,0,0]\n"
"v_pk_mul_f32 v[182:183], v[112:113], v[168:169]\n"
"v_pk_fma_f32 v[112:113], v[112:113], v[160:161], s[68:69]\n"
"v_exp_f32_e64 v182, v182 clamp\n"
"v_exp_f32_e64 v183, v183 clamp\n"
"v_max_f32 v112, s68, v112\n"
"v_max_f32 v113, s68, v113\n"
"v_pk_add_f32 v[112:113], v[112:113], v[182:183]\n"
"v_cvt_pk_bf16_f32 v219, v112, v113\n"
"v_pk_fma_f32 v[114:115], v[154:155], v[174:175], v[114:115] op_sel_hi:[1,0,1] neg_lo:[1,0,0] neg_hi:[1,0,0]\n"
"v_pk_mul_f32 v[180:181], v[114:115], v[170:171]\n"
"v_pk_fma_f32 v[114:115], v[114:115], v[162:163], s[68:69]\n"
"v_exp_f32_e64 v180, v180 clamp\n"
"v_exp_f32_e64 v181, v181 clamp\n"
"v_max_f32 v114, s68, v114\n"
"v_max_f32 v115, s68, v115\n"
"v_pk_add_f32 v[114:115], v[114:115], v[180:181]\n"
"v_cvt_pk_bf16_f32 v220, v114, v115\n"
"v_pk_fma_f32 v[116:117], v[156:157], v[174:175], v[116:117] op_sel_hi:[1,0,1] neg_lo:[1,0,0] neg_hi:[1,0,0]\n"
"v_pk_mul_f32 v[182:183], v[116:117], v[172:173]\n"
"v_pk_fma_f32 v[116:117], v[116:117], v[164:165], s[68:69]\n"
"v_exp_f32_e64 v182, v182 clamp\n"
"v_exp_f32_e64 v183, v183 clamp\n"
"v_max_f32 v116, s68, v116\n"
"v_max_f32 v117, s68, v117\n"
"v_pk_add_f32 v[116:117], v[116:117], v[182:183]\n"
"v_cvt_pk_bf16_f32 v221, v116, v117\n"
"buffer_store_dwordx4 v[30:33], v44, s[96:99], 0 offen sc1\n"
"ds_write_b128 v18, v[142:145]\n"
"ds_write_b128 v19, v[146:149]\n"
"ds_write_b128 v20, v[126:129]\n"
"v_mfma_f32_16x16x32_bf16 v[130:133], v[218:221], v[226:229], 0\n"
"v_mfma_f32_16x16x32_bf16 v[134:137], v[218:221], v[238:241], 0\n"
"v_pk_fma_f32 v[118:119], v[150:151], v[174:175], v[118:119] op_sel:[0,1,0] neg_lo:[1,0,0] neg_hi:[1,0,0]\n"
"v_pk_mul_f32 v[180:181], v[118:119], v[166:167]\n"
"v_pk_fma_f32 v[118:119], v[118:119], v[158:159], s[68:69]\n"
"v_exp_f32_e64 v180, v180 clamp\n"
"v_exp_f32_e64 v181, v181 clamp\n"
"v_max_f32 v118, s68, v118\n"
"v_max_f32 v119, s68, v119\n"
"v_pk_add_f32 v[118:119], v[118:119], v[180:181]\n"
"v_cvt_pk_bf16_f32 v222, v118, v119\n"
"v_pk_fma_f32 v[120:121], v[152:153], v[174:175], v[120:121] op_sel:[0,1,0] neg_lo:[1,0,0] neg_hi:[1,0,0]\n"
"v_pk_mul_f32 v[182:183], v[120:121], v[168:169]\n"
"v_pk_fma_f32 v[120:121], v[120:121], v[160:161], s[68:69]\n"
"v_exp_f32_e64 v182, v182 clamp\n"
"v_exp_f32_e64 v183, v183 clamp\n"
"v_max_f32 v120, s68, v120\n"
"v_max_f32 v121, s68, v121\n"
"v_pk_add_f32 v[120:121], v[120:121], v[182:183]\n"
"v_cvt_pk_bf16_f32 v223, v120, v121\n"
"v_pk_fma_f32 v[122:123], v[154:155], v[174:175], v[122:123] op_sel:[0,1,0] neg_lo:[1,0,0] neg_hi:[1,0,0]\n"
"v_pk_mul_f32 v[180:181], v[122:123], v[170:171]\n"
"v_pk_fma_f32 v[122:123], v[122:123], v[162:163], s[68:69]\n"
"v_exp_f32_e64 v180, v180 clamp\n"
"v_exp_f32_e64 v181, v181 clamp\n"
"v_max_f32 v122, s68, v122\n"
"v_max_f32 v123, s68, v123\n"
"v_pk_add_f32 v[122:123], v[122:123], v[180:181]\n"
"v_cvt_pk_bf16_f32 v224, v122, v123\n"
"v_pk_fma_f32 v[124:125], v[156:157], v[174:175], v[124:125] op_sel:[0,1,0] neg_lo:[1,0,0] neg_hi:[1,0,0]\n"
"v_pk_mul_f32 v[182:183], v[124:125], v[172:173]\n"
"v_pk_fma_f32 v[124:125], v[124:125], v[164:165], s[68:69]\n"
"v_exp_f32_e64 v182, v182 clamp\n"
"v_exp_f32_e64 v183, v183 clamp\n"
"v_max_f32 v124, s68, v124\n"
"v_max_f32 v125, s68, v125\n"
"v_pk_add_f32 v[124:125], v[124:125], v[182:183]\n"
"v_cvt_pk_bf16_f32 v225, v124, v125\n"
"s_nop 1\n"
"v_mfma_f32_16x16x32_bf16 v[110:113], v[222:225], v[230:233], 0\n"
"v_mfma_f32_16x16x32_bf16 v[114:117], v[222:225], v[238:241], 0\n"
"ds_write_b128 v15, v[130:133]\n"
"ds_write_b128 v16, v[134:137]\n"
"s_nop 4\n"
"ds_write_b128 v15, v[110:113] offset:1680\n"
"ds_write_b128 v17, v[114:117]\n"
"s_waitcnt vmcnt(17) lgkmcnt(0)\n"
"s_barrier\n"
"ds_read_b128 v[218:221], v45\n"
"ds_read_b128 v[222:225], v45 offset:4096\n"
"ds_read_b128 v[226:229], v45 offset:8192\n"
"ds_read_b128 v[230:233], v45 offset:12288\n"
"ds_read_b128 v[54:57], v4\n"
"ds_read_b128 v[58:61], v4 offset:1024\n"
"ds_read_b128 v[62:65], v4 offset:2048\n"
"ds_read_b128 v[66:69], v4 offset:3072\n"
"ds_read_b128 v[70:73], v2 offset:53248\n"
"ds_read_b128 v[74:77], v2 offset:54272\n"
"s_waitcnt lgkmcnt(8)\n"
"v_pk_add_f32 v[218:219], v[218:219], v[222:223]\n"
"v_pk_add_f32 v[220:221], v[220:221], v[224:225]\n"
"s_waitcnt lgkmcnt(6)\n"
"v_pk_add_f32 v[226:227], v[226:227], v[230:231]\n"
"v_pk_add_f32 v[228:229], v[228:229], v[232:233]\n"
"v_pk_add_f32 v[218:219], v[218:219], v[226:227]\n"
"v_pk_add_f32 v[220:221], v[220:221], v[228:229]\n"
"v_cvt_pk_bf16_f32 v218, v218, v219\n"
"v_cvt_pk_bf16_f32 v219, v220, v221\n"
"buffer_store_dwordx2 v[218:219], v21, s[96:99], s78 offen sc1\n"
"s_waitcnt lgkmcnt(5)\n"
"v_mfma_f32_16x16x32_bf16 v[86:89], v[54:57], a[0:3], 0\n"
"s_waitcnt lgkmcnt(4)\n"
"ds_read_b128 v[78:81], v2 offset:55296\n"
"v_mfma_f32_16x16x32_bf16 v[90:93], v[54:57], a[4:7], 0\n"
"s_add_u32 m0, s59, 98304\n"
"v_mfma_f32_16x16x32_bf16 v[94:97], v[58:61], a[0:3], 0\n"
"s_waitcnt lgkmcnt(4)\n"
"ds_read_b128 v[82:85], v2 offset:56320\n"
"v_cmp_gt_u32 vcc, 172, v0\n"
"v_mfma_f32_16x16x32_bf16 v[98:101], v[58:61], a[4:7], 0\n"
"buffer_load_dwordx4 v1, s[20:23], m0 offen sc1 lds\n"
"v_mfma_f32_16x16x32_bf16 v[102:105], v[62:65], a[0:3], 0\n"
"s_waitcnt lgkmcnt(4)\n"
"ds_read_b128 v[54:57], v2 offset:57344\n"
"v_mov_b32 v118, 161216\n"
"v_mfma_f32_16x16x32_bf16 v[106:109], v[62:65], a[4:7], 0\n"
"s_add_u32 m0, s59, 102400\n"
"v_lshlrev_b32 v110, 4, v0\n"
"v_add_u32 v110, 158464, v110\n"
"v_mfma_f32_16x16x32_bf16 v[86:89], v[66:69], a[8:11], v[86:89]\n"
"s_waitcnt lgkmcnt(4)\n"
"ds_read_b128 v[58:61], v2 offset:58368\n"
"v_cndmask_b32 v110, v118, v110, vcc\n"
"v_mfma_f32_16x16x32_bf16 v[90:93], v[66:69], a[12:15], v[90:93]\n"
"v_mov_b32 v114, 0\n"
"v_mov_b32 v115, 0\n"
"v_mfma_f32_16x16x32_bf16 v[94:97], v[70:73], a[8:11], v[94:97]\n"
"s_waitcnt lgkmcnt(4)\n"
"ds_read_b128 v[62:65], v2 offset:59392\n"
"v_mov_b32 v116, 0\n"
"v_mfma_f32_16x16x32_bf16 v[98:101], v[70:73], a[12:15], v[98:101]\n"
"v_mov_b32 v117, 0\n"
"v_bfe_u32 v120, v0, 4, 2\n"
"v_mfma_f32_16x16x32_bf16 v[102:105], v[74:77], a[8:11], v[102:105]\n"
"s_waitcnt lgkmcnt(4)\n"
"ds_read_b128 v[66:69], v2 offset:60416\n"
"v_lshrrev_b32 v119, 1, v120\n"
"v_mfma_f32_16x16x32_bf16 v[106:109], v[74:77], a[12:15], v[106:109]\n"
"buffer_load_dwordx4 v1, s[20:23], m0 offen sc1 lds\n"
"v_mfma_f32_16x16x32_bf16 v[86:89], v[78:81], a[16:19], v[86:89]\n"
"s_waitcnt lgkmcnt(4)\n"
"ds_read_b128 v[70:73], v2 offset:61440\n"
"v_and_b32 v121, 15, v0\n"
"v_mfma_f32_16x16x32_bf16 v[90:93], v[78:81], a[20:23], v[90:93]\n"
"s_add_u32 m0, s59, 106496\n"
"v_and_b32 v122, 1, v120\n"
"v_and_b32 v124, 31, v0\n"
"v_mfma_f32_16x16x32_bf16 v[94:97], v[82:85], a[16:19], v[94:97]\n"
"s_waitcnt lgkmcnt(4)\n"
"ds_read_b128 v[74:77], v2 offset:62464\n"
"s_lshl_b32 s4, s58, 6\n"
"v_mfma_f32_16x16x32_bf16 v[98:101], v[82:85], a[20:23], v[98:101]\n"
"ds_write_b128 v110, v[114:117]\n"
"v_mfma_f32_16x16x32_bf16 v[102:105], v[54:57], a[16:19], v[102:105]\n"
"s_waitcnt lgkmcnt(5)\n"
"ds_read_b128 v[78:81], v2 offset:63488\n"
"v_add_u32 v113, s4, v122\n"
"v_mfma_f32_16x16x32_bf16 v[106:109], v[54:57], a[20:23], v[106:109]\n"
"s_mov_b32 s63, 64\n"
"v_lshl_add_u32 v113, v119, 5, v113\n"
"v_lshl_add_u32 v113, v121, 1, v113\n"
"v_mfma_f32_16x16x32_bf16 v[86:89], v[58:61], a[24:27], v[86:89]\n"
"s_waitcnt lgkmcnt(5)\n"
"ds_read_b128 v[82:85], v2 offset:64512\n"
"buffer_load_dwordx2 v[254:255], v13, s[20:23], s63 offen sc1\n"
"v_mfma_f32_16x16x32_bf16 v[90:93], v[58:61], a[28:31], v[90:93]\n"
"buffer_load_dwordx4 v1, s[20:23], m0 offen sc1 lds\n"
"v_mfma_f32_16x16x32_bf16 v[94:97], v[62:65], a[24:27], v[94:97]\n"
"s_waitcnt lgkmcnt(5)\n"
"ds_read_b128 v[54:57], v3\n"
"s_lshl_b32 s5, s58, 5\n"
"v_mfma_f32_16x16x32_bf16 v[98:101], v[62:65], a[28:31], v[98:101]\n"
"v_add_u32 v123, s5, v124\n"
"v_cmp_gt_u32 vcc, 105, v123\n"
"v_mfma_f32_16x16x32_bf16 v[102:105], v[66:69], a[24:27], v[102:105]\n"
"s_waitcnt lgkmcnt(5)\n"
"ds_read_b128 v[58:61], v3 offset:1024\n"
"v_mov_b32 v125, 1073741824\n"
"v_mfma_f32_16x16x32_bf16 v[106:109], v[66:69], a[28:31], v[106:109]\n"
"s_lshl_b32 s6, s57, 7\n"
"s_add_u32 s7, s6, s58\n"
"v_mfma_f32_16x16x32_bf16 v[86:89], v[70:73], a[32:35], v[86:89]\n"
"ds_read_b128 v[62:65], v3 offset:2048\n"
"s_mov_b32 s3, 131072\n"
"s_waitcnt vmcnt(4)\n"
"v_mfma_f32_16x16x32_bf16 v[90:93], v[70:73], a[36:39], v[90:93]\n"
"s_waitcnt lgkmcnt(6)\n"
"s_lshl_b32 s8, s7, 9\n"
"s_add_u32 s9, s8, 2752512\n"
"v_mfma_f32_16x16x32_bf16 v[94:97], v[74:77], a[32:35], v[94:97]\n"
"ds_read_b128 v[66:69], v3 offset:3072\n"
"buffer_store_dwordx2 v[8:9], v14, s[20:23], s3 offen sc1\n"
"v_mfma_f32_16x16x32_bf16 v[98:101], v[74:77], a[36:39], v[98:101]\n"
"s_waitcnt lgkmcnt(6)\n"
"s_barrier\n"
"s_waitcnt lgkmcnt(5)\n"
"v_mfma_f32_16x16x32_bf16 v[102:105], v[78:81], a[32:35], v[102:105]\n"
"s_waitcnt lgkmcnt(4)\n"
"ds_read_b128 v[70:73], v3 offset:4096\n"
"v_lshl_add_u32 v53, v124, 4, s9\n"
"v_mfma_f32_16x16x32_bf16 v[106:109], v[78:81], a[36:39], v[106:109]\n"
"v_lshl_add_u32 v53, v119, 12, v53\n"
"v_cndmask_b32 v53, v125, v53, vcc\n"
"v_mfma_f32_16x16x32_bf16 v[86:89], v[82:85], a[40:43], v[86:89]\n"
"s_waitcnt lgkmcnt(4)\n"
"ds_read_b128 v[74:77], v3 offset:5120\n"
"v_and_b32 v126, 1, v0\n"
"v_mfma_f32_16x16x32_bf16 v[90:93], v[82:85], a[44:47], v[90:93]\n"
"v_cmp_eq_u32 vcc, 0, v126\n"
"v_mov_b32 v127, 1065353216\n"
"v_mfma_f32_16x16x32_bf16 v[94:97], v[54:57], a[40:43], v[94:97]\n"
"s_waitcnt lgkmcnt(4)\n"
"ds_read_b128 v[78:81], v3 offset:6144\n"
"v_mov_b32 v128, 16256\n"
"v_mfma_f32_16x16x32_bf16 v[98:101], v[54:57], a[44:47], v[98:101]\n"
"v_cndmask_b32 v238, v127, v128, vcc\n"
"v_lshrrev_b32 v130, 2, v0\n"
"v_mfma_f32_16x16x32_bf16 v[102:105], v[58:61], a[40:43], v[102:105]\n"
"s_waitcnt lgkmcnt(4)\n"
"ds_read_b128 v[82:85], v3 offset:7168\n"
"v_and_b32 v129, 4, v130\n"
"v_mfma_f32_16x16x32_bf16 v[106:109], v[58:61], a[44:47], v[106:109]\n"
"v_lshrrev_b32 v131, 1, v121\n"
"v_xor_b32 v239, v129, v131\n"
"v_mfma_f32_16x16x32_bf16 v[86:89], v[62:65], a[48:51], v[86:89]\n"
"s_waitcnt lgkmcnt(4)\n"
"ds_read_b128 v[54:57], v3 offset:8192\n"
"v_cmp_eq_u32 vcc, 0, v239\n"
"v_mfma_f32_16x16x32_bf16 v[90:93], v[62:65], a[52:55], v[90:93]\n"
"v_mul_u32_u24 v132, 52429, v113\n"
"v_lshrrev_b32 v112, 18, v132\n"
"v_mfma_f32_16x16x32_bf16 v[94:97], v[66:69], a[48:51], v[94:97]\n"
"s_waitcnt lgkmcnt(4)\n"
"ds_read_b128 v[58:61], v3 offset:9216\n"
"v_cndmask_b32 v234, 0, v238, vcc\n"
"v_mfma_f32_16x16x32_bf16 v[98:101], v[66:69], a[52:55], v[98:101]\n"
"v_cmp_eq_u32 vcc, 1, v239\n"
"v_mad_i32_i24 v111, v112, -5, v113\n"
"v_mfma_f32_16x16x32_bf16 v[102:105], v[70:73], a[48:51], v[102:105]\n"
"s_waitcnt lgkmcnt(4)\n"
"ds_read_b128 v[62:65], v3 offset:10240\n"
"v_lshlrev_b32 v133, 4, v111\n"
"v_mfma_f32_16x16x32_bf16 v[106:109], v[70:73], a[52:55], v[106:109]\n"
"v_cndmask_b32 v235, 0, v238, vcc\n"
"v_cmp_eq_u32 vcc, 2, v239\n"
"v_mfma_f32_16x16x32_bf16 v[86:89], v[74:77], a[56:59], v[86:89]\n"
"ds_read_b128 v[66:69], v3 offset:11264\n"
"v_mov_b32 v135, 0\n"
"v_mfma_f32_16x16x32_bf16 v[90:93], v[74:77], a[60:63], v[90:93]\n"
"s_waitcnt vmcnt(2)\n"
"v_cmp_ne_u64 s[18:19], v[254:255], v[8:9]\n"
"s_nop 0\n"
"s_and_b32 s18, s18, -1\n"
"s_cbranch_scc0 L3_%=\n"
"s_mov_b32 s72, 0\n"
"L4_%=:\n"
"buffer_load_dwordx2 v[254:255], v13, s[20:23], s63 offen sc1\n"
"s_add_u32 s72, s72, 1\n"
"s_waitcnt vmcnt(0)\n"
"v_cmp_ne_u64 s[18:19], v[254:255], v[8:9]\n"
"s_nop 0\n"
"s_and_b32 s18, s18, -1\n"
"s_cbranch_scc0 L3_%=\n"
"s_cmp_lt_u32 s72, 4194304\n"
"s_cbranch_scc1 L4_%=\n"
"L3_%=:\n"
"s_waitcnt vmcnt(1)\n"
"s_barrier\n"
"s_waitcnt lgkmcnt(5)\n"
"v_mfma_f32_16x16x32_bf16 v[94:97], v[78:81], a[56:59], v[94:97]\n"
"s_waitcnt lgkmcnt(4)\n"
"ds_read_b128 v[70:73], v3 offset:12288\n"
"s_add_u32 m0, s59, 135168\n"
"v_mfma_f32_16x16x32_bf16 v[98:101], v[78:81], a[60:63], v[98:101]\n"
"s_add_u32 s60, s65, 4294832128\n"
"v_add_u32 v1, s60, v2\n"
"v_cndmask_b32 v236, 0, v238, vcc\n"
"v_mfma_f32_16x16x32_bf16 v[102:105], v[82:85], a[56:59], v[102:105]\n"
"s_waitcnt lgkmcnt(4)\n"
"ds_read_b128 v[74:77], v3 offset:13312\n"
"v_cmp_eq_u32 vcc, 3, v239\n"
"v_mfma_f32_16x16x32_bf16 v[106:109], v[82:85], a[60:63], v[106:109]\n"
"buffer_load_dwordx4 v1, s[20:23], m0 offen sc1 lds\n"
"v_mfma_f32_16x16x32_bf16 v[86:89], v[54:57], a[64:67], v[86:89]\n"
"s_waitcnt lgkmcnt(4)\n"
"ds_read_b128 v[78:81], v3 offset:14336\n"
"s_add_u32 m0, s59, 139264\n"
"v_mfma_f32_16x16x32_bf16 v[90:93], v[54:57], a[68:71], v[90:93]\n"
"buffer_load_dwordx4 v1, s[20:23], m0 offen sc1 lds\n"
"v_mfma_f32_16x16x32_bf16 v[94:97], v[58:61], a[64:67], v[94:97]\n"
"s_waitcnt lgkmcnt(4)\n"
"ds_read_b128 v[82:85], v3 offset:15360\n"
"v_cndmask_b32 v237, 0, v238, vcc\n"
"v_mfma_f32_16x16x32_bf16 v[98:101], v[58:61], a[68:71], v[98:101]\n"
"s_add_u32 m0, s59, 143360\n"
"v_cmp_gt_u32 vcc, 4, v111\n"
"v_lshlrev_b32 v26, 4, v120\n"
"v_mfma_f32_16x16x32_bf16 v[102:105], v[62:65], a[64:67], v[102:105]\n"
"s_waitcnt lgkmcnt(4)\n"
"ds_read_b128 v[54:57], v3 offset:16384\n"
"v_add_u32 v26, 158464, v26\n"
"v_mfma_f32_16x16x32_bf16 v[106:109], v[62:65], a[68:71], v[106:109]\n"
"buffer_load_dwordx4 v1, s[20:23], m0 offen sc1 lds\n"
"v_mfma_f32_16x16x32_bf16 v[86:89], v[66:69], a[72:75], v[86:89]\n"
"s_waitcnt lgkmcnt(4)\n"
"ds_read_b128 v[58:61], v3 offset:17408\n"
"v_cndmask_b32 v133, 8, v133, vcc\n"
"v_mfma_f32_16x16x32_bf16 v[90:93], v[66:69], a[76:79], v[90:93]\n"
"s_add_u32 m0, s59, 147456\n"
"v_cmp_gt_u32 vcc, 105, v113\n"
"v_add_u32 v22, 158464, v133\n"
"v_mfma_f32_16x16x32_bf16 v[94:97], v[70:73], a[72:75], v[94:97]\n"
"s_waitcnt lgkmcnt(4)\n"
"ds_read_b128 v[62:65], v3 offset:18432\n"
"v_lshl_add_u32 v23, v121, 6, v26\n"
"v_mfma_f32_16x16x32_bf16 v[98:101], v[70:73], a[76:79], v[98:101]\n"
"buffer_load_dwordx4 v1, s[20:23], m0 offen sc1 lds\n"
"v_mfma_f32_16x16x32_bf16 v[102:105], v[74:77], a[72:75], v[102:105]\n"
"s_waitcnt lgkmcnt(4)\n"
"ds_read_b128 v[66:69], v3 offset:19456\n"
"v_cndmask_b32 v134, 8, v135, vcc\n"
"v_mfma_f32_16x16x32_bf16 v[106:109], v[74:77], a[76:79], v[106:109]\n"
"s_add_u32 m0, s59, 151552\n"
"v_add_u32 v22, v22, v134\n"
"v_lshl_add_u32 v22, v112, 6, v22\n"
"v_mfma_f32_16x16x32_bf16 v[86:89], v[78:81], a[80:83], v[86:89]\n"
"s_waitcnt lgkmcnt(4)\n"
"ds_read_b128 v[70:73], v3 offset:20480\n"
"v_cmp_gt_u32 vcc, 210, v113\n"
"v_mfma_f32_16x16x32_bf16 v[90:93], v[78:81], a[84:87], v[90:93]\n"
"buffer_load_dwordx4 v1, s[20:23], m0 offen sc1 lds\n"
"v_mfma_f32_16x16x32_bf16 v[94:97], v[82:85], a[80:83], v[94:97]\n"
"s_waitcnt lgkmcnt(4)\n"
"ds_read_b128 v[74:77], v3 offset:21504\n"
"v_cndmask_b32 v22, v118, v22, vcc\n"
"v_mfma_f32_16x16x32_bf16 v[98:101], v[82:85], a[84:87], v[98:101]\n"
"s_add_u32 m0, s59, 0\n"
"v_add_u32 v1, 155648, v1\n"
"v_cmp_gt_u32 vcc, 4, v121\n"
"v_mfma_f32_16x16x32_bf16 v[102:105], v[54:57], a[80:83], v[102:105]\n"
"s_waitcnt lgkmcnt(4)\n"
"ds_read_b128 v[78:81], v3 offset:22528\n"
"v_add_u32 v136, 16, v121\n"
"v_mfma_f32_16x16x32_bf16 v[106:109], v[54:57], a[84:87], v[106:109]\n"
"buffer_load_dwordx4 v1, s[20:23], m0 offen sc1 lds\n"
"v_mfma_f32_16x16x32_bf16 v[86:89], v[58:61], a[88:91], v[86:89]\n"
"s_waitcnt lgkmcnt(4)\n"
"ds_read_b128 v[82:85], v3 offset:23552\n"
"v_cndmask_b32 v136, 42, v136, vcc\n"
"v_mfma_f32_16x16x32_bf16 v[90:93], v[58:61], a[92:95], v[90:93]\n"
"s_add_u32 m0, s59, 4096\n"
"v_lshl_add_u32 v24, v136, 6, v26\n"
"v_lshrrev_b32 v138, 2, v121\n"
"v_mfma_f32_16x16x32_bf16 v[94:97], v[62:65], a[88:91], v[94:97]\n"
"s_waitcnt lgkmcnt(4)\n"
"ds_read_b128 v[54:57], v3 offset:24576\n"
"v_cmp_eq_u32 vcc, 1, v138\n"
"v_mfma_f32_16x16x32_bf16 v[98:101], v[62:65], a[92:95], v[98:101]\n"
"buffer_load_dwordx4 v1, s[20:23], m0 offen sc1 lds\n"
"v_mfma_f32_16x16x32_bf16 v[102:105], v[66:69], a[88:91], v[102:105]\n"
"s_waitcnt lgkmcnt(4)\n"
"ds_read_b128 v[58:61], v3 offset:25600\n"
"v_add_u32 v137, 33, v121\n"
"v_mfma_f32_16x16x32_bf16 v[106:109], v[66:69], a[92:95], v[106:109]\n"
"s_add_u32 m0, s59, 8192\n"
"v_cndmask_b32 v137, 42, v137, vcc\n"
"v_lshl_add_u32 v25, v137, 6, v26\n"
"v_mfma_f32_16x16x32_bf16 v[86:89], v[70:73], a[96:99], v[86:89]\n"
"s_waitcnt lgkmcnt(4)\n"
"ds_read_b128 v[62:65], v3 offset:26624\n"
"s_lshl2_add_u32 s10, s56, s58\n"
"v_mfma_f32_16x16x32_bf16 v[90:93], v[70:73], a[100:103], v[90:93]\n"
"buffer_load_dwordx4 v1, s[20:23], m0 offen sc1 lds\n"
"v_mfma_f32_16x16x32_bf16 v[94:97], v[74:77], a[96:99], v[94:97]\n"
"s_waitcnt lgkmcnt(4)\n"
"ds_read_b128 v[66:69], v3 offset:27648\n"
"s_add_u32 m0, s59, 12288\n"
"v_mfma_f32_16x16x32_bf16 v[98:101], v[74:77], a[100:103], v[98:101]\n"
"buffer_load_dwordx4 v1, s[20:23], m0 offen sc1 lds\n"
"v_mfma_f32_16x16x32_bf16 v[102:105], v[78:81], a[96:99], v[102:105]\n"
"s_waitcnt lgkmcnt(4)\n"
"ds_read_b128 v[70:73], v3 offset:28672\n"
"s_lshl_b32 s11, s10, 5\n"
"v_mfma_f32_16x16x32_bf16 v[106:109], v[78:81], a[100:103], v[106:109]\n"
"s_add_u32 m0, s59, 16384\n"
"v_cmp_gt_u32 vcc, 2, v120\n"
"v_mul_u32_u24 v29, 20224, v120\n"
"v_mfma_f32_16x16x32_bf16 v[86:89], v[82:85], a[104:107], v[86:89]\n"
"s_waitcnt lgkmcnt(4)\n"
"ds_read_b128 v[74:77], v3 offset:29696\n"
"s_add_u32 s12, s11, s57\n"
"v_mfma_f32_16x16x32_bf16 v[90:93], v[82:85], a[108:111], v[90:93]\n"
"buffer_load_dwordx4 v1, s[20:23], m0 offen sc1 lds\n"
"v_mfma_f32_16x16x32_bf16 v[94:97], v[54:57], a[104:107], v[94:97]\n"
"s_waitcnt lgkmcnt(4)\n"
"ds_read_b128 v[78:81], v3 offset:30720\n"
"s_mul_i32 s13, s12, 1280\n"
"v_mfma_f32_16x16x32_bf16 v[98:101], v[54:57], a[108:111], v[98:101]\n"
"s_add_u32 m0, s59, 20480\n"
"v_cmp_eq_u32 s[76:77], 1, v120\n"
"s_add_u32 s14, s13, 3801088\n"
"v_mfma_f32_16x16x32_bf16 v[102:105], v[58:61], a[104:107], v[102:105]\n"
"s_waitcnt lgkmcnt(4)\n"
"ds_read_b128 v[82:85], v3 offset:31744\n"
"v_add_u32 v28, s14, v2\n"
"v_mfma_f32_16x16x32_bf16 v[106:109], v[58:61], a[108:111], v[106:109]\n"
"buffer_load_dwordx4 v1, s[20:23], m0 offen sc1 lds\n"
"v_mfma_f32_16x16x32_bf16 v[86:89], v[62:65], a[112:115], v[86:89]\n"
"s_waitcnt lgkmcnt(4)\n"
"ds_read_b128 v[54:57], v3 offset:32768\n"
"v_add_u32 v29, v29, v28\n"
"v_mfma_f32_16x16x32_bf16 v[90:93], v[62:65], a[116:119], v[90:93]\n"
"s_add_u32 m0, s59, 24576\n"
"v_add_u32 v29, 1024, v29\n"
"v_cndmask_b32 v29, v125, v29, vcc\n"
"v_mfma_f32_16x16x32_bf16 v[94:97], v[66:69], a[112:115], v[94:97]\n"
"s_waitcnt lgkmcnt(4)\n"
"ds_read_b128 v[58:61], v3 offset:33792\n"
"v_mfma_f32_16x16x32_bf16 v[98:101], v[66:69], a[116:119], v[98:101]\n"
"buffer_load_dwordx4 v1, s[20:23], m0 offen sc1 lds\n"
"v_mfma_f32_16x16x32_bf16 v[102:105], v[70:73], a[112:115], v[102:105]\n"
"s_waitcnt lgkmcnt(4)\n"
"ds_read_b128 v[62:65], v3 offset:34816\n"
"v_mfma_f32_16x16x32_bf16 v[106:109], v[70:73], a[116:119], v[106:109]\n"
"s_add_u32 m0, s59, 28672\n"
"v_mfma_f32_16x16x32_bf16 v[86:89], v[74:77], a[120:123], v[86:89]\n"
"s_waitcnt lgkmcnt(4)\n"
"ds_read_b128 v[66:69], v3 offset:35840\n"
"v_mfma_f32_16x16x32_bf16 v[90:93], v[74:77], a[124:127], v[90:93]\n"
"buffer_load_dwordx4 v1, s[20:23], m0 offen sc1 lds\n"
"v_mfma_f32_16x16x32_bf16 v[94:97], v[78:81], a[120:123], v[94:97]\n"
"s_waitcnt lgkmcnt(4)\n"
"ds_read_b128 v[70:73], v3 offset:36864\n"
"v_mfma_f32_16x16x32_bf16 v[98:101], v[78:81], a[124:127], v[98:101]\n"
"s_add_u32 m0, s59, 32768\n"
"v_mfma_f32_16x16x32_bf16 v[102:105], v[82:85], a[120:123], v[102:105]\n"
"s_waitcnt lgkmcnt(4)\n"
"ds_read_b128 v[74:77], v3 offset:37888\n"
"v_mfma_f32_16x16x32_bf16 v[106:109], v[82:85], a[124:127], v[106:109]\n"
"buffer_load_dwordx4 v1, s[20:23], m0 offen sc1 lds\n"
"v_mfma_f32_16x16x32_bf16 v[86:89], v[54:57], a[128:131], v[86:89]\n"
"s_waitcnt lgkmcnt(4)\n"
"ds_read_b128 v[78:81], v3 offset:38912\n"
"v_mfma_f32_16x16x32_bf16 v[90:93], v[54:57], a[132:135], v[90:93]\n"
"s_add_u32 m0, s59, 36864\n"
"v_mfma_f32_16x16x32_bf16 v[94:97], v[58:61], a[128:131], v[94:97]\n"
"s_waitcnt lgkmcnt(4)\n"
"ds_read_b128 v[82:85], v3 offset:39936\n"
"v_mfma_f32_16x16x32_bf16 v[98:101], v[58:61], a[132:135], v[98:101]\n"
"buffer_load_dwordx4 v1, s[20:23], m0 offen sc1 lds\n"
"v_mfma_f32_16x16x32_bf16 v[102:105], v[62:65], a[128:131], v[102:105]\n"
"s_waitcnt lgkmcnt(4)\n"
"ds_read_b128 v[54:57], v3 offset:40960\n"
"ds_read_b32 v174, v48\n"
"v_mfma_f32_16x16x32_bf16 v[106:109], v[62:65], a[132:135], v[106:109]\n"
"v_mfma_f32_16x16x32_bf16 v[86:89], v[66:69], a[136:139], v[86:89]\n"
"s_waitcnt lgkmcnt(5)\n"
"ds_read_b128 v[58:61], v3 offset:41984\n"
"ds_read_b32 v175, v48 offset:64\n"
"v_mfma_f32_16x16x32_bf16 v[90:93], v[66:69], a[140:143], v[90:93]\n"
"v_mfma_f32_16x16x32_bf16 v[94:97], v[70:73], a[136:139], v[94:97]\n"
"s_waitcnt lgkmcnt(6)\n"
"ds_read_b128 v[62:65], v3 offset:43008\n"
"v_mfma_f32_16x16x32_bf16 v[98:101], v[70:73], a[140:143], v[98:101]\n"
"ds_read_b32 v176, v48 offset:128\n"
"v_mfma_f32_16x16x32_bf16 v[102:105], v[74:77], a[136:139], v[102:105]\n"
"s_waitcnt lgkmcnt(7)\n"
"ds_read_b128 v[66:69], v3 offset:44032\n"
"v_mfma_f32_16x16x32_bf16 v[106:109], v[74:77], a[140:143], v[106:109]\n"
"s_mov_b32 s63, 131072\n"
"v_mfma_f32_16x16x32_bf16 v[86:89], v[78:81], a[144:147], v[86:89]\n"
"buffer_load_dwordx2 v[254:255], v13, s[20:23], s63 offen sc1\n"
"v_mfma_f32_16x16x32_bf16 v[90:93], v[78:81], a[148:151], v[90:93]\n"
"s_waitcnt lgkmcnt(7)\n"
"ds_read_b32 v177, v48 offset:192\n"
"v_mfma_f32_16x16x32_bf16 v[94:97], v[82:85], a[144:147], v[94:97]\n"
"ds_read_b128 v[180:183], v50 offset:320\n"
"v_mfma_f32_16x16x32_bf16 v[98:101], v[82:85], a[148:151], v[98:101]\n"
"s_waitcnt lgkmcnt(8)\n"
"v_mfma_f32_16x16x32_bf16 v[102:105], v[54:57], a[144:147], v[102:105]\n"
"v_mfma_f32_16x16x32_bf16 v[106:109], v[54:57], a[148:151], v[106:109]\n"
"s_waitcnt lgkmcnt(6)\n"
"ds_read_b128 v[184:187], v50 offset:384\n"
"v_mfma_f32_16x16x32_bf16 v[86:89], v[58:61], a[152:155], v[86:89]\n"
"v_mfma_f32_16x16x32_bf16 v[90:93], v[58:61], a[156:159], v[90:93]\n"
"s_waitcnt lgkmcnt(5)\n"
"ds_read_b128 v[188:191], v50 offset:448\n"
"v_mfma_f32_16x16x32_bf16 v[94:97], v[62:65], a[152:155], v[94:97]\n"
"v_mfma_f32_16x16x32_bf16 v[98:101], v[62:65], a[156:159], v[98:101]\n"
"s_waitcnt lgkmcnt(4)\n"
"v_mfma_f32_16x16x32_bf16 v[102:105], v[66:69], a[152:155], v[102:105]\n"
"v_mfma_f32_16x16x32_bf16 v[106:109], v[66:69], a[156:159], v[106:109]\n"
"s_waitcnt lgkmcnt(3)\n"
"v_pk_mul_f32 v[178:179], v[176:177], s[70:71]\n"
"s_waitcnt lgkmcnt(2)\n"
"v_pk_fma_f32 v[86:87], v[174:175], v[180:181], v[86:87] op_sel_hi:[0,1,1] neg_lo:[1,0,0] neg_hi:[1,0,0]\n"
"v_pk_mul_f32 v[192:193], v[86:87], v[178:179] op_sel_hi:[1,0]\n"
"v_pk_fma_f32 v[86:87], v[86:87], v[176:177], s[68:69] op_sel_hi:[1,0,1]\n"
"v_exp_f32_e64 v192, v192 clamp\n"
"v_exp_f32_e64 v193, v193 clamp\n"
"v_max_f32 v86, s68, v86\n"
"v_max_f32 v87, s68, v87\n"
"v_pk_add_f32 v[86:87], v[86:87], v[192:193]\n"
"v_cvt_pk_bf16_f32 v218, v86, v87\n"
"v_pk_fma_f32 v[88:89], v[174:175], v[182:183], v[88:89] op_sel_hi:[0,1,1] neg_lo:[1,0,0] neg_hi:[1,0,0]\n"
"v_pk_mul_f32 v[194:195], v[88:89], v[178:179] op_sel_hi:[1,0]\n"
"v_pk_fma_f32 v[88:89], v[88:89], v[176:177], s[68:69] op_sel_hi:[1,0,1]\n"
"v_exp_f32_e64 v194, v194 clamp\n"
"v_exp_f32_e64 v195, v195 clamp\n"
"v_max_f32 v88, s68, v88\n"
"v_max_f32 v89, s68, v89\n"
"v_pk_add_f32 v[88:89], v[88:89], v[194:195]\n"
"v_cvt_pk_bf16_f32 v219, v88, v89\n"
"v_pk_fma_f32 v[90:91], v[174:175], v[180:181], v[90:91] op_sel:[1,0,0] neg_lo:[1,0,0] neg_hi:[1,0,0]\n"
"v_pk_mul_f32 v[192:193], v[90:91], v[178:179] op_sel:[0,1]\n"
"v_pk_fma_f32 v[90:91], v[90:91], v[176:177], s[68:69] op_sel:[0,1,0]\n"
"v_exp_f32_e64 v192, v192 clamp\n"
"v_exp_f32_e64 v193, v193 clamp\n"
"v_max_f32 v90, s68, v90\n"
"v_max_f32 v91, s68, v91\n"
"v_pk_add_f32 v[90:91], v[90:91], v[192:193]\n"
"v_cvt_pk_bf16_f32 v224, v90, v91\n"
"v_pk_fma_f32 v[92:93], v[174:175], v[182:183], v[92:93] op_sel:[1,0,0] neg_lo:[1,0,0] neg_hi:[1,0,0]\n"
"v_pk_mul_f32 v[194:195], v[92:93], v[178:179] op_sel:[0,1]\n"
"v_pk_fma_f32 v[92:93], v[92:93], v[176:177], s[68:69] op_sel:[0,1,0]\n"
"v_exp_f32_e64 v194, v194 clamp\n"
"v_exp_f32_e64 v195, v195 clamp\n"
"v_max_f32 v92, s68, v92\n"
"v_max_f32 v93, s68, v93\n"
"v_pk_add_f32 v[92:93], v[92:93], v[194:195]\n"
"v_cvt_pk_bf16_f32 v225, v92, v93\n"
"s_waitcnt lgkmcnt(1)\n"
"v_pk_fma_f32 v[94:95], v[174:175], v[184:185], v[94:95] op_sel_hi:[0,1,1] neg_lo:[1,0,0] neg_hi:[1,0,0]\n"
"v_pk_mul_f32 v[192:193], v[94:95], v[178:179] op_sel_hi:[1,0]\n"
"v_pk_fma_f32 v[94:95], v[94:95], v[176:177], s[68:69] op_sel_hi:[1,0,1]\n"
"v_exp_f32_e64 v192, v192 clamp\n"
"v_exp_f32_e64 v193, v193 clamp\n"
"v_max_f32 v94, s68, v94\n"
"v_max_f32 v95, s68, v95\n"
"v_pk_add_f32 v[94:95], v[94:95], v[192:193]\n"
"v_cvt_pk_bf16_f32 v222, v94, v95\n"
"v_pk_fma_f32 v[96:97], v[174:175], v[186:187], v[96:97] op_sel_hi:[0,1,1] neg_lo:[1,0,0] neg_hi:[1,0,0]\n"
"v_pk_mul_f32 v[194:195], v[96:97], v[178:179] op_sel_hi:[1,0]\n"
"v_pk_fma_f32 v[96:97], v[96:97], v[176:177], s[68:69] op_sel_hi:[1,0,1]\n"
"v_exp_f32_e64 v194, v194 clamp\n"
"v_exp_f32_e64 v195, v195 clamp\n"
"v_max_f32 v96, s68, v96\n"
"v_max_f32 v97, s68, v97\n"
"v_pk_add_f32 v[96:97], v[96:97], v[194:195]\n"
"v_cvt_pk_bf16_f32 v223, v96, v97\n"
"v_pk_fma_f32 v[98:99], v[174:175], v[184:185], v[98:99] op_sel:[1,0,0] neg_lo:[1,0,0] neg_hi:[1,0,0]\n"
"v_pk_mul_f32 v[192:193], v[98:99], v[178:179] op_sel:[0,1]\n"
"v_pk_fma_f32 v[98:99], v[98:99], v[176:177], s[68:69] op_sel:[0,1,0]\n"
"v_exp_f32_e64 v192, v192 clamp\n"
"v_exp_f32_e64 v193, v193 clamp\n"
"v_max_f32 v98, s68, v98\n"
"v_max_f32 v99, s68, v99\n"
"v_pk_add_f32 v[98:99], v[98:99], v[192:193]\n"
"v_cvt_pk_bf16_f32 v228, v98, v99\n"
"v_pk_fma_f32 v[100:101], v[174:175], v[186:187], v[100:101] op_sel:[1,0,0] neg_lo:[1,0,0] neg_hi:[1,0,0]\n"
"v_pk_mul_f32 v[194:195], v[100:101], v[178:179] op_sel:[0,1]\n"
"v_pk_fma_f32 v[100:101], v[100:101], v[176:177], s[68:69] op_sel:[0,1,0]\n"
"v_exp_f32_e64 v194, v194 clamp\n"
"v_exp_f32_e64 v195, v195 clamp\n"
"v_max_f32 v100, s68, v100\n"
"v_max_f32 v101, s68, v101\n"
"v_pk_add_f32 v[100:101], v[100:101], v[194:195]\n"
"v_cvt_pk_bf16_f32 v229, v100, v101\n"
"s_waitcnt lgkmcnt(0)\n"
"v_pk_fma_f32 v[102:103], v[174:175], v[188:189], v[102:103] op_sel_hi:[0,1,1] neg_lo:[1,0,0] neg_hi:[1,0,0]\n"
"v_pk_mul_f32 v[192:193], v[102:103], v[178:179] op_sel_hi:[1,0]\n"
"v_pk_fma_f32 v[102:103], v[102:103], v[176:177], s[68:69] op_sel_hi:[1,0,1]\n"
"v_exp_f32_e64 v192, v192 clamp\n"
"v_exp_f32_e64 v193, v193 clamp\n"
"v_max_f32 v102, s68, v102\n"
"v_max_f32 v103, s68, v103\n"
"v_pk_add_f32 v[102:103], v[102:103], v[192:193]\n"
"v_cvt_pk_bf16_f32 v220, v102, v103\n"
"s_waitcnt vmcnt(0)\n"
"v_cmp_ne_u64 s[18:19], v[254:255], v[8:9]\n"
"s_nop 0\n"
"s_and_b32 s18, s18, -1\n"
"s_cbranch_scc0 L5_%=\n"
"s_mov_b32 s72, 0\n"
"L6_%=:\n"
"buffer_load_dwordx2 v[254:255], v13, s[20:23], s63 offen sc1\n"
"s_add_u32 s72, s72, 1\n"
"s_waitcnt vmcnt(0)\n"
"v_cmp_ne_u64 s[18:19], v[254:255], v[8:9]\n"
"s_nop 0\n"
"s_and_b32 s18, s18, -1\n"
"s_cbranch_scc0 L5_%=\n"
"s_cmp_lt_u32 s72, 4194304\n"
"s_cbranch_scc1 L6_%=\n"
"L5_%=:\n"
"buffer_load_dwordx4 v[114:117], v53, s[96:99], 0 offen sc1\n"
"v_pk_fma_f32 v[104:105], v[174:175], v[190:191], v[104:105] op_sel_hi:[0,1,1] neg_lo:[1,0,0] neg_hi:[1,0,0]\n"
"v_pk_mul_f32 v[194:195], v[104:105], v[178:179] op_sel_hi:[1,0]\n"
"v_pk_fma_f32 v[104:105], v[104:105], v[176:177], s[68:69] op_sel_hi:[1,0,1]\n"
"buffer_load_dwordx4 v[118:121], v53, s[96:99], s81 offen sc1\n"
"v_exp_f32_e64 v194, v194 clamp\n"
"v_exp_f32_e64 v195, v195 clamp\n"
"v_max_f32 v104, s68, v104\n"
"buffer_load_dwordx4 v[122:125], v53, s[96:99], s83 offen sc1\n"
"v_max_f32 v105, s68, v105\n"
"v_pk_add_f32 v[104:105], v[104:105], v[194:195]\n"
"v_cvt_pk_bf16_f32 v221, v104, v105\n"
"v_pk_fma_f32 v[106:107], v[174:175], v[188:189], v[106:107] op_sel:[1,0,0] neg_lo:[1,0,0] neg_hi:[1,0,0]\n"
"buffer_load_dwordx4 v[126:129], v53, s[96:99], s85 offen sc1\n"
"v_pk_mul_f32 v[192:193], v[106:107], v[178:179] op_sel:[0,1]\n"
"v_pk_fma_f32 v[106:107], v[106:107], v[176:177], s[68:69] op_sel:[0,1,0]\n"
"v_exp_f32_e64 v192, v192 clamp\n"
"buffer_load_dwordx4 v[130:133], v53, s[96:99], s87 offen sc1\n"
"v_exp_f32_e64 v193, v193 clamp\n"
"v_max_f32 v106, s68, v106\n"
"v_max_f32 v107, s68, v107\n"
"buffer_load_dwordx4 v[134:137], v53, s[96:99], s89 offen sc1\n"
"v_pk_add_f32 v[106:107], v[106:107], v[192:193]\n"
"v_cvt_pk_bf16_f32 v226, v106, v107\n"
"v_pk_fma_f32 v[108:109], v[174:175], v[190:191], v[108:109] op_sel:[1,0,0] neg_lo:[1,0,0] neg_hi:[1,0,0]\n"
"v_pk_mul_f32 v[194:195], v[108:109], v[178:179] op_sel:[0,1]\n"
"buffer_load_dwordx4 v[138:141], v53, s[96:99], s91 offen sc1\n"
"v_pk_fma_f32 v[108:109], v[108:109], v[176:177], s[68:69] op_sel:[0,1,0]\n"
"v_exp_f32_e64 v194, v194 clamp\n"
"v_exp_f32_e64 v195, v195 clamp\n"
"buffer_load_dwordx4 v[142:145], v53, s[96:99], s93 offen sc1\n"
"v_max_f32 v108, s68, v108\n"
"v_max_f32 v109, s68, v109\n"
"v_pk_add_f32 v[108:109], v[108:109], v[194:195]\n"
"v_cvt_pk_bf16_f32 v227, v108, v109\n"
"s_barrier\n"
"v_lshl_add_u32 v244, s58, 7, v244\n"
"v_lshl_add_u32 v245, s58, 7, v245\n"
"buffer_load_dwordx4 v[86:89], v244, s[20:23], 0 offen offset:512 sc1\n"
"buffer_load_dwordx4 v[90:93], v244, s[20:23], s82 offen offset:512 sc1\n"
"buffer_load_dwordx4 v[94:97], v244, s[20:23], 0 offen offset:640 sc1\n"
"buffer_load_dwordx4 v[98:101], v244, s[20:23], s82 offen offset:640 sc1\n"
"s_waitcnt vmcnt(11)\n"
"v_mfma_f32_16x16x32_bf16 v[110:113], v[234:237], v[114:117], 0\n"
"s_waitcnt vmcnt(10)\n"
"v_mfma_f32_16x16x32_bf16 v[110:113], v[234:237], v[118:121], v[110:113]\n"
"s_waitcnt vmcnt(9)\n"
"v_mfma_f32_16x16x32_bf16 v[110:113], v[234:237], v[122:125], v[110:113]\n"
"s_waitcnt vmcnt(8)\n"
"v_mfma_f32_16x16x32_bf16 v[110:113], v[234:237], v[126:129], v[110:113]\n"
"s_waitcnt vmcnt(7)\n"
"v_mfma_f32_16x16x32_bf16 v[110:113], v[234:237], v[130:133], v[110:113]\n"
"s_waitcnt vmcnt(6)\n"
"v_mfma_f32_16x16x32_bf16 v[110:113], v[234:237], v[134:137], v[110:113]\n"
"s_waitcnt vmcnt(5)\n"
"v_mfma_f32_16x16x32_bf16 v[110:113], v[234:237], v[138:141], v[110:113]\n"
"s_waitcnt vmcnt(4)\n"
"v_mfma_f32_16x16x32_bf16 v[110:113], v[234:237], v[142:145], v[110:113]\n"
"s_nop 7\n"
"v_cvt_pk_bf16_f32 v110, v110, v111\n"
"v_cvt_pk_bf16_f32 v111, v112, v113\n"
"ds_write_b64 v22, v[110:111]\n"
"s_waitcnt lgkmcnt(0)\n"
"s_barrier\n"
"ds_read_b128 v[174:177], v26 offset:1280\n"
"ds_read_b128 v[178:181], v26 offset:2624\n"
"ds_read_b128 v[182:185], v23\n"
"ds_read_b128 v[186:189], v23 offset:1344\n"
"ds_read_b128 v[190:193], v24\n"
"ds_read_b128 v[194:197], v25\n"
"s_waitcnt lgkmcnt(5)\n"
"v_mfma_f32_16x16x32_bf16 v[122:125], v[174:177], v[218:221], 0\n"
"v_mfma_f32_16x16x32_bf16 v[142:145], v[174:177], v[224:227], 0\n"
"s_waitcnt lgkmcnt(4)\n"
"v_mfma_f32_16x16x32_bf16 v[126:129], v[178:181], v[220:223], 0\n"
"v_mfma_f32_16x16x32_bf16 v[146:149], v[178:181], v[226:229], 0\n"
"s_waitcnt lgkmcnt(3)\n"
"v_mfma_f32_16x16x32_bf16 v[110:113], v[182:185], v[218:221], 0\n"
"v_mfma_f32_16x16x32_bf16 v[130:133], v[182:185], v[224:227], 0\n"
"s_waitcnt lgkmcnt(2)\n"
"v_mfma_f32_16x16x32_bf16 v[114:117], v[186:189], v[220:223], 0\n"
"v_mfma_f32_16x16x32_bf16 v[134:137], v[186:189], v[226:229], 0\n"
"s_waitcnt lgkmcnt(1)\n"
"v_mfma_f32_16x16x32_bf16 v[118:121], v[190:193], v[218:221], 0\n"
"v_mfma_f32_16x16x32_bf16 v[138:141], v[190:193], v[224:227], 0\n"
"s_waitcnt lgkmcnt(0)\n"
"v_mfma_f32_16x16x32_bf16 v[118:121], v[194:197], v[220:223], v[118:121]\n"
"v_mfma_f32_16x16x32_bf16 v[138:141], v[194:197], v[226:229], v[138:141]\n"
"v_add_f32 v122, s68, v122\n"
"v_add_f32 v126, s68, v126\n"
"v_rcp_f32 v122, v122\n"
"v_rcp_f32 v126, v126\n"
"v_add_f32 v142, s68, v142\n"
"v_add_f32 v146, s68, v146\n"
"v_rcp_f32 v142, v142\n"
"v_rcp_f32 v146, v146\n"
"v_cndmask_b32 v123, v122, v126, s[76:77]\n"
"v_pk_mul_f32 v[110:111], v[110:111], v[122:123] op_sel_hi:[1,0]\n"
"v_pk_mul_f32 v[112:113], v[112:113], v[122:123] op_sel_hi:[1,0]\n"
"v_pk_mul_f32 v[114:115], v[114:115], v[126:127] op_sel_hi:[1,0]\n"
"v_pk_mul_f32 v[116:117], v[116:117], v[126:127] op_sel_hi:[1,0]\n"
"v_pk_mul_f32 v[118:119], v[118:119], v[122:123] op_sel:[0,1]\n"
"v_pk_mul_f32 v[120:121], v[120:121], v[122:123] op_sel:[0,1]\n"
"v_cvt_pk_bf16_f32 v218, v110, v111\n"
"v_cvt_pk_bf16_f32 v219, v112, v113\n"
"v_cvt_pk_bf16_f32 v220, v114, v115\n"
"v_cvt_pk_bf16_f32 v221, v116, v117\n"
"buffer_store_dwordx4 v[218:221], v28, s[96:99], 0 offen sc1\n"
"v_cvt_pk_bf16_f32 v230, v118, v119\n"
"v_cvt_pk_bf16_f32 v231, v120, v121\n"
"v_cndmask_b32 v143, v142, v146, s[76:77]\n"
"v_pk_mul_f32 v[130:131], v[130:131], v[142:143] op_sel_hi:[1,0]\n"
"v_pk_mul_f32 v[132:133], v[132:133], v[142:143] op_sel_hi:[1,0]\n"
"v_pk_mul_f32 v[134:135], v[134:135], v[146:147] op_sel_hi:[1,0]\n"
"v_pk_mul_f32 v[136:137], v[136:137], v[146:147] op_sel_hi:[1,0]\n"
"v_pk_mul_f32 v[138:139], v[138:139], v[142:143] op_sel:[0,1]\n"
"v_pk_mul_f32 v[140:141], v[140:141], v[142:143] op_sel:[0,1]\n"
"v_cvt_pk_bf16_f32 v222, v130, v131\n"
"v_cvt_pk_bf16_f32 v223, v132, v133\n"
"v_cvt_pk_bf16_f32 v224, v134, v135\n"
"v_cvt_pk_bf16_f32 v225, v136, v137\n"
"buffer_store_dwordx4 v[222:225], v28, s[96:99], s84 offen sc1\n"
"v_cvt_pk_bf16_f32 v232, v138, v139\n"
"v_cvt_pk_bf16_f32 v233, v140, v141\n"
"s_nop 0\n"
"v_permlane16_swap_b32 v230, v232\n"
"v_permlane16_swap_b32 v231, v233\n"
"buffer_store_dwordx4 v[230:233], v29, s[96:99], 0 offen sc1\n"
"v_bfe_u32 v56, v0, 4, 2\n"
"v_cmp_eq_u32 vcc, 0, v56\n"
"v_mov_b32 v57, 1073741824\n"
"v_and_b32 v58, 15, v0\n"
"s_lshr_b32 s4, s58, 1\n"
"s_and_b32 s5, s57, 1\n"
"s_lshl_b32 s6, s56, 5\n"
"s_add_u32 s7, s6, s5\n"
"s_lshl1_add_u32 s8, s7, s4\n"
"s_lshl_b32 s9, s8, 7\n"
"v_lshl_add_u32 v11, v58, 9, s9\n"
"v_cndmask_b32 v11, v57, v11, vcc\n"
"v_and_b32 v59, 14, v58\n"
"v_and_b32 v60, 1, v58\n"
"s_and_b32 s10, s58, 1\n"
"s_lshl4_add_u32 s11, s56, s4\n"
"s_lshl1_add_u32 s12, s11, s10\n"
"s_lshl_b32 s13, s12, 8\n"
"v_lshl_add_u32 v12, v59, 9, s13\n"
"v_lshl_add_u32 v12, v60, 7, v12\n"
"v_cndmask_b32 v12, v57, v12, vcc\n"
"s_lshl_b32 s14, s10, 10\n"
"v_add_u32 v5, s14, v2\n"
"v_add_u32 v6, 65536, v5\n"
"v_add_u32 v7, 131072, v5\n"
"s_lshl2_add_u32 s15, s56, s58\n"
"s_mul_i32 s16, s15, 40960\n"
"v_add_u32 v27, s16, v2\n"
"v_add_u32 v37, 6422528, v27\n"
"s_lshl1_add_u32 s17, s56, s5\n"
"s_lshl2_add_u32 s6, s17, s58\n"
"s_mul_i32 s7, s6, 20480\n"
"s_add_u32 s8, s7, 3801088\n"
"v_add_u32 v30, s8, v5\n"
"s_xor_b32 s9, s58, 1\n"
"s_lshl2_add_u32 s4, s17, s9\n"
"s_mul_i32 s11, s4, 20480\n"
"s_add_u32 s12, s11, 3801088\n"
"v_add_u32 v31, s12, v5\n"
"s_mul_i32 s13, s6, 55296\n"
"s_add_u32 s10, s13, 9175040\n"
"v_add_u32 v32, s10, v5\n"
"s_mul_i32 s14, s4, 55296\n"
"s_add_u32 s15, s14, 9175040\n"
"v_add_u32 v33, s15, v5\n"
"s_lshr_b32 s16, s57, 1\n"
"s_lshl3_add_u32 s5, s6, s16\n"
"s_mul_i32 s7, s5, 2560\n"
"s_add_u32 s8, s7, 6422528\n"
"v_add_u32 v34, s8, v2\n"
"v_and_b32 v61, 1, v56\n"
"v_cmp_eq_u32 vcc, 0, v61\n"
"s_add_u32 s9, s7, 6424576\n"
"v_lshl_add_u32 v35, v56, 7, s9\n"
"v_lshl_add_u32 v35, v58, 4, v35\n"
"v_cndmask_b32 v35, v57, v35, vcc\n"
"s_and_b32 s17, s58, 1\n"
"s_lshr_b32 s11, s58, 1\n"
"s_and_b32 s12, s57, 1\n"
"s_lshl3_add_u32 s13, s56, s16\n"
"s_lshl1_add_u32 s10, s13, s12\n"
"s_lshl1_add_u32 s4, s10, s11\n"
"s_lshl_b32 s14, s4, 5\n"
"s_add_u32 s15, s14, s17\n"
"s_lshl_b32 s6, s15, 3\n"
"s_add_u32 s5, s6, 9043968\n"
"v_lshl_add_u32 v36, v58, 4, s5\n"
"v_lshl_add_u32 v36, v56, 1, v36\n"
"v_cndmask_b32 v36, v57, v36, vcc\n"
"s_lshl_b32 s8, s56, 5\n"
"s_add_u32 s7, s8, s58\n"
"s_lshl_b32 s9, s7, 8\n"
"s_add_u32 s17, s9, 2621440\n"
"v_lshl_add_u32 v38, v56, 10, s17\n"
"v_lshl_add_u32 v38, v58, 4, v38\n"
"v_lshrrev_b32 v62, 1, v56\n"
"s_lshl2_add_u32 s11, s56, s58\n"
"s_mul_i32 s12, s11, 110592\n"
"s_add_u32 s16, s12, 9175040\n"
"v_mul_u32_u24 v54, 55296, v61\n"
"v_lshl_add_u32 v54, v62, 8, v54\n"
"v_lshl_add_u32 v54, v58, 4, v54\n"
"v_add_u32 v54, s16, v54\n"
"s_lshl_b32 s13, s57, 10\n"
"v_add_u32 v39, s13, v54\n"
"s_lshr_b32 s10, s57, 2\n"
"s_and_b32 s4, s57, 3\n"
"s_add_u32 s14, s4, 1\n"
"s_lshr_b32 s15, s14, 2\n"
"s_lshl_b32 s6, s15, 21\n"
"s_add_u32 s5, s6, s57\n"
"s_sub_u32 s8, s5, s10\n"
"s_lshl_b32 s7, s8, 9\n"
"s_add_u32 s9, s7, 49152\n"
"v_add_u32 v42, s9, v54\n"
"s_mul_i32 s17, s58, 5120\n"
"s_add_u32 s11, s17, 135168\n"
"v_add_u32 v43, s11, v2\n"
"s_xor_b32 s12, s58, 1\n"
"s_mul_i32 s16, s12, 5120\n"
"s_add_u32 s13, s16, 135168\n"
"v_add_u32 v44, s13, v2\n"
"v_lshl_add_u32 v46, v62, 6, v48\n"
"s_mov_b32 s79, 9830400\n"
"s_mov_b32 s95, 9830400\n"
"s_mov_b32 s54, 2621440\n"
"s_mov_b32 s55, 131072\n"
"s_mov_b32 s3, 262144\n"
"s_waitcnt vmcnt(0)\n"
"buffer_store_dwordx2 v[8:9], v14, s[20:23], s3 offen sc1\n"
"s_add_u32 m0, s59, 40960\n"
"s_nop 0\n"
"buffer_load_dwordx4 v1, s[20:23], m0 offen sc1 lds\n"
"s_add_u32 m0, s59, 45056\n"
"s_nop 0\n"
"buffer_load_dwordx4 v1, s[20:23], m0 offen sc1 lds\n"
"s_add_u32 m0, s59, 49152\n"
"s_nop 0\n"
"buffer_load_dwordx4 v1, s[20:23], m0 offen sc1 lds\n"
"s_add_u32 m0, s59, 53248\n"
"s_nop 0\n"
"buffer_load_dwordx4 v1, s[20:23], m0 offen sc1 lds\n"
"s_add_u32 m0, s59, 57344\n"
"s_nop 0\n"
"buffer_load_dwordx4 v1, s[20:23], m0 offen sc1 lds\n"
"s_add_u32 m0, s59, 61440\n"
"s_nop 0\n"
"buffer_load_dwordx4 v1, s[20:23], m0 offen sc1 lds\n"
"s_add_u32 m0, s59, 65536\n"
"s_nop 0\n"
"buffer_load_dwordx4 v1, s[20:23], m0 offen sc1 lds\n"
"s_add_u32 m0, s59, 69632\n"
"s_nop 0\n"
"buffer_load_dwordx4 v1, s[20:23], m0 offen sc1 lds\n"
"s_add_u32 m0, s59, 73728\n"
"s_nop 0\n"
"buffer_load_dwordx4 v1, s[20:23], m0 offen sc1 lds\n"
"s_add_u32 m0, s59, 77824\n"
"s_nop 0\n"
"buffer_load_dwordx4 v1, s[20:23], m0 offen sc1 lds\n"
"s_add_u32 m0, s59, 81920\n"
"s_add_u32 s60, s66, 4294885376\n"
"v_add_u32 v1, s60, v2\n"
"buffer_load_dwordx4 v1, s[20:23], m0 offen sc1 lds\n"
"s_add_u32 m0, s59, 86016\n"
"s_nop 0\n"
"buffer_load_dwordx4 v1, s[20:23], m0 offen sc1 lds\n"
"s_add_u32 m0, s59, 90112\n"
"s_nop 0\n"
"buffer_load_dwordx4 v1, s[20:23], m0 offen sc1 lds\n"
"s_add_u32 m0, s59, 94208\n"
"s_nop 0\n"
"buffer_load_dwordx4 v1, s[20:23], m0 offen sc1 lds\n"
"s_add_u32 m0, s59, 98304\n"
"s_nop 0\n"
"buffer_load_dwordx4 v1, s[20:23], m0 offen sc1 lds\n"
"s_add_u32 m0, s59, 102400\n"
"s_nop 0\n"
"buffer_load_dwordx4 v1, s[20:23], m0 offen sc1 lds\n"
"s_add_u32 m0, s59, 106496\n"
"s_nop 0\n"
"buffer_load_dwordx4 v1, s[20:23], m0 offen sc1 lds\n"
"v_add_f32 v86, v86, v90\n"
"v_add_f32 v87, v87, v91\n"
"v_add_f32 v88, v88, v92\n"
"v_add_f32 v89, v89, v93\n"
"v_add_f32_dpp v86, v86, v86 row_ror:8 row_mask:0xf bank_mask:0xf\n"
"v_add_f32_dpp v87, v87, v87 row_ror:8 row_mask:0xf bank_mask:0xf\n"
"v_add_f32_dpp v88, v88, v88 row_ror:8 row_mask:0xf bank_mask:0xf\n"
"v_add_f32_dpp v89, v89, v89 row_ror:8 row_mask:0xf bank_mask:0xf\n"
"v_mov_b32 v90, v86\n"
"v_mov_b32 v91, v87\n"
"v_mov_b32 v92, v88\n"
"v_mov_b32 v93, v89\n"
"v_permlane16_swap_b32 v86, v90\n"
"v_permlane16_swap_b32 v87, v91\n"
"v_permlane16_swap_b32 v88, v92\n"
"v_permlane16_swap_b32 v89, v93\n"
"v_add_f32 v86, v86, v90\n"
"v_add_f32 v87, v87, v91\n"
"v_add_f32 v88, v88, v92\n"
"v_add_f32 v89, v89, v93\n"
"v_mov_b32 v90, v86\n"
"v_mov_b32 v91, v87\n"
"v_mov_b32 v92, v88\n"
"v_mov_b32 v93, v89\n"
"v_permlane32_swap_b32 v86, v90\n"
"v_permlane32_swap_b32 v87, v91\n"
"v_permlane32_swap_b32 v88, v92\n"
"v_permlane32_swap_b32 v89, v93\n"
"v_add_f32 v86, v86, v90\n"
"v_add_f32 v87, v87, v91\n"
"v_add_f32 v88, v88, v92\n"
"v_add_f32 v89, v89, v93\n"
"ds_write_b128 v245, v[86:89] offset:512\n"
"v_add_f32 v94, v94, v98\n"
"v_add_f32 v95, v95, v99\n"
"v_add_f32 v96, v96, v100\n"
"v_add_f32 v97, v97, v101\n"
"v_add_f32_dpp v94, v94, v94 row_ror:8 row_mask:0xf bank_mask:0xf\n"
"v_add_f32_dpp v95, v95, v95 row_ror:8 row_mask:0xf bank_mask:0xf\n"
"v_add_f32_dpp v96, v96, v96 row_ror:8 row_mask:0xf bank_mask:0xf\n"
"v_add_f32_dpp v97, v97, v97 row_ror:8 row_mask:0xf bank_mask:0xf\n"
"v_mov_b32 v98, v94\n"
"v_mov_b32 v99, v95\n"
"v_mov_b32 v100, v96\n"
"v_mov_b32 v101, v97\n"
"v_permlane16_swap_b32 v94, v98\n"
"v_permlane16_swap_b32 v95, v99\n"
"v_permlane16_swap_b32 v96, v100\n"
"v_permlane16_swap_b32 v97, v101\n"
"v_add_f32 v94, v94, v98\n"
"v_add_f32 v95, v95, v99\n"
"v_add_f32 v96, v96, v100\n"
"v_add_f32 v97, v97, v101\n"
"v_mov_b32 v98, v94\n"
"v_mov_b32 v99, v95\n"
"v_mov_b32 v100, v96\n"
"v_mov_b32 v101, v97\n"
"v_permlane32_swap_b32 v94, v98\n"
"v_permlane32_swap_b32 v95, v99\n"
"v_permlane32_swap_b32 v96, v100\n"
"v_permlane32_swap_b32 v97, v101\n"
"v_add_f32 v94, v94, v98\n"
"v_add_f32 v95, v95, v99\n"
"v_add_f32 v96, v96, v100\n"
"v_add_f32 v97, v97, v101\n"
"ds_write_b128 v245, v[94:97] offset:640\n"
"s_waitcnt lgkmcnt(0)\n"
"s_barrier\n"
"s_mov_b32 s63, 262144\n"
"s_mov_b32 s72, 0\n"
"L8_%=:\n"
"buffer_load_dwordx2 v[254:255], v11, s[20:23], s63 offen sc1\n"
"s_add_u32 s72, s72, 1\n"
"s_waitcnt vmcnt(0)\n"
"v_cmp_ne_u64 s[18:19], v[254:255], v[8:9]\n"
"s_nop 0\n"
"s_and_b32 s18, s18, 65535\n"
"s_cbranch_scc0 L7_%=\n"
"s_cmp_lt_u32 s72, 4194304\n"
"s_cbranch_scc1 L8_%=\n"
"L7_%=:\n"
"; P3 layer 0\n"
"buffer_load_dwordx4 a[0:3], v30, s[96:99], 0 offen sc1\n"
"buffer_load_dwordx4 a[4:7], v31, s[96:99], 0 offen sc1\n"
"buffer_load_dwordx4 a[8:11], v30, s[96:99], 0 offen offset:2048 sc1\n"
"buffer_load_dwordx4 a[12:15], v31, s[96:99], 0 offen offset:2048 sc1\n"
"buffer_load_dwordx4 a[16:19], v30, s[96:99], s80 offen sc1\n"
"buffer_load_dwordx4 a[20:23], v31, s[96:99], s80 offen sc1\n"
"buffer_load_dwordx4 a[24:27], v30, s[96:99], s80 offen offset:2048 sc1\n"
"buffer_load_dwordx4 a[28:31], v31, s[96:99], s80 offen offset:2048 sc1\n"
"buffer_load_dwordx4 a[32:35], v30, s[96:99], s81 offen sc1\n"
"buffer_load_dwordx4 a[36:39], v31, s[96:99], s81 offen sc1\n"
"buffer_load_dwordx4 a[40:43], v30, s[96:99], s81 offen offset:2048 sc1\n"
"buffer_load_dwordx4 a[44:47], v31, s[96:99], s81 offen offset:2048 sc1\n"
"ds_read_b128 v[54:57], v7 offset:4096\n"
"ds_read_b128 v[58:61], v7 offset:6144\n"
"ds_read_b128 v[62:65], v7 offset:8192\n"
"ds_read_b128 v[66:69], v7 offset:10240\n"
"ds_read_b128 v[70:73], v7 offset:12288\n"
"ds_read_b128 v[74:77], v7 offset:14336\n"
"s_waitcnt vmcnt(11) lgkmcnt(5)\n"
"v_mfma_f32_16x16x32_bf16 v[150:153], v[54:57], a[0:3], v[198:201]\n"
"s_waitcnt vmcnt(10) lgkmcnt(4)\n"
"ds_read_b128 v[78:81], v7 offset:16384\n"
"v_mfma_f32_16x16x32_bf16 v[154:157], v[54:57], a[4:7], 0\n"
"buffer_load_dwordx4 a[48:51], v30, s[96:99], s82 offen sc1\n"
"v_mfma_f32_16x16x32_bf16 v[158:161], v[58:61], a[0:3], v[202:205]\n"
"s_waitcnt lgkmcnt(4)\n"
"ds_read_b128 v[82:85], v7 offset:18432\n"
"v_mfma_f32_16x16x32_bf16 v[162:165], v[58:61], a[4:7], 0\n"
"v_mfma_f32_16x16x32_bf16 v[182:185], v[62:65], a[0:3], v[214:217]\n"
"s_waitcnt vmcnt(10) lgkmcnt(4)\n"
"ds_read_b128 v[54:57], v7 offset:20480\n"
"v_mfma_f32_16x16x32_bf16 v[186:189], v[62:65], a[4:7], 0\n"
"buffer_load_dwordx4 a[52:55], v31, s[96:99], s82 offen sc1\n"
"v_mfma_f32_16x16x32_bf16 v[150:153], v[66:69], a[8:11], v[150:153]\n"
"s_waitcnt vmcnt(10) lgkmcnt(4)\n"
"ds_read_b128 v[58:61], v7 offset:22528\n"
"v_mfma_f32_16x16x32_bf16 v[154:157], v[66:69], a[12:15], v[154:157]\n"
"v_mfma_f32_16x16x32_bf16 v[158:161], v[70:73], a[8:11], v[158:161]\n"
"s_waitcnt lgkmcnt(4)\n"
"ds_read_b128 v[62:65], v5\n"
"v_mfma_f32_16x16x32_bf16 v[162:165], v[70:73], a[12:15], v[162:165]\n"
"buffer_load_dwordx4 a[56:59], v30, s[96:99], s82 offen offset:2048 sc1\n"
"v_mfma_f32_16x16x32_bf16 v[182:185], v[74:77], a[8:11], v[182:185]\n"
"s_waitcnt vmcnt(10) lgkmcnt(4)\n"
"ds_read_b128 v[66:69], v5 offset:2048\n"
"v_mfma_f32_16x16x32_bf16 v[186:189], v[74:77], a[12:15], v[186:189]\n"
"v_mfma_f32_16x16x32_bf16 v[150:153], v[78:81], a[16:19], v[150:153]\n"
"s_waitcnt vmcnt(9) lgkmcnt(4)\n"
"ds_read_b128 v[70:73], v5 offset:4096\n"
"v_mfma_f32_16x16x32_bf16 v[154:157], v[78:81], a[20:23], v[154:157]\n"
"buffer_load_dwordx4 a[60:63], v31, s[96:99], s82 offen offset:2048 sc1\n"
"v_mfma_f32_16x16x32_bf16 v[158:161], v[82:85], a[16:19], v[158:161]\n"
"s_waitcnt lgkmcnt(4)\n"
"ds_read_b128 v[74:77], v5 offset:6144\n"
"v_mfma_f32_16x16x32_bf16 v[162:165], v[82:85], a[20:23], v[162:165]\n"
"v_mfma_f32_16x16x32_bf16 v[182:185], v[54:57], a[16:19], v[182:185]\n"
"s_waitcnt vmcnt(9) lgkmcnt(4)\n"
"ds_read_b128 v[78:81], v5 offset:8192\n"
"v_mfma_f32_16x16x32_bf16 v[186:189], v[54:57], a[20:23], v[186:189]\n"
"buffer_load_dwordx4 a[64:67], v30, s[96:99], s83 offen sc1\n"
"v_mfma_f32_16x16x32_bf16 v[150:153], v[58:61], a[24:27], v[150:153]\n"
"s_waitcnt vmcnt(9)\n"
"ds_read_b128 v[82:85], v5 offset:10240\n"
"v_mfma_f32_16x16x32_bf16 v[154:157], v[58:61], a[28:31], v[154:157]\n"
"s_barrier\n"
"s_waitcnt lgkmcnt(5)\n"
"v_mfma_f32_16x16x32_bf16 v[158:161], v[62:65], a[24:27], v[158:161]\n"
"s_waitcnt lgkmcnt(4)\n"
"ds_read_b128 v[54:57], v5 offset:12288\n"
"v_mfma_f32_16x16x32_bf16 v[162:165], v[62:65], a[28:31], v[162:165]\n"
"buffer_load_dwordx4 a[68:71], v31, s[96:99], s83 offen sc1\n"
"v_mfma_f32_16x16x32_bf16 v[182:185], v[66:69], a[24:27], v[182:185]\n"
"s_waitcnt vmcnt(9) lgkmcnt(4)\n"
"ds_read_b128 v[58:61], v5 offset:14336\n"
"v_mfma_f32_16x16x32_bf16 v[186:189], v[66:69], a[28:31], v[186:189]\n"
"v_mfma_f32_16x16x32_bf16 v[150:153], v[70:73], a[32:35], v[150:153]\n"
"s_waitcnt vmcnt(8) lgkmcnt(4)\n"
"ds_read_b128 v[62:65], v5 offset:16384\n"
"v_mfma_f32_16x16x32_bf16 v[154:157], v[70:73], a[36:39], v[154:157]\n"
"buffer_load_dwordx4 a[72:75], v30, s[96:99], s83 offen offset:2048 sc1\n"
"v_mfma_f32_16x16x32_bf16 v[158:161], v[74:77], a[32:35], v[158:161]\n"
"s_waitcnt lgkmcnt(4)\n"
"ds_read_b128 v[66:69], v5 offset:18432\n"
"v_mfma_f32_16x16x32_bf16 v[162:165], v[74:77], a[36:39], v[162:165]\n"
"v_mfma_f32_16x16x32_bf16 v[182:185], v[78:81], a[32:35], v[182:185]\n"
"s_waitcnt vmcnt(8) lgkmcnt(4)\n"
"ds_read_b128 v[70:73], v5 offset:20480\n"
"v_mfma_f32_16x16x32_bf16 v[186:189], v[78:81], a[36:39], v[186:189]\n"
"buffer_load_dwordx4 a[76:79], v31, s[96:99], s83 offen offset:2048 sc1\n"
"v_mfma_f32_16x16x32_bf16 v[150:153], v[82:85], a[40:43], v[150:153]\n"
"s_waitcnt vmcnt(8) lgkmcnt(4)\n"
"ds_read_b128 v[74:77], v5 offset:22528\n"
"v_mfma_f32_16x16x32_bf16 v[154:157], v[82:85], a[44:47], v[154:157]\n"
"v_mfma_f32_16x16x32_bf16 v[158:161], v[54:57], a[40:43], v[158:161]\n"
"s_waitcnt lgkmcnt(4)\n"
"ds_read_b128 v[78:81], v5 offset:24576\n"
"v_mfma_f32_16x16x32_bf16 v[162:165], v[54:57], a[44:47], v[162:165]\n"
"v_mfma_f32_16x16x32_bf16 v[182:185], v[58:61], a[40:43], v[182:185]\n"
"s_waitcnt vmcnt(7) lgkmcnt(4)\n"
"ds_read_b128 v[82:85], v5 offset:26624\n"
"v_mfma_f32_16x16x32_bf16 v[186:189], v[58:61], a[44:47], v[186:189]\n"
"v_mfma_f32_16x16x32_bf16 v[150:153], v[62:65], a[48:51], v[150:153]\n"
"s_waitcnt vmcnt(6) lgkmcnt(4)\n"
"ds_read_b128 v[54:57], v5 offset:28672\n"
"v_mfma_f32_16x16x32_bf16 v[154:157], v[62:65], a[52:55], v[154:157]\n"
"s_add_u32 m0, s59, 110592\n"
"v_mfma_f32_16x16x32_bf16 v[158:161], v[66:69], a[48:51], v[158:161]\n"
"s_waitcnt lgkmcnt(4)\n"
"ds_read_b128 v[58:61], v5 offset:30720\n"
"v_mfma_f32_16x16x32_bf16 v[162:165], v[66:69], a[52:55], v[162:165]\n"
"buffer_load_dwordx4 v1, s[20:23], m0 offen sc1 lds\n"
"v_mfma_f32_16x16x32_bf16 v[182:185], v[70:73], a[48:51], v[182:185]\n"
"s_waitcnt vmcnt(6) lgkmcnt(4)\n"
"ds_read_b128 v[62:65], v5 offset:32768\n"
"v_mfma_f32_16x16x32_bf16 v[186:189], v[70:73], a[52:55], v[186:189]\n"
"s_add_u32 m0, s59, 114688\n"
"v_mfma_f32_16x16x32_bf16 v[150:153], v[74:77], a[56:59], v[150:153]\n"
"s_waitcnt vmcnt(5) lgkmcnt(4)\n"
"ds_read_b128 v[66:69], v5 offset:34816\n"
"v_mfma_f32_16x16x32_bf16 v[154:157], v[74:77], a[60:63], v[154:157]\n"
"buffer_load_dwordx4 v1, s[20:23], m0 offen sc1 lds\n"
"v_mfma_f32_16x16x32_bf16 v[158:161], v[78:81], a[56:59], v[158:161]\n"
"s_waitcnt lgkmcnt(4)\n"
"ds_read_b128 v[70:73], v5 offset:36864\n"
"v_mfma_f32_16x16x32_bf16 v[162:165], v[78:81], a[60:63], v[162:165]\n"
"s_add_u32 m0, s59, 118784\n"
"v_mfma_f32_16x16x32_bf16 v[182:185], v[82:85], a[56:59], v[182:185]\n"
"ds_read_b128 v[74:77], v5 offset:38912\n"
"v_mfma_f32_16x16x32_bf16 v[186:189], v[82:85], a[60:63], v[186:189]\n"
"s_barrier\n"
"s_waitcnt vmcnt(5) lgkmcnt(5)\n"
"v_mfma_f32_16x16x32_bf16 v[150:153], v[54:57], a[64:67], v[150:153]\n"
"s_waitcnt vmcnt(4) lgkmcnt(4)\n"
"ds_read_b128 v[78:81], v5 offset:40960\n"
"v_mfma_f32_16x16x32_bf16 v[154:157], v[54:57], a[68:71], v[154:157]\n"
"buffer_load_dwordx4 v1, s[20:23], m0 offen sc1 lds\n"
"v_mfma_f32_16x16x32_bf16 v[158:161], v[58:61], a[64:67], v[158:161]\n"
"s_waitcnt lgkmcnt(4)\n"
"ds_read_b128 v[82:85], v5 offset:43008\n"
"s_add_u32 m0, s59, 122880\n"
"v_mfma_f32_16x16x32_bf16 v[162:165], v[58:61], a[68:71], v[162:165]\n"
"buffer_load_dwordx4 v1, s[20:23], m0 offen sc1 lds\n"
"v_mfma_f32_16x16x32_bf16 v[182:185], v[62:65], a[64:67], v[182:185]\n"
"s_waitcnt vmcnt(5) lgkmcnt(4)\n"
"ds_read_b128 v[54:57], v5 offset:45056\n"
"v_mfma_f32_16x16x32_bf16 v[186:189], v[62:65], a[68:71], v[186:189]\n"
"s_add_u32 m0, s59, 126976\n"
"v_mfma_f32_16x16x32_bf16 v[150:153], v[66:69], a[72:75], v[150:153]\n"
"s_waitcnt vmcnt(4) lgkmcnt(4)\n"
"ds_read_b128 v[58:61], v5 offset:47104\n"
"v_mfma_f32_16x16x32_bf16 v[154:157], v[66:69], a[76:79], v[154:157]\n"
"buffer_load_dwordx4 v1, s[20:23], m0 offen sc1 lds\n"
"v_mfma_f32_16x16x32_bf16 v[158:161], v[70:73], a[72:75], v[158:161]\n"
"s_waitcnt lgkmcnt(4)\n"
"ds_read_b128 v[62:65], v5 offset:49152\n"
"v_mfma_f32_16x16x32_bf16 v[162:165], v[70:73], a[76:79], v[162:165]\n"
"s_add_u32 m0, s59, 131072\n"
"v_mfma_f32_16x16x32_bf16 v[182:185], v[74:77], a[72:75], v[182:185]\n"
"s_waitcnt lgkmcnt(4)\n"
"ds_read_b128 v[66:69], v5 offset:51200\n"
"v_mfma_f32_16x16x32_bf16 v[186:189], v[74:77], a[76:79], v[186:189]\n"
"buffer_load_dwordx4 v1, s[20:23], m0 offen sc1 lds\n"
"v_mfma_f32_16x16x32_bf16 v[166:169], v[78:81], a[0:3], v[206:209]\n"
"s_waitcnt lgkmcnt(4)\n"
"ds_read_b128 v[70:73], v5 offset:53248\n"
"v_mfma_f32_16x16x32_bf16 v[170:173], v[78:81], a[4:7], 0\n"
"v_mfma_f32_16x16x32_bf16 v[174:177], v[82:85], a[0:3], v[210:213]\n"
"s_waitcnt lgkmcnt(4)\n"
"ds_read_b128 v[74:77], v5 offset:55296\n"
"v_mfma_f32_16x16x32_bf16 v[178:181], v[82:85], a[4:7], 0\n"
"ds_write_b128 v43, v[154:157]\n"
"v_mfma_f32_16x16x32_bf16 v[166:169], v[54:57], a[8:11], v[166:169]\n"
"s_waitcnt lgkmcnt(5)\n"
"ds_read_b128 v[78:81], v5 offset:57344\n"
"v_mfma_f32_16x16x32_bf16 v[170:173], v[54:57], a[12:15], v[170:173]\n"
"ds_write_b128 v43, v[162:165] offset:1024\n"
"v_mfma_f32_16x16x32_bf16 v[174:177], v[58:61], a[8:11], v[174:177]\n"
"ds_read_b128 v[82:85], v5 offset:59392\n"
"ds_write_b128 v43, v[186:189] offset:4096\n"
"v_mfma_f32_16x16x32_bf16 v[178:181], v[58:61], a[12:15], v[178:181]\n"
"s_waitcnt lgkmcnt(0)\n"
"s_barrier\n"
"v_mfma_f32_16x16x32_bf16 v[166:169], v[62:65], a[16:19], v[166:169]\n"
"ds_read_b128 v[54:57], v5 offset:61440\n"
"ds_read_b128 v[86:89], v44\n"
"v_mfma_f32_16x16x32_bf16 v[170:173], v[62:65], a[20:23], v[170:173]\n"
"s_add_u32 m0, s59, 0\n"
"v_add_u32 v1, 135168, v1\n"
"v_mfma_f32_16x16x32_bf16 v[174:177], v[66:69], a[16:19], v[174:177]\n"
"ds_read_b128 v[58:61], v5 offset:63488\n"
"ds_read_b128 v[90:93], v44 offset:1024\n"
"ds_read_b128 v[102:105], v44 offset:4096\n"
"v_mfma_f32_16x16x32_bf16 v[178:181], v[66:69], a[20:23], v[178:181]\n"
"buffer_load_dwordx4 v1, s[20:23], m0 offen sc1 lds\n"
"v_mfma_f32_16x16x32_bf16 v[166:169], v[70:73], a[24:27], v[166:169]\n"
"ds_read_b128 v[62:65], v6\n"
"v_mfma_f32_16x16x32_bf16 v[170:173], v[70:73], a[28:31], v[170:173]\n"
"s_add_u32 m0, s59, 4096\n"
"v_mfma_f32_16x16x32_bf16 v[174:177], v[74:77], a[24:27], v[174:177]\n"
"s_waitcnt lgkmcnt(4)\n"
"ds_read_b128 v[66:69], v6 offset:2048\n"
"v_add_f32 v198, v150, v86\n"
"v_add_f32 v199, v151, v87\n"
"v_add_f32 v200, v152, v88\n"
"v_add_f32 v201, v153, v89\n"
"v_mfma_f32_16x16x32_bf16 v[178:181], v[74:77], a[28:31], v[178:181]\n"
"buffer_load_dwordx4 v1, s[20:23], m0 offen sc1 lds\n"
"v_mfma_f32_16x16x32_bf16 v[166:169], v[78:81], a[32:35], v[166:169]\n"
"s_waitcnt lgkmcnt(3)\n"
"ds_read_b128 v[70:73], v6 offset:4096\n"
"v_cvt_pk_bf16_f32 v218, v198, v199\n"
"v_cvt_pk_bf16_f32 v219, v200, v201\n"
"v_mfma_f32_16x16x32_bf16 v[170:173], v[78:81], a[36:39], v[170:173]\n"
"s_add_u32 m0, s59, 8192\n"
"v_add_f32 v202, v158, v90\n"
"v_add_f32 v203, v159, v91\n"
"v_add_f32 v204, v160, v92\n"
"v_mfma_f32_16x16x32_bf16 v[174:177], v[82:85], a[32:35], v[174:177]\n"
"ds_read_b128 v[74:77], v6 offset:6144\n"
"v_add_f32 v205, v161, v93\n"
"v_cvt_pk_bf16_f32 v220, v202, v203\n"
"v_mfma_f32_16x16x32_bf16 v[178:181], v[82:85], a[36:39], v[178:181]\n"
"buffer_load_dwordx4 v1, s[20:23], m0 offen sc1 lds\n"
"v_cvt_pk_bf16_f32 v221, v204, v205\n"
"v_mfma_f32_16x16x32_bf16 v[166:169], v[54:57], a[40:43], v[166:169]\n"
"s_waitcnt lgkmcnt(4)\n"
"ds_read_b128 v[78:81], v6 offset:8192\n"
"v_add_f32 v214, v182, v102\n"
"v_add_f32 v215, v183, v103\n"
"v_mfma_f32_16x16x32_bf16 v[170:173], v[54:57], a[44:47], v[170:173]\n"
"s_add_u32 m0, s59, 12288\n"
"v_add_f32 v216, v184, v104\n"
"v_add_f32 v217, v185, v105\n"
"v_mfma_f32_16x16x32_bf16 v[174:177], v[58:61], a[40:43], v[174:177]\n"
"s_waitcnt lgkmcnt(4)\n"
"ds_read_b128 v[82:85], v6 offset:10240\n"
"v_cvt_pk_bf16_f32 v226, v214, v215\n"
"v_cvt_pk_bf16_f32 v227, v216, v217\n"
"v_mfma_f32_16x16x32_bf16 v[178:181], v[58:61], a[44:47], v[178:181]\n"
"buffer_load_dwordx4 v1, s[20:23], m0 offen sc1 lds\n"
"v_mov_b32 v228, v226\n"
"v_mfma_f32_16x16x32_bf16 v[166:169], v[62:65], a[48:51], v[166:169]\n"
"s_waitcnt lgkmcnt(4)\n"
"ds_read_b128 v[54:57], v6 offset:12288\n"
"v_mov_b32 v229, v227\n"
"v_add_f32 v230, v198, v200\n"
"v_mfma_f32_16x16x32_bf16 v[170:173], v[62:65], a[52:55], v[170:173]\n"
"s_add_u32 m0, s59, 16384\n"
"v_add_f32 v231, v199, v201\n"
"v_mul_f32 v232, v198, v198\n"
"v_mul_f32 v233, v199, v199\n"
"v_mfma_f32_16x16x32_bf16 v[174:177], v[66:69], a[48:51], v[174:177]\n"
"s_waitcnt lgkmcnt(4)\n"
"ds_read_b128 v[58:61], v6 offset:14336\n"
"v_fmac_f32 v232, v200, v200\n"
"v_fmac_f32 v233, v201, v201\n"
"v_mfma_f32_16x16x32_bf16 v[178:181], v[66:69], a[52:55], v[178:181]\n"
"buffer_load_dwordx4 v1, s[20:23], m0 offen sc1 lds\n"
"v_mfma_f32_16x16x32_bf16 v[166:169], v[70:73], a[56:59], v[166:169]\n"
"s_add_u32 m0, s59, 20480\n"
"v_add_f32 v230, v230, v202\n"
"v_fmac_f32 v232, v202, v202\n"
"v_add_f32 v231, v231, v203\n"
"v_mfma_f32_16x16x32_bf16 v[170:173], v[70:73], a[60:63], v[170:173]\n"
"s_waitcnt lgkmcnt(4)\n"
"buffer_load_dwordx4 v1, s[20:23], m0 offen sc1 lds\n"
"v_fmac_f32 v233, v203, v203\n"
"v_mfma_f32_16x16x32_bf16 v[174:177], v[74:77], a[56:59], v[174:177]\n"
"s_add_u32 m0, s59, 24576\n"
"v_add_f32 v230, v230, v204\n"
"v_fmac_f32 v232, v204, v204\n"
"v_add_f32 v231, v231, v205\n"
"v_mfma_f32_16x16x32_bf16 v[178:181], v[74:77], a[60:63], v[178:181]\n"
"s_waitcnt lgkmcnt(3)\n"
"buffer_store_dwordx4 v[218:221], v34, s[96:99], 0 offen sc1\n"
"v_mfma_f32_16x16x32_bf16 v[166:169], v[78:81], a[64:67], v[166:169]\n"
"v_permlane16_swap_b32 v226, v228\n"
"v_fmac_f32 v233, v205, v205\n"
"v_mfma_f32_16x16x32_bf16 v[170:173], v[78:81], a[68:71], v[170:173]\n"
"s_waitcnt lgkmcnt(2)\n"
"buffer_load_dwordx4 v1, s[20:23], m0 offen sc1 lds\n"
"v_add_f32 v230, v230, v214\n"
"v_mfma_f32_16x16x32_bf16 v[174:177], v[82:85], a[64:67], v[174:177]\n"
"s_add_u32 m0, s59, 28672\n"
"v_fmac_f32 v232, v214, v214\n"
"v_add_f32 v231, v231, v215\n"
"v_fmac_f32 v233, v215, v215\n"
"v_mfma_f32_16x16x32_bf16 v[178:181], v[82:85], a[68:71], v[178:181]\n"
"s_waitcnt lgkmcnt(1)\n"
"v_permlane16_swap_b32 v227, v229\n"
"v_mfma_f32_16x16x32_bf16 v[166:169], v[54:57], a[72:75], v[166:169]\n"
"buffer_store_dwordx4 v[226:229], v35, s[96:99], 0 offen sc1\n"
"v_add_f32 v230, v230, v216\n"
"v_mfma_f32_16x16x32_bf16 v[170:173], v[54:57], a[76:79], v[170:173]\n"
"s_waitcnt lgkmcnt(0)\n"
"buffer_load_dwordx4 v1, s[20:23], m0 offen sc1 lds\n"
"v_fmac_f32 v232, v216, v216\n"
"v_mfma_f32_16x16x32_bf16 v[174:177], v[58:61], a[72:75], v[174:177]\n"
"v_add_f32 v231, v231, v217\n"
"v_fmac_f32 v233, v217, v217\n"
"v_mfma_f32_16x16x32_bf16 v[178:181], v[58:61], a[76:79], v[178:181]\n"
"s_nop 0\n"
"ds_write_b128 v43, v[170:173] offset:2048\n"
"s_nop 5\n"
"ds_write_b128 v43, v[178:181] offset:3072\n"
"s_waitcnt lgkmcnt(0)\n"
"s_barrier\n"
"ds_read_b128 v[94:97], v44 offset:2048\n"
"ds_read_b128 v[98:101], v44 offset:3072\n"
"s_waitcnt lgkmcnt(1)\n"
"v_pk_add_f32 v[206:207], v[166:167], v[94:95]\n"
"v_pk_add_f32 v[208:209], v[168:169], v[96:97]\n"
"v_cvt_pk_bf16_f32 v222, v206, v207\n"
"v_cvt_pk_bf16_f32 v223, v208, v209\n"
"s_waitcnt lgkmcnt(0)\n"
"v_pk_add_f32 v[210:211], v[174:175], v[98:99]\n"
"v_pk_add_f32 v[212:213], v[176:177], v[100:101]\n"
"v_cvt_pk_bf16_f32 v224, v210, v211\n"
"v_cvt_pk_bf16_f32 v225, v212, v213\n"
"buffer_store_dwordx4 v[222:225], v34, s[96:99], 0 offen offset:1024 sc1\n"
"v_pk_add_f32 v[230:231], v[230:231], v[206:207]\n"
"v_pk_fma_f32 v[232:233], v[206:207], v[206:207], v[232:233]\n"
"v_pk_add_f32 v[230:231], v[230:231], v[208:209]\n"
"v_pk_fma_f32 v[232:233], v[208:209], v[208:209], v[232:233]\n"
"v_pk_add_f32 v[230:231], v[230:231], v[210:211]\n"
"v_pk_fma_f32 v[232:233], v[210:211], v[210:211], v[232:233]\n"
"v_pk_add_f32 v[230:231], v[230:231], v[212:213]\n"
"v_pk_fma_f32 v[232:233], v[212:213], v[212:213], v[232:233]\n"
"v_add_f32 v230, v230, v231\n"
"v_add_f32 v232, v232, v233\n"
"s_nop 1\n"
"v_permlane32_swap_b32 v230, v232\n"
"v_add_f32 v230, v230, v232\n"
"v_mov_b32 v232, v230\n"
"s_nop 1\n"
"v_permlane16_swap_b32 v230, v232\n"
"v_add_f32 v230, v230, v232\n"
"buffer_store_dword v230, v36, s[96:99], 0 offen sc1\n"
"s_add_u32 m0, s59, 32768\n"
"s_nop 0\n"
"buffer_load_dwordx4 v1, s[20:23], m0 offen sc1 lds\n"
"s_add_u32 m0, s59, 36864\n"
"s_nop 0\n"
"buffer_load_dwordx4 v1, s[20:23], m0 offen sc1 lds\n"
"s_add_u32 m0, s59, 40960\n"
"s_nop 0\n"
"buffer_load_dwordx4 v1, s[20:23], m0 offen sc1 lds\n"
"s_mov_b32 s3, 393216\n"
"s_waitcnt vmcnt(3)\n"
"buffer_store_dwordx2 v[8:9], v14, s[20:23], s3 offen sc1\n"
"s_barrier\n"
"s_add_u32 m0, s59, 135168\n"
"v_add_u32 v1, 4294877184, v1\n"
"buffer_load_dwordx4 v1, s[20:23], m0 offen sc1 lds\n"
"s_add_u32 m0, s59, 139264\n"
"s_nop 0\n"
"buffer_load_dwordx4 v1, s[20:23], m0 offen sc1 lds\n"
"s_add_u32 m0, s59, 143360\n"
"s_nop 0\n"
"buffer_load_dwordx4 v1, s[20:23], m0 offen sc1 lds\n"
"s_add_u32 m0, s59, 147456\n"
"s_nop 0\n"
"buffer_load_dwordx4 v1, s[20:23], m0 offen sc1 lds\n"
"s_add_u32 m0, s59, 151552\n"
"s_nop 0\n"
"buffer_load_dwordx4 v1, s[20:23], m0 offen sc1 lds\n"
"s_add_u32 m0, s59, 45056\n"
"v_add_u32 v1, 110592, v1\n"
"buffer_load_dwordx4 v1, s[20:23], m0 offen sc1 lds\n"
"s_add_u32 m0, s59, 49152\n"
"s_nop 0\n"
"buffer_load_dwordx4 v1, s[20:23], m0 offen sc1 lds\n"
"s_add_u32 m0, s59, 53248\n"
"s_nop 0\n"
"buffer_load_dwordx4 v1, s[20:23], m0 offen sc1 lds\n"
"s_barrier\n"
"s_mov_b32 s63, 393216\n"
"s_mov_b32 s72, 0\n"
"L10_%=:\n"
"buffer_load_dwordx2 v[254:255], v12, s[20:23], s63 offen sc1\n"
"s_add_u32 s72, s72, 1\n"
"s_waitcnt vmcnt(0)\n"
"v_cmp_ne_u64 s[18:19], v[254:255], v[8:9]\n"
"s_nop 0\n"
"s_and_b32 s18, s18, 65535\n"
"s_cbranch_scc0 L9_%=\n"
"s_cmp_lt_u32 s72, 4194304\n"
"s_cbranch_scc1 L10_%=\n"
"L9_%=:\n"
"; P4 layer 0\n"
"s_mov_b32 s73, 6422528\n"
"s_mov_b32 s62, 6426624\n"
"buffer_load_dwordx4 a[0:3], v37, s[96:99], 0 offen sc1\n"
"buffer_load_dwordx4 a[4:7], v37, s[96:99], s84 offen sc1\n"
"buffer_load_dwordx4 a[8:11], v37, s[96:99], 0 offen offset:1024 sc1\n"
"buffer_load_dwordx4 a[12:15], v37, s[96:99], s84 offen offset:1024 sc1\n"
"buffer_load_dwordx4 a[16:19], v37, s[96:99], 0 offen offset:2048 sc1\n"
"buffer_load_dwordx4 a[20:23], v37, s[96:99], s84 offen offset:2048 sc1\n"
"buffer_load_dwordx4 a[24:27], v37, s[96:99], 0 offen offset:3072 sc1\n"
"buffer_load_dwordx4 a[28:31], v37, s[96:99], s84 offen offset:3072 sc1\n"
"buffer_load_dwordx4 a[32:35], v37, s[96:99], s80 offen sc1\n"
"buffer_load_dwordx4 a[36:39], v37, s[96:99], s85 offen sc1\n"
"buffer_load_dwordx4 a[40:43], v37, s[96:99], s80 offen offset:1024 sc1\n"
"buffer_load_dwordx4 a[44:47], v37, s[96:99], s85 offen offset:1024 sc1\n"
"ds_read_b128 v[54:57], v3 offset:16384\n"
"ds_read_b128 v[58:61], v3 offset:17408\n"
"ds_read_b128 v[62:65], v3 offset:18432\n"
"ds_read_b128 v[66:69], v3 offset:19456\n"
"ds_read_b128 v[70:73], v3 offset:20480\n"
"ds_read_b128 v[74:77], v3 offset:21504\n"
"s_waitcnt vmcnt(11) lgkmcnt(5)\n"
"v_mfma_f32_16x16x32_bf16 v[86:89], v[54:57], a[0:3], 0\n"
"s_waitcnt vmcnt(10) lgkmcnt(4)\n"
"ds_read_b128 v[78:81], v3 offset:22528\n"
"v_mfma_f32_16x16x32_bf16 v[90:93], v[54:57], a[4:7], 0\n"
"buffer_load_dwordx4 v[218:221], v38, s[96:99], s73 offen sc1\n"
"v_mfma_f32_16x16x32_bf16 v[94:97], v[58:61], a[0:3], 0\n"
"s_waitcnt lgkmcnt(4)\n"
"ds_read_b128 v[82:85], v3 offset:23552\n"
"v_mfma_f32_16x16x32_bf16 v[98:101], v[58:61], a[4:7], 0\n"
"v_mfma_f32_16x16x32_bf16 v[102:105], v[62:65], a[0:3], 0\n"
"s_waitcnt lgkmcnt(4)\n"
"ds_read_b128 v[54:57], v3 offset:24576\n"
"v_mfma_f32_16x16x32_bf16 v[106:109], v[62:65], a[4:7], 0\n"
"buffer_load_dwordx4 v[222:225], v38, s[96:99], s62 offen sc1\n"
"v_mfma_f32_16x16x32_bf16 v[110:113], v[66:69], a[0:3], 0\n"
"s_waitcnt vmcnt(11) lgkmcnt(4)\n"
"ds_read_b128 v[58:61], v3 offset:25600\n"
"v_mfma_f32_16x16x32_bf16 v[114:117], v[66:69], a[4:7], 0\n"
"v_mfma_f32_16x16x32_bf16 v[86:89], v[70:73], a[8:11], v[86:89]\n"
"s_waitcnt vmcnt(10) lgkmcnt(4)\n"
"ds_read_b128 v[62:65], v3 offset:26624\n"
"v_mfma_f32_16x16x32_bf16 v[90:93], v[70:73], a[12:15], v[90:93]\n"
"buffer_load_dwordx4 a[48:51], v37, s[96:99], s80 offen offset:2048 sc1\n"
"v_mfma_f32_16x16x32_bf16 v[94:97], v[74:77], a[8:11], v[94:97]\n"
"s_waitcnt lgkmcnt(4)\n"
"ds_read_b128 v[66:69], v3 offset:27648\n"
"v_mfma_f32_16x16x32_bf16 v[98:101], v[74:77], a[12:15], v[98:101]\n"
"v_mfma_f32_16x16x32_bf16 v[102:105], v[78:81], a[8:11], v[102:105]\n"
"s_waitcnt lgkmcnt(4)\n"
"ds_read_b128 v[70:73], v3 offset:28672\n"
"v_mfma_f32_16x16x32_bf16 v[106:109], v[78:81], a[12:15], v[106:109]\n"
"buffer_load_dwordx4 a[52:55], v37, s[96:99], s85 offen offset:2048 sc1\n"
"v_mfma_f32_16x16x32_bf16 v[110:113], v[82:85], a[8:11], v[110:113]\n"
"s_waitcnt vmcnt(11) lgkmcnt(4)\n"
"ds_read_b128 v[74:77], v3 offset:29696\n"
"v_mfma_f32_16x16x32_bf16 v[114:117], v[82:85], a[12:15], v[114:117]\n"
"v_mfma_f32_16x16x32_bf16 v[86:89], v[54:57], a[16:19], v[86:89]\n"
"s_waitcnt vmcnt(10) lgkmcnt(4)\n"
"ds_read_b128 v[78:81], v3 offset:30720\n"
"v_mfma_f32_16x16x32_bf16 v[90:93], v[54:57], a[20:23], v[90:93]\n"
"buffer_load_dwordx4 a[56:59], v37, s[96:99], s80 offen offset:3072 sc1\n"
"v_mfma_f32_16x16x32_bf16 v[94:97], v[58:61], a[16:19], v[94:97]\n"
"s_waitcnt lgkmcnt(4)\n"
"ds_read_b128 v[82:85], v3 offset:31744\n"
"v_mfma_f32_16x16x32_bf16 v[98:101], v[58:61], a[20:23], v[98:101]\n"
"v_mfma_f32_16x16x32_bf16 v[102:105], v[62:65], a[16:19], v[102:105]\n"
"s_waitcnt lgkmcnt(4)\n"
"ds_read_b128 v[54:57], v3 offset:32768\n"
"v_mfma_f32_16x16x32_bf16 v[106:109], v[62:65], a[20:23], v[106:109]\n"
"buffer_load_dwordx4 a[60:63], v37, s[96:99], s85 offen offset:3072 sc1\n"
"v_mfma_f32_16x16x32_bf16 v[110:113], v[66:69], a[16:19], v[110:113]\n"
"s_waitcnt vmcnt(11) lgkmcnt(4)\n"
"ds_read_b128 v[58:61], v3 offset:33792\n"
"v_mfma_f32_16x16x32_bf16 v[114:117], v[66:69], a[20:23], v[114:117]\n"
"v_mfma_f32_16x16x32_bf16 v[86:89], v[70:73], a[24:27], v[86:89]\n"
"s_waitcnt vmcnt(10) lgkmcnt(4)\n"
"ds_read_b128 v[62:65], v3 offset:34816\n"
"v_mfma_f32_16x16x32_bf16 v[90:93], v[70:73], a[28:31], v[90:93]\n"
"buffer_load_dwordx4 a[64:67], v37, s[96:99], s81 offen sc1\n"
"v_mfma_f32_16x16x32_bf16 v[94:97], v[74:77], a[24:27], v[94:97]\n"
"s_waitcnt lgkmcnt(4)\n"
"ds_read_b128 v[66:69], v3 offset:35840\n"
"v_mfma_f32_16x16x32_bf16 v[98:101], v[74:77], a[28:31], v[98:101]\n"
"v_mfma_f32_16x16x32_bf16 v[102:105], v[78:81], a[24:27], v[102:105]\n"
"s_waitcnt lgkmcnt(4)\n"
"ds_read_b128 v[70:73], v3 offset:36864\n"
"v_mfma_f32_16x16x32_bf16 v[106:109], v[78:81], a[28:31], v[106:109]\n"
"buffer_load_dwordx4 a[68:71], v37, s[96:99], s86 offen sc1\n"
"v_mfma_f32_16x16x32_bf16 v[110:113], v[82:85], a[24:27], v[110:113]\n"
"s_waitcnt vmcnt(11) lgkmcnt(4)\n"
"ds_read_b128 v[74:77], v3 offset:37888\n"
"v_mfma_f32_16x16x32_bf16 v[114:117], v[82:85], a[28:31], v[114:117]\n"
"v_mfma_f32_16x16x32_bf16 v[86:89], v[54:57], a[32:35], v[86:89]\n"
"s_waitcnt vmcnt(10) lgkmcnt(4)\n"
"ds_read_b128 v[78:81], v3 offset:38912\n"
"v_mfma_f32_16x16x32_bf16 v[90:93], v[54:57], a[36:39], v[90:93]\n"
"buffer_load_dwordx4 a[72:75], v37, s[96:99], s81 offen offset:1024 sc1\n"
"v_mfma_f32_16x16x32_bf16 v[94:97], v[58:61], a[32:35], v[94:97]\n"
"s_waitcnt lgkmcnt(4)\n"
"ds_read_b128 v[82:85], v3 offset:39936\n"
"v_mfma_f32_16x16x32_bf16 v[98:101], v[58:61], a[36:39], v[98:101]\n"
"v_mfma_f32_16x16x32_bf16 v[102:105], v[62:65], a[32:35], v[102:105]\n"
"s_waitcnt vmcnt(7) lgkmcnt(4)\n"
"ds_read_b128 v[54:57], v3 offset:40960\n"
"v_add_f32 v218, v218, v222\n"
"v_mfma_f32_16x16x32_bf16 v[106:109], v[62:65], a[36:39], v[106:109]\n"
"buffer_load_dwordx4 a[76:79], v37, s[96:99], s86 offen offset:1024 sc1\n"
"v_add_f32 v219, v219, v223\n"
"v_mfma_f32_16x16x32_bf16 v[110:113], v[66:69], a[32:35], v[110:113]\n"
"s_waitcnt lgkmcnt(4)\n"
"ds_read_b128 v[58:61], v3 offset:41984\n"
"v_add_f32 v220, v220, v224\n"
"v_mfma_f32_16x16x32_bf16 v[114:117], v[66:69], a[36:39], v[114:117]\n"
"v_add_f32 v221, v221, v225\n"
"v_mfma_f32_16x16x32_bf16 v[86:89], v[70:73], a[40:43], v[86:89]\n"
"s_waitcnt lgkmcnt(4)\n"
"ds_read_b128 v[62:65], v3 offset:43008\n"
"ds_read_b128 v[122:125], v50 offset:640\n"
"ds_read_b128 v[130:133], v50 offset:1152\n"
"v_mfma_f32_16x16x32_bf16 v[90:93], v[70:73], a[44:47], v[90:93]\n"
"buffer_load_dwordx4 a[80:83], v37, s[96:99], s81 offen offset:2048 sc1\n"
"v_mfma_f32_16x16x32_bf16 v[94:97], v[74:77], a[40:43], v[94:97]\n"
"s_waitcnt lgkmcnt(6)\n"
"ds_read_b128 v[66:69], v3 offset:44032\n"
"v_permlane32_swap_b32 v218, v220\n"
"v_mfma_f32_16x16x32_bf16 v[98:101], v[74:77], a[44:47], v[98:101]\n"
"v_permlane32_swap_b32 v219, v221\n"
"v_add_f32 v218, v218, v220\n"
"v_mfma_f32_16x16x32_bf16 v[102:105], v[78:81], a[40:43], v[102:105]\n"
"s_waitcnt lgkmcnt(6)\n"
"ds_read_b128 v[70:73], v3 offset:45056\n"
"v_add_f32 v219, v219, v221\n"
"v_mfma_f32_16x16x32_bf16 v[106:109], v[78:81], a[44:47], v[106:109]\n"
"buffer_load_dwordx4 a[84:87], v37, s[96:99], s86 offen offset:2048 sc1\n"
"v_mov_b32 v220, v218\n"
"v_mfma_f32_16x16x32_bf16 v[110:113], v[82:85], a[40:43], v[110:113]\n"
"s_waitcnt vmcnt(9) lgkmcnt(6)\n"
"ds_read_b128 v[74:77], v3 offset:46080\n"
"v_mov_b32 v221, v219\n"
"v_mfma_f32_16x16x32_bf16 v[114:117], v[82:85], a[44:47], v[114:117]\n"
"v_permlane16_swap_b32 v218, v220\n"
"v_mfma_f32_16x16x32_bf16 v[86:89], v[54:57], a[48:51], v[86:89]\n"
"s_waitcnt vmcnt(8) lgkmcnt(6)\n"
"ds_read_b128 v[78:81], v3 offset:47104\n"
"v_add_f32 v218, v218, v220\n"
"v_mul_f32 v218, 986500301, v218\n"
"v_mfma_f32_16x16x32_bf16 v[90:93], v[54:57], a[52:55], v[90:93]\n"
"buffer_load_dwordx4 a[88:91], v37, s[96:99], s81 offen offset:3072 sc1\n"
"v_mul_f32 v226, -1.0, v218\n"
"v_mfma_f32_16x16x32_bf16 v[94:97], v[58:61], a[48:51], v[94:97]\n"
"s_waitcnt lgkmcnt(6)\n"
"ds_read_b128 v[82:85], v3 offset:48128\n"
"v_mul_f32 v227, -1.0, v218\n"
"v_mfma_f32_16x16x32_bf16 v[98:101], v[58:61], a[52:55], v[98:101]\n"
"v_permlane16_swap_b32 v219, v221\n"
"v_mfma_f32_16x16x32_bf16 v[102:105], v[62:65], a[48:51], v[102:105]\n"
"s_waitcnt lgkmcnt(4)\n"
"ds_read_b128 v[54:57], v3 offset:49152\n"
"v_add_f32 v219, v219, v221\n"
"v_mul_f32 v219, 986500301, v219\n"
"v_fma_f32 v219, -v218, v218, v219\n"
"v_mfma_f32_16x16x32_bf16 v[106:109], v[62:65], a[52:55], v[106:109]\n"
"buffer_load_dwordx4 a[92:95], v37, s[96:99], s86 offen offset:3072 sc1\n"
"v_mfma_f32_16x16x32_bf16 v[110:113], v[66:69], a[48:51], v[110:113]\n"
"s_waitcnt vmcnt(9) lgkmcnt(4)\n"
"ds_read_b128 v[58:61], v3 offset:50176\n"
"v_max_f32 v219, 0, v219\n"
"v_add_f32 v219, 925353388, v219\n"
"v_mfma_f32_16x16x32_bf16 v[114:117], v[66:69], a[52:55], v[114:117]\n"
"v_rsq_f32 v220, v219\n"
"v_mfma_f32_16x16x32_bf16 v[86:89], v[70:73], a[56:59], v[86:89]\n"
"s_waitcnt vmcnt(8) lgkmcnt(4)\n"
"ds_read_b128 v[62:65], v3 offset:51200\n"
"v_mul_f32 v228, 1.0, v219\n"
"v_mul_f32 v229, 1.0, v219\n"
"v_mfma_f32_16x16x32_bf16 v[90:93], v[70:73], a[60:63], v[90:93]\n"
"buffer_load_dwordx4 a[96:99], v37, s[96:99], s82 offen sc1\n"
"v_mfma_f32_16x16x32_bf16 v[94:97], v[74:77], a[56:59], v[94:97]\n"
"s_waitcnt lgkmcnt(4)\n"
"ds_read_b128 v[66:69], v3 offset:52224\n"
"v_mul_f32 v230, 3216550459, v220\n"
"v_mul_f32 v231, 3216550459, v220\n"
"v_mfma_f32_16x16x32_bf16 v[98:101], v[74:77], a[60:63], v[98:101]\n"
"v_permlane32_swap_b32 v226, v227\n"
"v_mfma_f32_16x16x32_bf16 v[102:105], v[78:81], a[56:59], v[102:105]\n"
"s_waitcnt lgkmcnt(4)\n"
"ds_read_b128 v[70:73], v3 offset:53248\n"
"v_mul_f32 v118, v226, v122\n"
"v_mul_f32 v119, v226, v123\n"
"v_mfma_f32_16x16x32_bf16 v[106:109], v[78:81], a[60:63], v[106:109]\n"
"v_permlane32_swap_b32 v228, v229\n"
"v_mfma_f32_16x16x32_bf16 v[110:113], v[82:85], a[56:59], v[110:113]\n"
"s_waitcnt vmcnt(8) lgkmcnt(4)\n"
"ds_read_b128 v[74:77], v3 offset:54272\n"
"v_mul_f32 v120, v226, v124\n"
"v_mul_f32 v121, v226, v125\n"
"v_mfma_f32_16x16x32_bf16 v[114:117], v[82:85], a[60:63], v[114:117]\n"
"buffer_load_dwordx4 a[100:103], v37, s[96:99], s87 offen sc1\n"
"v_mfma_f32_16x16x32_bf16 v[86:89], v[54:57], a[64:67], v[86:89]\n"
"s_waitcnt vmcnt(8) lgkmcnt(4)\n"
"ds_read_b128 v[78:81], v3 offset:55296\n"
"v_mul_f32 v122, v227, v122\n"
"v_mul_f32 v123, v227, v123\n"
"v_mfma_f32_16x16x32_bf16 v[90:93], v[54:57], a[68:71], v[90:93]\n"
"v_permlane32_swap_b32 v230, v231\n"
"v_mfma_f32_16x16x32_bf16 v[94:97], v[58:61], a[64:67], v[94:97]\n"
"ds_read_b128 v[82:85], v3 offset:56320\n"
"v_mul_f32 v124, v227, v124\n"
"v_mul_f32 v125, v227, v125\n"
"v_mfma_f32_16x16x32_bf16 v[98:101], v[58:61], a[68:71], v[98:101]\n"
"s_barrier\n"
"s_waitcnt lgkmcnt(5)\n"
"v_mfma_f32_16x16x32_bf16 v[102:105], v[62:65], a[64:67], v[102:105]\n"
"s_waitcnt lgkmcnt(4)\n"
"ds_read_b128 v[54:57], v3 offset:57344\n"
"v_mul_f32 v126, v226, v130\n"
"v_mul_f32 v127, v226, v131\n"
"v_mfma_f32_16x16x32_bf16 v[106:109], v[62:65], a[68:71], v[106:109]\n"
"buffer_load_dwordx4 a[104:107], v37, s[96:99], s82 offen offset:1024 sc1\n"
"v_mul_f32 v128, v226, v132\n"
"v_mfma_f32_16x16x32_bf16 v[110:113], v[66:69], a[64:67], v[110:113]\n"
"s_waitcnt vmcnt(8) lgkmcnt(4)\n"
"ds_read_b128 v[58:61], v3 offset:58368\n"
"v_mul_f32 v129, v226, v133\n"
"v_mfma_f32_16x16x32_bf16 v[114:117], v[66:69], a[68:71], v[114:117]\n"
"buffer_load_dwordx4 a[108:111], v37, s[96:99], s87 offen offset:1024 sc1\n"
"v_mul_f32 v130, v227, v130\n"
"v_mfma_f32_16x16x32_bf16 v[86:89], v[70:73], a[72:75], v[86:89]\n"
"s_waitcnt vmcnt(8) lgkmcnt(4)\n"
"ds_read_b128 v[62:65], v3 offset:59392\n"
"v_mul_f32 v131, v227, v131\n"
"v_mfma_f32_16x16x32_bf16 v[90:93], v[70:73], a[76:79], v[90:93]\n"
"v_mul_f32 v132, v227, v132\n"
"v_mul_f32 v133, v227, v133\n"
"v_mfma_f32_16x16x32_bf16 v[94:97], v[74:77], a[72:75], v[94:97]\n"
"s_waitcnt lgkmcnt(4)\n"
"ds_read_b128 v[66:69], v3 offset:60416\n"
"ds_read_b128 v[138:141], v50 offset:704\n"
"ds_read_b128 v[146:149], v50 offset:1216\n"
"v_mfma_f32_16x16x32_bf16 v[98:101], v[74:77], a[76:79], v[98:101]\n"
"buffer_load_dwordx4 a[112:115], v37, s[96:99], s82 offen offset:2048 sc1\n"
"ds_read_b128 v[154:157], v50 offset:768\n"
"v_mfma_f32_16x16x32_bf16 v[102:105], v[78:81], a[72:75], v[102:105]\n"
"s_waitcnt lgkmcnt(7)\n"
"ds_read_b128 v[70:73], v3 offset:61440\n"
"ds_read_b128 v[162:165], v50 offset:1280\n"
"v_mfma_f32_16x16x32_bf16 v[106:109], v[78:81], a[76:79], v[106:109]\n"
"v_mfma_f32_16x16x32_bf16 v[110:113], v[82:85], a[72:75], v[110:113]\n"
"s_waitcnt vmcnt(8) lgkmcnt(8)\n"
"ds_read_b128 v[74:77], v3 offset:62464\n"
"v_mfma_f32_16x16x32_bf16 v[114:117], v[82:85], a[76:79], v[114:117]\n"
"buffer_load_dwordx4 a[116:119], v37, s[96:99], s87 offen offset:2048 sc1\n"
"v_mfma_f32_16x16x32_bf16 v[86:89], v[54:57], a[80:83], v[86:89]\n"
"s_waitcnt vmcnt(8) lgkmcnt(5)\n"
"ds_read_b128 v[78:81], v3 offset:63488\n"
"v_mul_f32 v134, v226, v138\n"
"v_mul_f32 v135, v226, v139\n"
"v_mul_f32 v136, v226, v140\n"
"v_mfma_f32_16x16x32_bf16 v[90:93], v[54:57], a[84:87], v[90:93]\n"
"v_mul_f32 v137, v226, v141\n"
"v_mul_f32 v138, v227, v138\n"
"v_mul_f32 v139, v227, v139\n"
"v_mfma_f32_16x16x32_bf16 v[94:97], v[58:61], a[80:83], v[94:97]\n"
"ds_read_b128 v[82:85], v3 offset:64512\n"
"v_mul_f32 v140, v227, v140\n"
"v_mfma_f32_16x16x32_bf16 v[98:101], v[58:61], a[84:87], v[98:101]\n"
"buffer_load_dwordx4 a[120:123], v37, s[96:99], s82 offen offset:3072 sc1\n"
"v_mul_f32 v141, v227, v141\n"
"v_mfma_f32_16x16x32_bf16 v[102:105], v[62:65], a[80:83], v[102:105]\n"
"s_waitcnt lgkmcnt(6)\n"
"ds_read_b128 v[54:57], v4\n"
"v_mul_f32 v142, v226, v146\n"
"v_mfma_f32_16x16x32_bf16 v[106:109], v[62:65], a[84:87], v[106:109]\n"
"v_mul_f32 v143, v226, v147\n"
"v_mul_f32 v144, v226, v148\n"
"v_mul_f32 v145, v226, v149\n"
"v_mfma_f32_16x16x32_bf16 v[110:113], v[66:69], a[80:83], v[110:113]\n"
"s_waitcnt vmcnt(8) lgkmcnt(5)\n"
"ds_read_b128 v[58:61], v4 offset:1024\n"
"v_mul_f32 v146, v227, v146\n"
"v_mul_f32 v147, v227, v147\n"
"v_mfma_f32_16x16x32_bf16 v[114:117], v[66:69], a[84:87], v[114:117]\n"
"buffer_load_dwordx4 a[124:127], v37, s[96:99], s87 offen offset:3072 sc1\n"
"v_mfma_f32_16x16x32_bf16 v[86:89], v[70:73], a[88:91], v[86:89]\n"
"s_waitcnt vmcnt(8) lgkmcnt(4)\n"
"ds_read_b128 v[62:65], v4 offset:2048\n"
"v_mul_f32 v148, v227, v148\n"
"v_mul_f32 v149, v227, v149\n"
"v_mfma_f32_16x16x32_bf16 v[90:93], v[70:73], a[92:95], v[90:93]\n"
"v_mul_f32 v150, v226, v154\n"
"v_mul_f32 v151, v226, v155\n"
"v_mfma_f32_16x16x32_bf16 v[94:97], v[74:77], a[88:91], v[94:97]\n"
"s_waitcnt lgkmcnt(4)\n"
"ds_read_b128 v[66:69], v4 offset:3072\n"
"v_mul_f32 v152, v226, v156\n"
"v_mul_f32 v153, v226, v157\n"
"v_mfma_f32_16x16x32_bf16 v[98:101], v[74:77], a[92:95], v[98:101]\n"
"buffer_load_dwordx4 a[128:131], v37, s[96:99], s83 offen sc1\n"
"v_mfma_f32_16x16x32_bf16 v[102:105], v[78:81], a[88:91], v[102:105]\n"
"s_waitcnt lgkmcnt(4)\n"
"ds_read_b128 v[70:73], v2\n"
"v_mul_f32 v154, v227, v154\n"
"v_mul_f32 v155, v227, v155\n"
"v_mfma_f32_16x16x32_bf16 v[106:109], v[78:81], a[92:95], v[106:109]\n"
"v_mul_f32 v156, v227, v156\n"
"v_mul_f32 v157, v227, v157\n"
"v_mfma_f32_16x16x32_bf16 v[110:113], v[82:85], a[88:91], v[110:113]\n"
"s_waitcnt vmcnt(8) lgkmcnt(4)\n"
"ds_read_b128 v[74:77], v2 offset:1024\n"
"v_mul_f32 v158, v226, v162\n"
"v_mul_f32 v159, v226, v163\n"
"v_mfma_f32_16x16x32_bf16 v[114:117], v[82:85], a[92:95], v[114:117]\n"
"buffer_load_dwordx4 a[132:135], v37, s[96:99], s88 offen sc1\n"
"v_mfma_f32_16x16x32_bf16 v[86:89], v[54:57], a[96:99], v[86:89]\n"
"s_waitcnt vmcnt(8) lgkmcnt(4)\n"
"ds_read_b128 v[78:81], v2 offset:2048\n"
"v_mul_f32 v160, v226, v164\n"
"v_mul_f32 v161, v226, v165\n"
"v_mfma_f32_16x16x32_bf16 v[90:93], v[54:57], a[100:103], v[90:93]\n"
"v_mul_f32 v162, v227, v162\n"
"v_mul_f32 v163, v227, v163\n"
"v_mfma_f32_16x16x32_bf16 v[94:97], v[58:61], a[96:99], v[94:97]\n"
"s_waitcnt lgkmcnt(4)\n"
"ds_read_b128 v[82:85], v2 offset:3072\n"
"v_mul_f32 v164, v227, v164\n"
"v_mul_f32 v165, v227, v165\n"
"v_mfma_f32_16x16x32_bf16 v[98:101], v[58:61], a[100:103], v[98:101]\n"
"buffer_load_dwordx4 a[136:139], v37, s[96:99], s83 offen offset:1024 sc1\n"
"ds_read_b128 v[170:173], v50 offset:832\n"
"v_mfma_f32_16x16x32_bf16 v[102:105], v[62:65], a[96:99], v[102:105]\n"
"s_waitcnt lgkmcnt(5)\n"
"ds_read_b128 v[54:57], v2 offset:4096\n"
"ds_read_b128 v[178:181], v50 offset:1344\n"
"v_mfma_f32_16x16x32_bf16 v[106:109], v[62:65], a[100:103], v[106:109]\n"
"ds_read_b128 v[186:189], v50 offset:896\n"
"ds_read_b128 v[194:197], v50 offset:1408\n"
"v_mfma_f32_16x16x32_bf16 v[110:113], v[66:69], a[96:99], v[110:113]\n"
"s_waitcnt vmcnt(8) lgkmcnt(8)\n"
"ds_read_b128 v[58:61], v2 offset:5120\n"
"v_mfma_f32_16x16x32_bf16 v[114:117], v[66:69], a[100:103], v[114:117]\n"
"buffer_load_dwordx4 a[140:143], v37, s[96:99], s88 offen offset:1024 sc1\n"
"v_mfma_f32_16x16x32_bf16 v[86:89], v[70:73], a[104:107], v[86:89]\n"
"s_waitcnt vmcnt(8) lgkmcnt(5)\n"
"ds_read_b128 v[62:65], v2 offset:6144\n"
"v_mfma_f32_16x16x32_bf16 v[90:93], v[70:73], a[108:111], v[90:93]\n"
"v_mul_f32 v166, v226, v170\n"
"v_mul_f32 v167, v226, v171\n"
"v_mul_f32 v168, v226, v172\n"
"v_mul_f32 v169, v226, v173\n"
"v_mul_f32 v170, v227, v170\n"
"v_mfma_f32_16x16x32_bf16 v[94:97], v[74:77], a[104:107], v[94:97]\n"
"ds_read_b128 v[66:69], v2 offset:7168\n"
"v_mul_f32 v171, v227, v171\n"
"v_mfma_f32_16x16x32_bf16 v[98:101], v[74:77], a[108:111], v[98:101]\n"
"buffer_load_dwordx4 a[144:147], v37, s[96:99], s83 offen offset:2048 sc1\n"
"v_mul_f32 v172, v227, v172\n"
"v_mfma_f32_16x16x32_bf16 v[102:105], v[78:81], a[104:107], v[102:105]\n"
"s_waitcnt lgkmcnt(5)\n"
"ds_read_b128 v[70:73], v2 offset:8192\n"
"v_mul_f32 v173, v227, v173\n"
"v_mfma_f32_16x16x32_bf16 v[106:109], v[78:81], a[108:111], v[106:109]\n"
"v_mul_f32 v174, v226, v178\n"
"v_mul_f32 v175, v226, v179\n"
"v_mul_f32 v176, v226, v180\n"
"v_mfma_f32_16x16x32_bf16 v[110:113], v[82:85], a[104:107], v[110:113]\n"
"s_waitcnt vmcnt(8)\n"
"ds_read_b128 v[74:77], v2 offset:9216\n"
"v_mul_f32 v177, v226, v181\n"
"v_mfma_f32_16x16x32_bf16 v[114:117], v[82:85], a[108:111], v[114:117]\n"
"buffer_load_dwordx4 a[148:151], v37, s[96:99], s88 offen offset:2048 sc1\n"
"v_mul_f32 v178, v227, v178\n"
"v_mfma_f32_16x16x32_bf16 v[86:89], v[54:57], a[112:115], v[86:89]\n"
"s_waitcnt vmcnt(8) lgkmcnt(4)\n"
"ds_read_b128 v[78:81], v2 offset:10240\n"
"v_mul_f32 v179, v227, v179\n"
"v_mfma_f32_16x16x32_bf16 v[90:93], v[54:57], a[116:119], v[90:93]\n"
"v_mul_f32 v180, v227, v180\n"
"v_mul_f32 v181, v227, v181\n"
"v_mul_f32 v182, v226, v186\n"
"v_mfma_f32_16x16x32_bf16 v[94:97], v[58:61], a[112:115], v[94:97]\n"
"s_waitcnt lgkmcnt(4)\n"
"ds_read_b128 v[82:85], v2 offset:11264\n"
"v_mul_f32 v183, v226, v187\n"
"v_mfma_f32_16x16x32_bf16 v[98:101], v[58:61], a[116:119], v[98:101]\n"
"buffer_load_dwordx4 a[152:155], v37, s[96:99], s83 offen offset:3072 sc1\n"
"v_mul_f32 v184, v226, v188\n"
"v_mfma_f32_16x16x32_bf16 v[102:105], v[62:65], a[112:115], v[102:105]\n"
"s_waitcnt lgkmcnt(4)\n"
"ds_read_b128 v[54:57], v2 offset:12288\n"
"v_mul_f32 v185, v226, v189\n"
"v_mfma_f32_16x16x32_bf16 v[106:109], v[62:65], a[116:119], v[106:109]\n"
"v_mul_f32 v186, v227, v186\n"
"v_mul_f32 v187, v227, v187\n"
"v_mul_f32 v188, v227, v188\n"
"v_mfma_f32_16x16x32_bf16 v[110:113], v[66:69], a[112:115], v[110:113]\n"
"s_waitcnt vmcnt(8) lgkmcnt(4)\n"
"ds_read_b128 v[58:61], v2 offset:13312\n"
"v_mul_f32 v189, v227, v189\n"
"v_mul_f32 v190, v226, v194\n"
"v_mfma_f32_16x16x32_bf16 v[114:117], v[66:69], a[116:119], v[114:117]\n"
"buffer_load_dwordx4 a[156:159], v37, s[96:99], s88 offen offset:3072 sc1\n"
"v_mfma_f32_16x16x32_bf16 v[86:89], v[70:73], a[120:123], v[86:89]\n"
"s_waitcnt vmcnt(8) lgkmcnt(4)\n"
"ds_read_b128 v[62:65], v2 offset:14336\n"
"v_mul_f32 v191, v226, v195\n"
"v_mul_f32 v192, v226, v196\n"
"v_mfma_f32_16x16x32_bf16 v[90:93], v[70:73], a[124:127], v[90:93]\n"
"v_mul_f32 v193, v226, v197\n"
"v_mul_f32 v194, v227, v194\n"
"v_mfma_f32_16x16x32_bf16 v[94:97], v[74:77], a[120:123], v[94:97]\n"
"s_waitcnt lgkmcnt(4)\n"
"ds_read_b128 v[66:69], v2 offset:15360\n"
"v_mul_f32 v195, v227, v195\n"
"v_mul_f32 v196, v227, v196\n"
"v_mfma_f32_16x16x32_bf16 v[98:101], v[74:77], a[124:127], v[98:101]\n"
"s_add_u32 m0, s59, 57344\n"
"v_mul_f32 v197, v227, v197\n"
"v_mfma_f32_16x16x32_bf16 v[102:105], v[78:81], a[120:123], v[102:105]\n"
"s_waitcnt lgkmcnt(4)\n"
"ds_read_b128 v[70:73], v2 offset:16384\n"
"ds_read_b128 v[232:235], v50 offset:512\n"
"ds_read_b128 v[236:239], v50 offset:1024\n"
"v_mfma_f32_16x16x32_bf16 v[106:109], v[78:81], a[124:127], v[106:109]\n"
"buffer_load_dwordx4 v1, s[20:23], m0 offen sc1 lds\n"
"v_mfma_f32_16x16x32_bf16 v[110:113], v[82:85], a[120:123], v[110:113]\n"
"s_waitcnt vmcnt(8) lgkmcnt(6)\n"
"ds_read_b128 v[74:77], v2 offset:17408\n"
"v_mfma_f32_16x16x32_bf16 v[114:117], v[82:85], a[124:127], v[114:117]\n"
"s_add_u32 m0, s59, 61440\n"
"v_mfma_f32_16x16x32_bf16 v[86:89], v[54:57], a[128:131], v[86:89]\n"
"s_waitcnt vmcnt(7) lgkmcnt(6)\n"
"ds_read_b128 v[78:81], v2 offset:18432\n"
"v_mfma_f32_16x16x32_bf16 v[90:93], v[54:57], a[132:135], v[90:93]\n"
"buffer_load_dwordx4 v1, s[20:23], m0 offen sc1 lds\n"
"v_mfma_f32_16x16x32_bf16 v[94:97], v[58:61], a[128:131], v[94:97]\n"
"s_waitcnt lgkmcnt(6)\n"
"ds_read_b128 v[82:85], v2 offset:19456\n"
"v_mfma_f32_16x16x32_bf16 v[98:101], v[58:61], a[132:135], v[98:101]\n"
"s_add_u32 m0, s59, 65536\n"
"v_mfma_f32_16x16x32_bf16 v[102:105], v[62:65], a[128:131], v[102:105]\n"
"s_waitcnt lgkmcnt(6)\n"
"ds_read_b128 v[54:57], v2 offset:20480\n"
"v_mfma_f32_16x16x32_bf16 v[106:109], v[62:65], a[132:135], v[106:109]\n"
"buffer_load_dwordx4 v1, s[20:23], m0 offen sc1 lds\n"
"v_mfma_f32_16x16x32_bf16 v[110:113], v[66:69], a[128:131], v[110:113]\n"
"s_waitcnt vmcnt(8) lgkmcnt(6)\n"
"ds_read_b128 v[58:61], v2 offset:21504\n"
"v_mfma_f32_16x16x32_bf16 v[114:117], v[66:69], a[132:135], v[114:117]\n"
"s_add_u32 m0, s59, 69632\n"
"v_mfma_f32_16x16x32_bf16 v[86:89], v[70:73], a[136:139], v[86:89]\n"
"s_waitcnt vmcnt(7) lgkmcnt(4)\n"
"ds_read_b128 v[62:65], v2 offset:22528\n"
"v_mfma_f32_16x16x32_bf16 v[90:93], v[70:73], a[140:143], v[90:93]\n"
"buffer_load_dwordx4 v1, s[20:23], m0 offen sc1 lds\n"
"v_mfma_f32_16x16x32_bf16 v[94:97], v[74:77], a[136:139], v[94:97]\n"
"s_waitcnt lgkmcnt(4)\n"
"ds_read_b128 v[66:69], v2 offset:23552\n"
"v_mfma_f32_16x16x32_bf16 v[98:101], v[74:77], a[140:143], v[98:101]\n"
"s_add_u32 m0, s59, 73728\n"
"v_mfma_f32_16x16x32_bf16 v[102:105], v[78:81], a[136:139], v[102:105]\n"
"s_waitcnt lgkmcnt(4)\n"
"ds_read_b128 v[70:73], v2 offset:24576\n"
"v_mfma_f32_16x16x32_bf16 v[106:109], v[78:81], a[140:143], v[106:109]\n"
"buffer_load_dwordx4 v1, s[20:23], m0 offen sc1 lds\n"
"v_mfma_f32_16x16x32_bf16 v[110:113], v[82:85], a[136:139], v[110:113]\n"
"s_waitcnt vmcnt(8) lgkmcnt(4)\n"
"ds_read_b128 v[74:77], v2 offset:25600\n"
"v_mfma_f32_16x16x32_bf16 v[114:117], v[82:85], a[140:143], v[114:117]\n"
"s_add_u32 m0, s59, 77824\n"
"v_mfma_f32_16x16x32_bf16 v[86:89], v[54:57], a[144:147], v[86:89]\n"
"s_waitcnt vmcnt(7) lgkmcnt(4)\n"
"ds_read_b128 v[78:81], v2 offset:26624\n"
"v_mfma_f32_16x16x32_bf16 v[90:93], v[54:57], a[148:151], v[90:93]\n"
"buffer_load_dwordx4 v1, s[20:23], m0 offen sc1 lds\n"
"v_mfma_f32_16x16x32_bf16 v[94:97], v[58:61], a[144:147], v[94:97]\n"
"ds_read_b128 v[82:85], v2 offset:27648\n"
"v_mfma_f32_16x16x32_bf16 v[98:101], v[58:61], a[148:151], v[98:101]\n"
"s_barrier\n"
"s_waitcnt lgkmcnt(5)\n"
"v_mfma_f32_16x16x32_bf16 v[102:105], v[62:65], a[144:147], v[102:105]\n"
"s_waitcnt lgkmcnt(4)\n"
"ds_read_b128 v[54:57], v2 offset:28672\n"
"v_mfma_f32_16x16x32_bf16 v[106:109], v[62:65], a[148:151], v[106:109]\n"
"s_add_u32 m0, s59, 81920\n"
"v_mfma_f32_16x16x32_bf16 v[110:113], v[66:69], a[144:147], v[110:113]\n"
"s_waitcnt vmcnt(7) lgkmcnt(4)\n"
"ds_read_b128 v[58:61], v2 offset:29696\n"
"v_mfma_f32_16x16x32_bf16 v[114:117], v[66:69], a[148:151], v[114:117]\n"
"buffer_load_dwordx4 v1, s[20:23], m0 offen sc1 lds\n"
"v_mfma_f32_16x16x32_bf16 v[86:89], v[70:73], a[152:155], v[86:89]\n"
"s_waitcnt vmcnt(7) lgkmcnt(4)\n"
"ds_read_b128 v[62:65], v2 offset:30720\n"
"v_mfma_f32_16x16x32_bf16 v[90:93], v[70:73], a[156:159], v[90:93]\n"
"s_add_u32 m0, s59, 86016\n"
"v_mfma_f32_16x16x32_bf16 v[94:97], v[74:77], a[152:155], v[94:97]\n"
"s_waitcnt lgkmcnt(4)\n"
"ds_read_b128 v[66:69], v2 offset:31744\n"
"v_mfma_f32_16x16x32_bf16 v[98:101], v[74:77], a[156:159], v[98:101]\n"
"buffer_load_dwordx4 v1, s[20:23], m0 offen sc1 lds\n"
"v_mfma_f32_16x16x32_bf16 v[102:105], v[78:81], a[152:155], v[102:105]\n"
"s_waitcnt lgkmcnt(4)\n"
"ds_read_b128 v[70:73], v2 offset:32768\n"
"v_mfma_f32_16x16x32_bf16 v[106:109], v[78:81], a[156:159], v[106:109]\n"
"s_add_u32 m0, s59, 90112\n"
"v_mfma_f32_16x16x32_bf16 v[110:113], v[82:85], a[152:155], v[110:113]\n"
"s_waitcnt lgkmcnt(4)\n"
"ds_read_b128 v[74:77], v2 offset:33792\n"
"v_mfma_f32_16x16x32_bf16 v[114:117], v[82:85], a[156:159], v[114:117]\n"
"buffer_load_dwordx4 v1, s[20:23], m0 offen sc1 lds\n"
"v_mfma_f32_16x16x32_bf16 v[118:121], v[54:57], a[0:3], v[118:121]\n"
"s_waitcnt lgkmcnt(4)\n"
"ds_read_b128 v[78:81], v2 offset:34816\n"
"v_mfma_f32_16x16x32_bf16 v[122:125], v[54:57], a[4:7], v[122:125]\n"
"s_add_u32 m0, s59, 94208\n"
"v_mfma_f32_16x16x32_bf16 v[126:129], v[58:61], a[0:3], v[126:129]\n"
"s_waitcnt lgkmcnt(4)\n"
"ds_read_b128 v[82:85], v2 offset:35840\n"
"v_mfma_f32_16x16x32_bf16 v[130:133], v[58:61], a[4:7], v[130:133]\n"
"buffer_load_dwordx4 v1, s[20:23], m0 offen sc1 lds\n"
"v_fmac_f32 v86, v226, v232\n"
"v_mul_f32 v218, v230, v86\n"
"v_fmac_f32 v87, v226, v233\n"
"v_mfma_f32_16x16x32_bf16 v[118:121], v[62:65], a[8:11], v[118:121]\n"
"s_waitcnt lgkmcnt(4)\n"
"ds_read_b128 v[54:57], v2 offset:36864\n"
"v_mul_f32 v219, v230, v87\n"
"v_fmac_f32 v94, v226, v236\n"
"v_mfma_f32_16x16x32_bf16 v[122:125], v[62:65], a[12:15], v[122:125]\n"
"s_add_u32 m0, s59, 98304\n"
"v_exp_f32 v218, v218\n"
"v_mul_f32 v86, v86, v94\n"
"v_mfma_f32_16x16x32_bf16 v[126:129], v[66:69], a[8:11], v[126:129]\n"
"s_waitcnt lgkmcnt(4)\n"
"ds_read_b128 v[58:61], v2 offset:37888\n"
"v_fma_f32 v218, v218, v228, v228\n"
"v_fmac_f32 v88, v226, v234\n"
"v_mfma_f32_16x16x32_bf16 v[130:133], v[66:69], a[12:15], v[130:133]\n"
"buffer_load_dwordx4 v1, s[20:23], m0 offen sc1 lds\n"
"v_mul_f32 v220, v230, v88\n"
"v_mfma_f32_16x16x32_bf16 v[118:121], v[70:73], a[16:19], v[118:121]\n"
"s_waitcnt lgkmcnt(4)\n"
"ds_read_b128 v[62:65], v2 offset:38912\n"
"v_fmac_f32 v95, v226, v237\n"
"v_mul_f32 v87, v87, v95\n"
"v_mfma_f32_16x16x32_bf16 v[122:125], v[70:73], a[20:23], v[122:125]\n"
"s_add_u32 m0, s59, 102400\n"
"v_exp_f32 v219, v219\n"
"v_rcp_f32 v218, v218\n"
"v_mfma_f32_16x16x32_bf16 v[126:129], v[74:77], a[16:19], v[126:129]\n"
"s_waitcnt lgkmcnt(4)\n"
"ds_read_b128 v[66:69], v2 offset:39936\n"
"v_fma_f32 v219, v219, v228, v228\n"
"v_mul_f32 v86, v86, v218\n"
"v_mfma_f32_16x16x32_bf16 v[130:133], v[74:77], a[20:23], v[130:133]\n"
"buffer_load_dwordx4 v1, s[20:23], m0 offen sc1 lds\n"
"v_fmac_f32 v89, v226, v235\n"
"v_mfma_f32_16x16x32_bf16 v[118:121], v[78:81], a[24:27], v[118:121]\n"
"s_waitcnt lgkmcnt(4)\n"
"ds_read_b128 v[70:73], v2 offset:40960\n"
"v_mul_f32 v221, v230, v89\n"
"v_fmac_f32 v96, v226, v238\n"
"v_mfma_f32_16x16x32_bf16 v[122:125], v[78:81], a[28:31], v[122:125]\n"
"s_add_u32 m0, s59, 106496\n"
"v_exp_f32 v220, v220\n"
"v_mul_f32 v88, v88, v96\n"
"v_mfma_f32_16x16x32_bf16 v[126:129], v[82:85], a[24:27], v[126:129]\n"
"s_waitcnt lgkmcnt(4)\n"
"ds_read_b128 v[74:77], v2 offset:41984\n"
"v_fma_f32 v220, v220, v228, v228\n"
"v_fmac_f32 v90, v227, v232\n"
"v_mfma_f32_16x16x32_bf16 v[130:133], v[82:85], a[28:31], v[130:133]\n"
"buffer_load_dwordx4 v1, s[20:23], m0 offen sc1 lds\n"
"v_rcp_f32 v219, v219\n"
"v_mfma_f32_16x16x32_bf16 v[118:121], v[54:57], a[32:35], v[118:121]\n"
"s_waitcnt lgkmcnt(4)\n"
"ds_read_b128 v[78:81], v2 offset:43008\n"
"v_mul_f32 v87, v87, v219\n"
"v_cvt_pk_bf16_f32 v94, v86, v87\n"
"v_mfma_f32_16x16x32_bf16 v[122:125], v[54:57], a[36:39], v[122:125]\n"
"s_add_u32 m0, s59, 110592\n"
"v_exp_f32 v221, v221\n"
"v_mul_f32 v222, v231, v90\n"
"v_mfma_f32_16x16x32_bf16 v[126:129], v[58:61], a[32:35], v[126:129]\n"
"s_waitcnt lgkmcnt(4)\n"
"ds_read_b128 v[82:85], v2 offset:44032\n"
"v_fmac_f32 v97, v226, v239\n"
"v_fma_f32 v221, v221, v228, v228\n"
"v_mfma_f32_16x16x32_bf16 v[130:133], v[58:61], a[36:39], v[130:133]\n"
"v_rcp_f32 v220, v220\n"
"v_mul_f32 v89, v89, v97\n"
"v_mfma_f32_16x16x32_bf16 v[118:121], v[62:65], a[40:43], v[118:121]\n"
"s_waitcnt lgkmcnt(4)\n"
"ds_read_b128 v[54:57], v4 offset:4096\n"
"v_mul_f32 v88, v88, v220\n"
"v_fmac_f32 v91, v227, v233\n"
"v_mfma_f32_16x16x32_bf16 v[122:125], v[62:65], a[44:47], v[122:125]\n"
"buffer_load_dwordx4 v1, s[20:23], m0 offen sc1 lds\n"
"v_exp_f32 v222, v222\n"
"v_mfma_f32_16x16x32_bf16 v[126:129], v[66:69], a[40:43], v[126:129]\n"
"s_waitcnt lgkmcnt(4)\n"
"ds_read_b128 v[58:61], v4 offset:5120\n"
"v_mul_f32 v223, v231, v91\n"
"v_fmac_f32 v98, v227, v236\n"
"v_mfma_f32_16x16x32_bf16 v[130:133], v[66:69], a[44:47], v[130:133]\n"
"s_add_u32 m0, s59, 114688\n"
"v_rcp_f32 v221, v221\n"
"v_fma_f32 v222, v222, v229, v229\n"
"v_mfma_f32_16x16x32_bf16 v[118:121], v[70:73], a[48:51], v[118:121]\n"
"s_waitcnt lgkmcnt(4)\n"
"ds_read_b128 v[62:65], v4 offset:6144\n"
"v_mul_f32 v90, v90, v98\n"
"v_mul_f32 v89, v89, v221\n"
"v_mfma_f32_16x16x32_bf16 v[122:125], v[70:73], a[52:55], v[122:125]\n"
"buffer_load_dwordx4 v1, s[20:23], m0 offen sc1 lds\n"
"v_cvt_pk_bf16_f32 v95, v88, v89\n"
"v_mfma_f32_16x16x32_bf16 v[126:129], v[74:77], a[48:51], v[126:129]\n"
"s_waitcnt lgkmcnt(4)\n"
"ds_read_b128 v[66:69], v4 offset:7168\n"
"v_fmac_f32 v92, v227, v234\n"
"v_mul_f32 v224, v231, v92\n"
"v_mfma_f32_16x16x32_bf16 v[130:133], v[74:77], a[52:55], v[130:133]\n"
"s_add_u32 m0, s59, 118784\n"
"v_exp_f32 v223, v223\n"
"v_rcp_f32 v222, v222\n"
"v_mfma_f32_16x16x32_bf16 v[118:121], v[78:81], a[56:59], v[118:121]\n"
"s_waitcnt lgkmcnt(4)\n"
"ds_read_b128 v[70:73], v4 offset:8192\n"
"v_fmac_f32 v99, v227, v237\n"
"v_fma_f32 v223, v223, v229, v229\n"
"v_mfma_f32_16x16x32_bf16 v[122:125], v[78:81], a[60:63], v[122:125]\n"
"buffer_load_dwordx4 v1, s[20:23], m0 offen sc1 lds\n"
"v_mul_f32 v91, v91, v99\n"
"v_mfma_f32_16x16x32_bf16 v[126:129], v[82:85], a[56:59], v[126:129]\n"
"s_waitcnt lgkmcnt(4)\n"
"ds_read_b128 v[74:77], v4 offset:9216\n"
"v_mul_f32 v90, v90, v222\n"
"v_fmac_f32 v93, v227, v235\n"
"v_mfma_f32_16x16x32_bf16 v[130:133], v[82:85], a[60:63], v[130:133]\n"
"s_add_u32 m0, s59, 122880\n"
"v_exp_f32 v224, v224\n"
"v_mul_f32 v225, v231, v93\n"
"v_mfma_f32_16x16x32_bf16 v[118:121], v[54:57], a[64:67], v[118:121]\n"
"s_waitcnt lgkmcnt(4)\n"
"ds_read_b128 v[78:81], v4 offset:10240\n"
"v_fmac_f32 v100, v227, v238\n"
"v_fma_f32 v224, v224, v229, v229\n"
"v_mfma_f32_16x16x32_bf16 v[122:125], v[54:57], a[68:71], v[122:125]\n"
"buffer_load_dwordx4 v1, s[20:23], m0 offen sc1 lds\n"
"v_mul_f32 v92, v92, v100\n"
"v_mfma_f32_16x16x32_bf16 v[126:129], v[58:61], a[64:67], v[126:129]\n"
"s_waitcnt lgkmcnt(4)\n"
"ds_read_b128 v[82:85], v4 offset:11264\n"
"v_fmac_f32 v101, v227, v239\n"
"v_rcp_f32 v223, v223\n"
"v_mfma_f32_16x16x32_bf16 v[130:133], v[58:61], a[68:71], v[130:133]\n"
"s_add_u32 m0, s59, 126976\n"
"v_exp_f32 v225, v225\n"
"v_mul_f32 v91, v91, v223\n"
"v_mfma_f32_16x16x32_bf16 v[118:121], v[62:65], a[72:75], v[118:121]\n"
"s_waitcnt lgkmcnt(4)\n"
"ds_read_b128 v[54:57], v4 offset:12288\n"
"v_cvt_pk_bf16_f32 v96, v90, v91\n"
"v_fma_f32 v225, v225, v229, v229\n"
"v_mfma_f32_16x16x32_bf16 v[122:125], v[62:65], a[76:79], v[122:125]\n"
"v_rcp_f32 v224, v224\n"
"v_mul_f32 v93, v93, v101\n"
"v_mfma_f32_16x16x32_bf16 v[126:129], v[66:69], a[72:75], v[126:129]\n"
"s_waitcnt lgkmcnt(4)\n"
"ds_read_b128 v[58:61], v4 offset:13312\n"
"v_mul_f32 v92, v92, v224\n"
"v_mfma_f32_16x16x32_bf16 v[130:133], v[66:69], a[76:79], v[130:133]\n"
"buffer_load_dwordx4 v1, s[20:23], m0 offen sc1 lds\n"
"v_rcp_f32 v225, v225\n"
"v_mfma_f32_16x16x32_bf16 v[118:121], v[70:73], a[80:83], v[118:121]\n"
"s_waitcnt lgkmcnt(4)\n"
"ds_read_b128 v[62:65], v4 offset:14336\n"
"v_mul_f32 v93, v93, v225\n"
"v_cvt_pk_bf16_f32 v97, v92, v93\n"
"v_mfma_f32_16x16x32_bf16 v[122:125], v[70:73], a[84:87], v[122:125]\n"
"s_add_u32 m0, s59, 131072\n"
"v_permlane16_swap_b32 v94, v96\n"
"v_mfma_f32_16x16x32_bf16 v[126:129], v[74:77], a[80:83], v[126:129]\n"
"s_waitcnt lgkmcnt(4)\n"
"ds_read_b128 v[66:69], v4 offset:15360\n"
"v_permlane16_swap_b32 v95, v97\n"
"v_mfma_f32_16x16x32_bf16 v[130:133], v[74:77], a[84:87], v[130:133]\n"
"buffer_load_dwordx4 v1, s[20:23], m0 offen sc1 lds\n"
"buffer_store_dwordx4 v[94:97], v39, s[96:99], 0 offen sc1\n"
"ds_read_b128 v[240:243], v50 offset:576\n"
"v_mfma_f32_16x16x32_bf16 v[118:121], v[78:81], a[88:91], v[118:121]\n"
"s_waitcnt lgkmcnt(5)\n"
"ds_read_b128 v[70:73], v4 offset:16384\n"
"ds_read_b128 v[244:247], v50 offset:1088\n"
"v_mfma_f32_16x16x32_bf16 v[122:125], v[78:81], a[92:95], v[122:125]\n"
"s_add_u32 m0, s59, 0\n"
"v_add_u32 v1, 135168, v1\n"
"v_mfma_f32_16x16x32_bf16 v[126:129], v[82:85], a[88:91], v[126:129]\n"
"s_waitcnt lgkmcnt(6)\n"
"ds_read_b128 v[74:77], v4 offset:17408\n"
"v_mfma_f32_16x16x32_bf16 v[130:133], v[82:85], a[92:95], v[130:133]\n"
"buffer_load_dwordx4 v1, s[20:23], m0 offen sc1 lds\n"
"v_mfma_f32_16x16x32_bf16 v[118:121], v[54:57], a[96:99], v[118:121]\n"
"s_waitcnt lgkmcnt(3)\n"
"ds_read_b128 v[78:81], v4 offset:18432\n"
"v_mfma_f32_16x16x32_bf16 v[122:125], v[54:57], a[100:103], v[122:125]\n"
"s_add_u32 m0, s59, 4096\n"
"v_fmac_f32 v102, v226, v240\n"
"v_mul_f32 v218, v230, v102\n"
"v_fmac_f32 v103, v226, v241\n"
"v_exp_f32 v218, v218\n"
"v_mfma_f32_16x16x32_bf16 v[126:129], v[58:61], a[96:99], v[126:129]\n"
"s_waitcnt lgkmcnt(2)\n"
"ds_read_b128 v[82:85], v4 offset:19456\n"
"v_mul_f32 v219, v230, v103\n"
"v_fmac_f32 v110, v226, v244\n"
"v_mfma_f32_16x16x32_bf16 v[130:133], v[58:61], a[100:103], v[130:133]\n"
"buffer_load_dwordx4 v1, s[20:23], m0 offen sc1 lds\n"
"v_fma_f32 v218, v218, v228, v228\n"
"v_mfma_f32_16x16x32_bf16 v[118:121], v[62:65], a[104:107], v[118:121]\n"
"ds_read_b128 v[54:57], v4 offset:20480\n"
"v_mul_f32 v102, v102, v110\n"
"v_fmac_f32 v104, v226, v242\n"
"v_mfma_f32_16x16x32_bf16 v[122:125], v[62:65], a[108:111], v[122:125]\n"
"s_add_u32 m0, s59, 8192\n"
"v_exp_f32 v219, v219\n"
"v_mul_f32 v220, v230, v104\n"
"v_fmac_f32 v111, v226, v245\n"
"v_mfma_f32_16x16x32_bf16 v[126:129], v[66:69], a[104:107], v[126:129]\n"
"ds_read_b128 v[58:61], v4 offset:21504\n"
"v_fma_f32 v219, v219, v228, v228\n"
"v_mul_f32 v103, v103, v111\n"
"v_mfma_f32_16x16x32_bf16 v[130:133], v[66:69], a[108:111], v[130:133]\n"
"buffer_load_dwordx4 v1, s[20:23], m0 offen sc1 lds\n"
"v_fmac_f32 v105, v226, v243\n"
"v_mfma_f32_16x16x32_bf16 v[118:121], v[70:73], a[112:115], v[118:121]\n"
"s_waitcnt lgkmcnt(4)\n"
"ds_read_b128 v[62:65], v4 offset:22528\n"
"v_mul_f32 v221, v230, v105\n"
"v_fmac_f32 v112, v226, v246\n"
"v_mfma_f32_16x16x32_bf16 v[122:125], v[70:73], a[116:119], v[122:125]\n"
"s_add_u32 m0, s59, 12288\n"
"v_rcp_f32 v218, v218\n"
"v_exp_f32 v220, v220\n"
"v_mfma_f32_16x16x32_bf16 v[126:129], v[74:77], a[112:115], v[126:129]\n"
"s_waitcnt lgkmcnt(4)\n"
"ds_read_b128 v[66:69], v4 offset:23552\n"
"v_mul_f32 v102, v102, v218\n"
"v_fma_f32 v220, v220, v228, v228\n"
"v_mfma_f32_16x16x32_bf16 v[130:133], v[74:77], a[116:119], v[130:133]\n"
"v_rcp_f32 v219, v219\n"
"v_mul_f32 v104, v104, v112\n"
"v_mfma_f32_16x16x32_bf16 v[118:121], v[78:81], a[120:123], v[118:121]\n"
"s_waitcnt lgkmcnt(4)\n"
"ds_read_b128 v[70:73], v2 offset:45056\n"
"v_mul_f32 v103, v103, v219\n"
"v_cvt_pk_bf16_f32 v110, v102, v103\n"
"v_fmac_f32 v106, v227, v240\n"
"v_mfma_f32_16x16x32_bf16 v[122:125], v[78:81], a[124:127], v[122:125]\n"
"buffer_load_dwordx4 v1, s[20:23], m0 offen sc1 lds\n"
"v_mul_f32 v222, v231, v106\n"
"v_mfma_f32_16x16x32_bf16 v[126:129], v[82:85], a[120:123], v[126:129]\n"
"s_waitcnt lgkmcnt(4)\n"
"ds_read_b128 v[74:77], v2 offset:46080\n"
"v_fmac_f32 v113, v226, v247\n"
"v_mul_f32 v105, v105, v113\n"
"v_mfma_f32_16x16x32_bf16 v[130:133], v[82:85], a[124:127], v[130:133]\n"
"s_add_u32 m0, s59, 16384\n"
"v_exp_f32 v221, v221\n"
"v_fmac_f32 v107, v227, v241\n"
"v_mfma_f32_16x16x32_bf16 v[118:121], v[54:57], a[128:131], v[118:121]\n"
"s_waitcnt lgkmcnt(4)\n"
"ds_read_b128 v[78:81], v2 offset:47104\n"
"v_fma_f32 v221, v221, v228, v228\n"
"v_rcp_f32 v220, v220\n"
"v_mfma_f32_16x16x32_bf16 v[122:125], v[54:57], a[132:135], v[122:125]\n"
"buffer_load_dwordx4 v1, s[20:23], m0 offen sc1 lds\n"
"v_mul_f32 v104, v104, v220\n"
"v_mfma_f32_16x16x32_bf16 v[126:129], v[58:61], a[128:131], v[126:129]\n"
"ds_read_b128 v[82:85], v2 offset:48128\n"
"v_mul_f32 v223, v231, v107\n"
"v_fmac_f32 v114, v227, v244\n"
"v_mfma_f32_16x16x32_bf16 v[130:133], v[58:61], a[132:135], v[130:133]\n"
"s_waitcnt vmcnt(17)\n"
"s_barrier\n"
"v_mul_f32 v106, v106, v114\n"
"s_waitcnt lgkmcnt(5)\n"
"v_mfma_f32_16x16x32_bf16 v[118:121], v[62:65], a[136:139], v[118:121]\n"
"s_waitcnt lgkmcnt(4)\n"
"ds_read_b128 v[54:57], v2 offset:49152\n"
"v_fmac_f32 v108, v227, v242\n"
"v_exp_f32 v222, v222\n"
"v_mfma_f32_16x16x32_bf16 v[122:125], v[62:65], a[140:143], v[122:125]\n"
"s_add_u32 m0, s59, 135168\n"
"v_add_u32 v1, 4294852608, v1\n"
"v_fma_f32 v222, v222, v229, v229\n"
"v_mul_f32 v224, v231, v108\n"
"v_mfma_f32_16x16x32_bf16 v[126:129], v[66:69], a[136:139], v[126:129]\n"
"s_waitcnt lgkmcnt(4)\n"
"ds_read_b128 v[58:61], v2 offset:50176\n"
"v_fmac_f32 v115, v227, v245\n"
"v_mul_f32 v107, v107, v115\n"
"v_mfma_f32_16x16x32_bf16 v[130:133], v[66:69], a[140:143], v[130:133]\n"
"buffer_load_dwordx4 v1, s[20:23], m0 offen sc1 lds\n"
"v_fmac_f32 v109, v227, v243\n"
"v_mfma_f32_16x16x32_bf16 v[118:121], v[70:73], a[144:147], v[118:121]\n"
"s_waitcnt lgkmcnt(4)\n"
"ds_read_b128 v[62:65], v2 offset:51200\n"
"v_mul_f32 v225, v231, v109\n"
"v_fmac_f32 v116, v227, v246\n"
"v_mfma_f32_16x16x32_bf16 v[122:125], v[70:73], a[148:151], v[122:125]\n"
"s_add_u32 m0, s59, 139264\n"
"v_rcp_f32 v221, v221\n"
"v_exp_f32 v223, v223\n"
"v_mfma_f32_16x16x32_bf16 v[126:129], v[74:77], a[144:147], v[126:129]\n"
"s_waitcnt lgkmcnt(4)\n"
"ds_read_b128 v[66:69], v2 offset:52224\n"
"v_mul_f32 v105, v105, v221\n"
"v_cvt_pk_bf16_f32 v111, v104, v105\n"
"v_mfma_f32_16x16x32_bf16 v[130:133], v[74:77], a[148:151], v[130:133]\n"
"buffer_load_dwordx4 v1, s[20:23], m0 offen sc1 lds\n"
"v_fma_f32 v223, v223, v229, v229\n"
"v_mfma_f32_16x16x32_bf16 v[118:121], v[78:81], a[152:155], v[118:121]\n"
"s_waitcnt lgkmcnt(4)\n"
"ds_read_b128 v[70:73], v2 offset:53248\n"
"v_mul_f32 v108, v108, v116\n"
"v_fmac_f32 v117, v227, v247\n"
"v_mfma_f32_16x16x32_bf16 v[122:125], v[78:81], a[156:159], v[122:125]\n"
"s_add_u32 m0, s59, 143360\n"
"v_rcp_f32 v222, v222\n"
"v_exp_f32 v224, v224\n"
"v_mfma_f32_16x16x32_bf16 v[126:129], v[82:85], a[152:155], v[126:129]\n"
"s_waitcnt lgkmcnt(4)\n"
"ds_read_b128 v[74:77], v2 offset:54272\n"
"v_mul_f32 v106, v106, v222\n"
"v_fma_f32 v224, v224, v229, v229\n"
"v_mfma_f32_16x16x32_bf16 v[130:133], v[82:85], a[156:159], v[130:133]\n"
"buffer_load_dwordx4 v1, s[20:23], m0 offen sc1 lds\n"
"v_mul_f32 v109, v109, v117\n"
"v_mfma_f32_16x16x32_bf16 v[134:137], v[54:57], a[0:3], v[134:137]\n"
"s_waitcnt lgkmcnt(4)\n"
"ds_read_b128 v[78:81], v2 offset:55296\n"
"v_rcp_f32 v223, v223\n"
"v_mfma_f32_16x16x32_bf16 v[138:141], v[54:57], a[4:7], v[138:141]\n"
"s_add_u32 m0, s59, 147456\n"
"v_exp_f32 v225, v225\n"
"v_mul_f32 v107, v107, v223\n"
"v_mfma_f32_16x16x32_bf16 v[142:145], v[58:61], a[0:3], v[142:145]\n"
"s_waitcnt lgkmcnt(4)\n"
"ds_read_b128 v[82:85], v2 offset:56320\n"
"v_cvt_pk_bf16_f32 v112, v106, v107\n"
"v_fma_f32 v225, v225, v229, v229\n"
"v_mfma_f32_16x16x32_bf16 v[146:149], v[58:61], a[4:7], v[146:149]\n"
"buffer_load_dwordx4 v1, s[20:23], m0 offen sc1 lds\n"
"v_rcp_f32 v224, v224\n"
"v_mfma_f32_16x16x32_bf16 v[134:137], v[62:65], a[8:11], v[134:137]\n"
"s_waitcnt lgkmcnt(4)\n"
"ds_read_b128 v[54:57], v2 offset:57344\n"
"v_mul_f32 v108, v108, v224\n"
"v_mul_f32 v218, v230, v118\n"
"v_mfma_f32_16x16x32_bf16 v[138:141], v[62:65], a[12:15], v[138:141]\n"
"s_add_u32 m0, s59, 20480\n"
"v_add_u32 v1, 131072, v1\n"
"v_mul_f32 v219, v230, v119\n"
"v_mul_f32 v118, v118, v126\n"
"v_mfma_f32_16x16x32_bf16 v[142:145], v[66:69], a[8:11], v[142:145]\n"
"s_waitcnt lgkmcnt(4)\n"
"ds_read_b128 v[58:61], v2 offset:58368\n"
"v_mul_f32 v220, v230, v120\n"
"v_rcp_f32 v225, v225\n"
"v_mfma_f32_16x16x32_bf16 v[146:149], v[66:69], a[12:15], v[146:149]\n"
"buffer_load_dwordx4 v1, s[20:23], m0 offen sc1 lds\n"
"v_mul_f32 v109, v109, v225\n"
"v_mfma_f32_16x16x32_bf16 v[134:137], v[70:73], a[16:19], v[134:137]\n"
"s_waitcnt lgkmcnt(4)\n"
"ds_read_b128 v[62:65], v2 offset:59392\n"
"v_cvt_pk_bf16_f32 v113, v108, v109\n"
"v_permlane16_swap_b32 v110, v112\n"
"v_mfma_f32_16x16x32_bf16 v[138:141], v[70:73], a[20:23], v[138:141]\n"
"s_add_u32 m0, s59, 24576\n"
"v_exp_f32 v218, v218\n"
"v_exp_f32 v219, v219\n"
"v_mul_f32 v119, v119, v127\n"
"v_mfma_f32_16x16x32_bf16 v[142:145], v[74:77], a[16:19], v[142:145]\n"
"s_waitcnt lgkmcnt(4)\n"
"ds_read_b128 v[66:69], v2 offset:60416\n"
"v_fma_f32 v218, v218, v228, v228\n"
"v_permlane16_swap_b32 v111, v113\n"
"v_fma_f32 v219, v219, v228, v228\n"
"buffer_store_dwordx4 v[110:113], v39, s[96:99], 0 offen offset:512 sc1\n"
"v_mfma_f32_16x16x32_bf16 v[146:149], v[74:77], a[20:23], v[146:149]\n"
"buffer_load_dwordx4 v1, s[20:23], m0 offen sc1 lds\n"
"v_mfma_f32_16x16x32_bf16 v[134:137], v[78:81], a[24:27], v[134:137]\n"
"s_waitcnt lgkmcnt(4)\n"
"ds_read_b128 v[70:73], v2 offset:61440\n"
"v_mul_f32 v221, v230, v121\n"
"v_mfma_f32_16x16x32_bf16 v[138:141], v[78:81], a[28:31], v[138:141]\n"
"s_add_u32 m0, s59, 28672\n"
"v_rcp_f32 v218, v218\n"
"v_mfma_f32_16x16x32_bf16 v[142:145], v[82:85], a[24:27], v[142:145]\n"
"s_waitcnt lgkmcnt(4)\n"
"ds_read_b128 v[74:77], v2 offset:62464\n"
"v_mul_f32 v118, v118, v218\n"
"v_mfma_f32_16x16x32_bf16 v[146:149], v[82:85], a[28:31], v[146:149]\n"
"buffer_load_dwordx4 v1, s[20:23], m0 offen sc1 lds\n"
"v_mfma_f32_16x16x32_bf16 v[134:137], v[54:57], a[32:35], v[134:137]\n"
"s_waitcnt lgkmcnt(4)\n"
"ds_read_b128 v[78:81], v2 offset:63488\n"
"v_mul_f32 v120, v120, v128\n"
"v_mfma_f32_16x16x32_bf16 v[138:141], v[54:57], a[36:39], v[138:141]\n"
"s_add_u32 m0, s59, 32768\n"
"v_exp_f32 v220, v220\n"
"v_mfma_f32_16x16x32_bf16 v[142:145], v[58:61], a[32:35], v[142:145]\n"
"s_waitcnt lgkmcnt(4)\n"
"ds_read_b128 v[82:85], v2 offset:64512\n"
"v_fma_f32 v220, v220, v228, v228\n"
"v_mfma_f32_16x16x32_bf16 v[146:149], v[58:61], a[36:39], v[146:149]\n"
"v_rcp_f32 v219, v219\n"
"v_mfma_f32_16x16x32_bf16 v[134:137], v[62:65], a[40:43], v[134:137]\n"
"s_waitcnt lgkmcnt(4)\n"
"ds_read_b128 v[54:57], v3\n"
"v_mul_f32 v119, v119, v219\n"
"v_mfma_f32_16x16x32_bf16 v[138:141], v[62:65], a[44:47], v[138:141]\n"
"buffer_load_dwordx4 v1, s[20:23], m0 offen sc1 lds\n"
"v_cvt_pk_bf16_f32 v126, v118, v119\n"
"v_mfma_f32_16x16x32_bf16 v[142:145], v[66:69], a[40:43], v[142:145]\n"
"s_waitcnt lgkmcnt(4)\n"
"ds_read_b128 v[58:61], v3 offset:1024\n"
"v_mul_f32 v222, v231, v122\n"
"v_mfma_f32_16x16x32_bf16 v[146:149], v[66:69], a[44:47], v[146:149]\n"
"s_add_u32 m0, s59, 36864\n"
"v_exp_f32 v221, v221\n"
"v_mfma_f32_16x16x32_bf16 v[134:137], v[70:73], a[48:51], v[134:137]\n"
"s_waitcnt lgkmcnt(4)\n"
"ds_read_b128 v[62:65], v3 offset:2048\n"
"v_fma_f32 v221, v221, v228, v228\n"
"v_mfma_f32_16x16x32_bf16 v[138:141], v[70:73], a[52:55], v[138:141]\n"
"buffer_load_dwordx4 v1, s[20:23], m0 offen sc1 lds\n"
"v_mfma_f32_16x16x32_bf16 v[142:145], v[74:77], a[48:51], v[142:145]\n"
"s_waitcnt lgkmcnt(4)\n"
"ds_read_b128 v[66:69], v3 offset:3072\n"
"v_mul_f32 v121, v121, v129\n"
"v_mfma_f32_16x16x32_bf16 v[146:149], v[74:77], a[52:55], v[146:149]\n"
"s_add_u32 m0, s59, 40960\n"
"v_rcp_f32 v220, v220\n"
"v_mfma_f32_16x16x32_bf16 v[134:137], v[78:81], a[56:59], v[134:137]\n"
"s_waitcnt lgkmcnt(4)\n"
"ds_read_b128 v[70:73], v3 offset:4096\n"
"v_mul_f32 v120, v120, v220\n"
"v_mfma_f32_16x16x32_bf16 v[138:141], v[78:81], a[60:63], v[138:141]\n"
"buffer_load_dwordx4 v1, s[20:23], m0 offen sc1 lds\n"
"v_mfma_f32_16x16x32_bf16 v[142:145], v[82:85], a[56:59], v[142:145]\n"
"s_waitcnt lgkmcnt(4)\n"
"ds_read_b128 v[74:77], v3 offset:5120\n"
"v_mul_f32 v223, v231, v123\n"
"v_mfma_f32_16x16x32_bf16 v[146:149], v[82:85], a[60:63], v[146:149]\n"
"s_add_u32 s62, s67, 90112\n"
"v_exp_f32 v222, v222\n"
"v_mfma_f32_16x16x32_bf16 v[134:137], v[54:57], a[64:67], v[134:137]\n"
"s_waitcnt lgkmcnt(4)\n"
"ds_read_b128 v[78:81], v3 offset:6144\n"
"v_fma_f32 v222, v222, v229, v229\n"
"v_mul_f32 v122, v122, v130\n"
"v_mfma_f32_16x16x32_bf16 v[138:141], v[54:57], a[68:71], v[138:141]\n"
"buffer_load_dwordx4 a[160:163], v5, s[20:23], s62 offen sc1\n"
"v_mfma_f32_16x16x32_bf16 v[142:145], v[58:61], a[64:67], v[142:145]\n"
"s_waitcnt lgkmcnt(4)\n"
"ds_read_b128 v[82:85], v3 offset:7168\n"
"v_mul_f32 v224, v231, v124\n"
"v_mfma_f32_16x16x32_bf16 v[146:149], v[58:61], a[68:71], v[146:149]\n"
"v_rcp_f32 v221, v221\n"
"v_mfma_f32_16x16x32_bf16 v[134:137], v[62:65], a[72:75], v[134:137]\n"
"s_waitcnt lgkmcnt(4)\n"
"ds_read_b128 v[54:57], v3 offset:8192\n"
"v_mul_f32 v121, v121, v221\n"
"v_mfma_f32_16x16x32_bf16 v[138:141], v[62:65], a[76:79], v[138:141]\n"
"v_exp_f32 v223, v223\n"
"v_mfma_f32_16x16x32_bf16 v[142:145], v[66:69], a[72:75], v[142:145]\n"
"s_waitcnt lgkmcnt(4)\n"
"ds_read_b128 v[58:61], v3 offset:9216\n"
"v_cvt_pk_bf16_f32 v127, v120, v121\n"
"v_mfma_f32_16x16x32_bf16 v[146:149], v[66:69], a[76:79], v[146:149]\n"
"buffer_load_dwordx4 a[164:167], v5, s[20:23], s62 offen offset:2048 sc1\n"
"v_mfma_f32_16x16x32_bf16 v[134:137], v[70:73], a[80:83], v[134:137]\n"
"s_waitcnt lgkmcnt(4)\n"
"ds_read_b128 v[62:65], v3 offset:10240\n"
"v_fma_f32 v223, v223, v229, v229\n"
"v_mfma_f32_16x16x32_bf16 v[138:141], v[70:73], a[84:87], v[138:141]\n"
"s_add_u32 s62, s67, 94208\n"
"v_rcp_f32 v222, v222\n"
"v_mfma_f32_16x16x32_bf16 v[142:145], v[74:77], a[80:83], v[142:145]\n"
"s_waitcnt lgkmcnt(4)\n"
"ds_read_b128 v[66:69], v3 offset:11264\n"
"v_mul_f32 v123, v123, v131\n"
"v_mfma_f32_16x16x32_bf16 v[146:149], v[74:77], a[84:87], v[146:149]\n"
"buffer_load_dwordx4 a[168:171], v5, s[20:23], s62 offen sc1\n"
"v_mfma_f32_16x16x32_bf16 v[134:137], v[78:81], a[88:91], v[134:137]\n"
"s_waitcnt lgkmcnt(4)\n"
"ds_read_b128 v[70:73], v3 offset:12288\n"
"v_mul_f32 v122, v122, v222\n"
"v_mul_f32 v225, v231, v125\n"
"v_mfma_f32_16x16x32_bf16 v[138:141], v[78:81], a[92:95], v[138:141]\n"
"v_exp_f32 v224, v224\n"
"v_mfma_f32_16x16x32_bf16 v[142:145], v[82:85], a[88:91], v[142:145]\n"
"s_waitcnt lgkmcnt(4)\n"
"ds_read_b128 v[74:77], v3 offset:13312\n"
"v_fma_f32 v224, v224, v229, v229\n"
"v_mfma_f32_16x16x32_bf16 v[146:149], v[82:85], a[92:95], v[146:149]\n"
"buffer_load_dwordx4 a[172:175], v5, s[20:23], s62 offen offset:2048 sc1\n"
"v_mfma_f32_16x16x32_bf16 v[134:137], v[54:57], a[96:99], v[134:137]\n"
"ds_read_b128 v[78:81], v3 offset:14336\n"
"s_mov_b32 s3, 524288\n"
"s_waitcnt vmcnt(9)\n"
"v_mfma_f32_16x16x32_bf16 v[138:141], v[54:57], a[100:103], v[138:141]\n"
"s_waitcnt lgkmcnt(5)\n"
"s_add_u32 s62, s67, 98304\n"
"v_rcp_f32 v223, v223\n"
"v_mfma_f32_16x16x32_bf16 v[142:145], v[58:61], a[96:99], v[142:145]\n"
"s_waitcnt lgkmcnt(4)\n"
"ds_read_b128 v[82:85], v3 offset:15360\n"
"v_mul_f32 v124, v124, v132\n"
"v_mfma_f32_16x16x32_bf16 v[146:149], v[58:61], a[100:103], v[146:149]\n"
"buffer_store_dwordx2 v[8:9], v14, s[20:23], s3 offen sc1\n"
"v_mfma_f32_16x16x32_bf16 v[134:137], v[62:65], a[104:107], v[134:137]\n"
"s_waitcnt lgkmcnt(4)\n"
"ds_read_b128 v[54:57], v3 offset:16384\n"
"v_mul_f32 v123, v123, v223\n"
"v_mfma_f32_16x16x32_bf16 v[138:141], v[62:65], a[108:111], v[138:141]\n"
"buffer_load_dwordx4 a[176:179], v5, s[20:23], s62 offen sc1\n"
"v_mfma_f32_16x16x32_bf16 v[142:145], v[66:69], a[104:107], v[142:145]\n"
"s_waitcnt lgkmcnt(4)\n"
"ds_read_b128 v[58:61], v3 offset:17408\n"
"v_cvt_pk_bf16_f32 v128, v122, v123\n"
"v_mfma_f32_16x16x32_bf16 v[146:149], v[66:69], a[108:111], v[146:149]\n"
"buffer_load_dwordx4 a[180:183], v5, s[20:23], s62 offen offset:2048 sc1\n"
"v_mul_f32 v125, v125, v133\n"
"v_mfma_f32_16x16x32_bf16 v[134:137], v[70:73], a[112:115], v[134:137]\n"
"s_waitcnt lgkmcnt(4)\n"
"ds_read_b128 v[62:65], v3 offset:18432\n"
"v_mfma_f32_16x16x32_bf16 v[138:141], v[70:73], a[116:119], v[138:141]\n"
"s_add_u32 s62, s67, 102400\n"
"v_exp_f32 v225, v225\n"
"v_mfma_f32_16x16x32_bf16 v[142:145], v[74:77], a[112:115], v[142:145]\n"
"s_waitcnt lgkmcnt(4)\n"
"ds_read_b128 v[66:69], v3 offset:19456\n"
"v_fma_f32 v225, v225, v229, v229\n"
"v_mfma_f32_16x16x32_bf16 v[146:149], v[74:77], a[116:119], v[146:149]\n"
"v_rcp_f32 v224, v224\n"
"v_mfma_f32_16x16x32_bf16 v[134:137], v[78:81], a[120:123], v[134:137]\n"
"s_waitcnt lgkmcnt(4)\n"
"ds_read_b128 v[70:73], v3 offset:20480\n"
"v_mul_f32 v124, v124, v224\n"
"v_mfma_f32_16x16x32_bf16 v[138:141], v[78:81], a[124:127], v[138:141]\n"
"buffer_load_dwordx4 a[184:187], v5, s[20:23], s62 offen sc1\n"
"v_mfma_f32_16x16x32_bf16 v[142:145], v[82:85], a[120:123], v[142:145]\n"
"s_waitcnt lgkmcnt(4)\n"
"ds_read_b128 v[74:77], v3 offset:21504\n"
"v_mfma_f32_16x16x32_bf16 v[146:149], v[82:85], a[124:127], v[146:149]\n"
"v_rcp_f32 v225, v225\n"
"v_mfma_f32_16x16x32_bf16 v[134:137], v[54:57], a[128:131], v[134:137]\n"
"s_waitcnt lgkmcnt(4)\n"
"ds_read_b128 v[78:81], v3 offset:22528\n"
"v_mul_f32 v125, v125, v225\n"
"v_mfma_f32_16x16x32_bf16 v[138:141], v[54:57], a[132:135], v[138:141]\n"
"buffer_load_dwordx4 a[188:191], v5, s[20:23], s62 offen offset:2048 sc1\n"
"v_mfma_f32_16x16x32_bf16 v[142:145], v[58:61], a[128:131], v[142:145]\n"
"ds_read_b128 v[82:85], v3 offset:23552\n"
"v_cvt_pk_bf16_f32 v129, v124, v125\n"
"v_mfma_f32_16x16x32_bf16 v[146:149], v[58:61], a[132:135], v[146:149]\n"
"s_barrier\n"
"s_waitcnt lgkmcnt(5)\n"
"v_mfma_f32_16x16x32_bf16 v[134:137], v[62:65], a[136:139], v[134:137]\n"
"s_waitcnt lgkmcnt(4)\n"
"ds_read_b128 v[54:57], v3 offset:24576\n"
"v_permlane16_swap_b32 v126, v128\n"
"v_mfma_f32_16x16x32_bf16 v[138:141], v[62:65], a[140:143], v[138:141]\n"
"s_add_u32 m0, s59, 151552\n"
"v_add_u32 v1, 4294860800, v1\n"
"v_mfma_f32_16x16x32_bf16 v[142:145], v[66:69], a[136:139], v[142:145]\n"
"s_waitcnt lgkmcnt(4)\n"
"ds_read_b128 v[58:61], v3 offset:25600\n"
"v_mfma_f32_16x16x32_bf16 v[146:149], v[66:69], a[140:143], v[146:149]\n"
"buffer_load_dwordx4 v1, s[20:23], m0 offen sc1 lds\n"
"v_mfma_f32_16x16x32_bf16 v[134:137], v[70:73], a[144:147], v[134:137]\n"
"s_waitcnt lgkmcnt(4)\n"
"ds_read_b128 v[62:65], v3 offset:26624\n"
"v_mfma_f32_16x16x32_bf16 v[138:141], v[70:73], a[148:151], v[138:141]\n"
"s_add_u32 m0, s59, 45056\n"
"v_add_u32 v1, 110592, v1\n"
"v_mfma_f32_16x16x32_bf16 v[142:145], v[74:77], a[144:147], v[142:145]\n"
"s_waitcnt lgkmcnt(4)\n"
"ds_read_b128 v[66:69], v3 offset:27648\n"
"v_mfma_f32_16x16x32_bf16 v[146:149], v[74:77], a[148:151], v[146:149]\n"
"buffer_load_dwordx4 v1, s[20:23], m0 offen sc1 lds\n"
"v_mfma_f32_16x16x32_bf16 v[134:137], v[78:81], a[152:155], v[134:137]\n"
"s_waitcnt lgkmcnt(4)\n"
"ds_read_b128 v[70:73], v3 offset:28672\n"
"v_permlane16_swap_b32 v127, v129\n"
"v_mfma_f32_16x16x32_bf16 v[138:141], v[78:81], a[156:159], v[138:141]\n"
"s_add_u32 m0, s59, 49152\n"
"v_mfma_f32_16x16x32_bf16 v[142:145], v[82:85], a[152:155], v[142:145]\n"
"s_waitcnt lgkmcnt(4)\n"
"ds_read_b128 v[74:77], v3 offset:29696\n"
"v_mfma_f32_16x16x32_bf16 v[146:149], v[82:85], a[156:159], v[146:149]\n"
"buffer_load_dwordx4 v1, s[20:23], m0 offen sc1 lds\n"
"v_mfma_f32_16x16x32_bf16 v[150:153], v[54:57], a[0:3], v[150:153]\n"
"s_waitcnt lgkmcnt(4)\n"
"ds_read_b128 v[78:81], v3 offset:30720\n"
"v_mfma_f32_16x16x32_bf16 v[154:157], v[54:57], a[4:7], v[154:157]\n"
"s_add_u32 m0, s59, 53248\n"
"v_mfma_f32_16x16x32_bf16 v[158:161], v[58:61], a[0:3], v[158:161]\n"
"s_waitcnt lgkmcnt(4)\n"
"ds_read_b128 v[82:85], v3 offset:31744\n"
"v_mfma_f32_16x16x32_bf16 v[162:165], v[58:61], a[4:7], v[162:165]\n"
"buffer_load_dwordx4 v1, s[20:23], m0 offen sc1 lds\n"
"v_mul_f32 v218, v230, v134\n"
"v_mfma_f32_16x16x32_bf16 v[150:153], v[62:65], a[8:11], v[150:153]\n"
"s_waitcnt lgkmcnt(4)\n"
"ds_read_b128 v[54:57], v3 offset:32768\n"
"v_mul_f32 v219, v230, v135\n"
"v_mfma_f32_16x16x32_bf16 v[154:157], v[62:65], a[12:15], v[154:157]\n"
"s_add_u32 m0, s59, 57344\n"
"s_add_u32 s60, s67, 4294909952\n"
"v_add_u32 v1, s60, v2\n"
"v_mul_f32 v134, v134, v142\n"
"v_mfma_f32_16x16x32_bf16 v[158:161], v[66:69], a[8:11], v[158:161]\n"
"s_waitcnt lgkmcnt(4)\n"
"ds_read_b128 v[58:61], v3 offset:33792\n"
"v_mul_f32 v220, v230, v136\n"
"v_mfma_f32_16x16x32_bf16 v[162:165], v[66:69], a[12:15], v[162:165]\n"
"buffer_store_dwordx4 v[126:129], v39, s[96:99], s83 offen sc1\n"
"v_mfma_f32_16x16x32_bf16 v[150:153], v[70:73], a[16:19], v[150:153]\n"
"s_waitcnt lgkmcnt(4)\n"
"ds_read_b128 v[62:65], v3 offset:34816\n"
"v_mul_f32 v135, v135, v143\n"
"v_mul_f32 v221, v230, v137\n"
"v_mfma_f32_16x16x32_bf16 v[154:157], v[70:73], a[20:23], v[154:157]\n"
"buffer_load_dwordx4 v1, s[20:23], m0 offen sc1 lds\n"
"v_mfma_f32_16x16x32_bf16 v[158:161], v[74:77], a[16:19], v[158:161]\n"
"s_waitcnt lgkmcnt(4)\n"
"ds_read_b128 v[66:69], v3 offset:35840\n"
"v_mul_f32 v136, v136, v144\n"
"v_mfma_f32_16x16x32_bf16 v[162:165], v[74:77], a[20:23], v[162:165]\n"
"s_add_u32 m0, s59, 61440\n"
"v_exp_f32 v218, v218\n"
"v_mfma_f32_16x16x32_bf16 v[150:153], v[78:81], a[24:27], v[150:153]\n"
"s_waitcnt lgkmcnt(4)\n"
"ds_read_b128 v[70:73], v3 offset:36864\n"
"v_fma_f32 v218, v218, v228, v228\n"
"v_mfma_f32_16x16x32_bf16 v[154:157], v[78:81], a[28:31], v[154:157]\n"
"buffer_load_dwordx4 v1, s[20:23], m0 offen sc1 lds\n"
"v_mul_f32 v222, v231, v138\n"
"v_mfma_f32_16x16x32_bf16 v[158:161], v[82:85], a[24:27], v[158:161]\n"
"s_waitcnt lgkmcnt(4)\n"
"ds_read_b128 v[74:77], v3 offset:37888\n"
"v_mul_f32 v137, v137, v145\n"
"v_mfma_f32_16x16x32_bf16 v[162:165], v[82:85], a[28:31], v[162:165]\n"
"s_add_u32 m0, s59, 65536\n"
"v_exp_f32 v219, v219\n"
"v_mfma_f32_16x16x32_bf16 v[150:153], v[54:57], a[32:35], v[150:153]\n"
"s_waitcnt lgkmcnt(4)\n"
"ds_read_b128 v[78:81], v3 offset:38912\n"
"v_fma_f32 v219, v219, v228, v228\n"
"v_mfma_f32_16x16x32_bf16 v[154:157], v[54:57], a[36:39], v[154:157]\n"
"v_rcp_f32 v218, v218\n"
"v_mfma_f32_16x16x32_bf16 v[158:161], v[58:61], a[32:35], v[158:161]\n"
"s_waitcnt lgkmcnt(4)\n"
"ds_read_b128 v[82:85], v3 offset:39936\n"
"buffer_load_dwordx4 v1, s[20:23], m0 offen sc1 lds\n"
"v_mul_f32 v134, v134, v218\n"
"v_mfma_f32_16x16x32_bf16 v[162:165], v[58:61], a[36:39], v[162:165]\n"
"s_mov_b32 s63, 524288\n"
"v_exp_f32 v220, v220\n"
"v_mfma_f32_16x16x32_bf16 v[150:153], v[62:65], a[40:43], v[150:153]\n"
"s_waitcnt lgkmcnt(4)\n"
"ds_read_b128 v[54:57], v3 offset:40960\n"
"v_fma_f32 v220, v220, v228, v228\n"
"v_mfma_f32_16x16x32_bf16 v[154:157], v[62:65], a[44:47], v[154:157]\n"
"buffer_load_dwordx2 v[254:255], v11, s[20:23], s63 offen sc1\n"
"v_mfma_f32_16x16x32_bf16 v[158:161], v[66:69], a[40:43], v[158:161]\n"
"s_waitcnt lgkmcnt(4)\n"
"ds_read_b128 v[58:61], v3 offset:41984\n"
"v_mul_f32 v223, v231, v139\n"
"v_mfma_f32_16x16x32_bf16 v[162:165], v[66:69], a[44:47], v[162:165]\n"
"s_add_u32 m0, s59, 69632\n"
"v_rcp_f32 v219, v219\n"
"v_mfma_f32_16x16x32_bf16 v[150:153], v[70:73], a[48:51], v[150:153]\n"
"s_waitcnt lgkmcnt(4)\n"
"ds_read_b128 v[62:65], v3 offset:43008\n"
"v_mul_f32 v135, v135, v219\n"
"v_cvt_pk_bf16_f32 v142, v134, v135\n"
"v_mfma_f32_16x16x32_bf16 v[154:157], v[70:73], a[52:55], v[154:157]\n"
"buffer_load_dwordx4 v1, s[20:23], m0 offen sc1 lds\n"
"v_mfma_f32_16x16x32_bf16 v[158:161], v[74:77], a[48:51], v[158:161]\n"
"s_waitcnt lgkmcnt(4)\n"
"ds_read_b128 v[66:69], v3 offset:44032\n"
"v_mul_f32 v138, v138, v146\n"
"v_mfma_f32_16x16x32_bf16 v[162:165], v[74:77], a[52:55], v[162:165]\n"
"s_add_u32 m0, s59, 73728\n"
"v_exp_f32 v221, v221\n"
"v_mfma_f32_16x16x32_bf16 v[150:153], v[78:81], a[56:59], v[150:153]\n"
"s_waitcnt lgkmcnt(4)\n"
"ds_read_b128 v[70:73], v3 offset:45056\n"
"v_fma_f32 v221, v221, v228, v228\n"
"v_mfma_f32_16x16x32_bf16 v[154:157], v[78:81], a[60:63], v[154:157]\n"
"buffer_load_dwordx4 v1, s[20:23], m0 offen sc1 lds\n"
"v_mul_f32 v224, v231, v140\n"
"v_mfma_f32_16x16x32_bf16 v[158:161], v[82:85], a[56:59], v[158:161]\n"
"s_waitcnt lgkmcnt(4)\n"
"ds_read_b128 v[74:77], v3 offset:46080\n"
"v_mul_f32 v139, v139, v147\n"
"v_mfma_f32_16x16x32_bf16 v[162:165], v[82:85], a[60:63], v[162:165]\n"
"s_add_u32 m0, s59, 77824\n"
"v_rcp_f32 v220, v220\n"
"v_mfma_f32_16x16x32_bf16 v[150:153], v[54:57], a[64:67], v[150:153]\n"
"s_waitcnt lgkmcnt(4)\n"
"ds_read_b128 v[78:81], v3 offset:47104\n"
"v_mul_f32 v136, v136, v220\n"
"v_mfma_f32_16x16x32_bf16 v[154:157], v[54:57], a[68:71], v[154:157]\n"
"buffer_load_dwordx4 v1, s[20:23], m0 offen sc1 lds\n"
"v_mfma_f32_16x16x32_bf16 v[158:161], v[58:61], a[64:67], v[158:161]\n"
"ds_read_b128 v[82:85], v3 offset:48128\n"
"v_mul_f32 v225, v231, v141\n"
"v_mfma_f32_16x16x32_bf16 v[162:165], v[58:61], a[68:71], v[162:165]\n"
"s_barrier\n"
"v_mul_f32 v140, v140, v148\n"
"s_waitcnt lgkmcnt(5)\n"
"v_mfma_f32_16x16x32_bf16 v[150:153], v[62:65], a[72:75], v[150:153]\n"
"s_waitcnt lgkmcnt(4)\n"
"ds_read_b128 v[54:57], v3 offset:49152\n"
"v_mul_f32 v141, v141, v149\n"
"v_mfma_f32_16x16x32_bf16 v[154:157], v[62:65], a[76:79], v[154:157]\n"
"s_add_u32 m0, s59, 81920\n"
"v_exp_f32 v222, v222\n"
"v_mfma_f32_16x16x32_bf16 v[158:161], v[66:69], a[72:75], v[158:161]\n"
"s_waitcnt lgkmcnt(4)\n"
"ds_read_b128 v[58:61], v3 offset:50176\n"
"v_fma_f32 v222, v222, v229, v229\n"
"v_mfma_f32_16x16x32_bf16 v[162:165], v[66:69], a[76:79], v[162:165]\n"
"buffer_load_dwordx4 v1, s[20:23], m0 offen sc1 lds\n"
"v_mfma_f32_16x16x32_bf16 v[150:153], v[70:73], a[80:83], v[150:153]\n"
"s_waitcnt lgkmcnt(4)\n"
"ds_read_b128 v[62:65], v3 offset:51200\n"
"v_mfma_f32_16x16x32_bf16 v[154:157], v[70:73], a[84:87], v[154:157]\n"
"s_add_u32 m0, s59, 86016\n"
"v_rcp_f32 v221, v221\n"
"v_mfma_f32_16x16x32_bf16 v[158:161], v[74:77], a[80:83], v[158:161]\n"
"s_waitcnt lgkmcnt(4)\n"
"ds_read_b128 v[66:69], v3 offset:52224\n"
"v_mul_f32 v137, v137, v221\n"
"v_cvt_pk_bf16_f32 v143, v136, v137\n"
"v_mfma_f32_16x16x32_bf16 v[162:165], v[74:77], a[84:87], v[162:165]\n"
"buffer_load_dwordx4 v1, s[20:23], m0 offen sc1 lds\n"
"v_mfma_f32_16x16x32_bf16 v[150:153], v[78:81], a[88:91], v[150:153]\n"
"s_waitcnt lgkmcnt(4)\n"
"ds_read_b128 v[70:73], v3 offset:53248\n"
"v_mfma_f32_16x16x32_bf16 v[154:157], v[78:81], a[92:95], v[154:157]\n"
"s_add_u32 m0, s59, 90112\n"
"v_exp_f32 v223, v223\n"
"v_mfma_f32_16x16x32_bf16 v[158:161], v[82:85], a[88:91], v[158:161]\n"
"s_waitcnt lgkmcnt(4)\n"
"ds_read_b128 v[74:77], v3 offset:54272\n"
"v_fma_f32 v223, v223, v229, v229\n"
"v_mfma_f32_16x16x32_bf16 v[162:165], v[82:85], a[92:95], v[162:165]\n"
"buffer_load_dwordx4 v1, s[20:23], m0 offen sc1 lds\n"
"v_mfma_f32_16x16x32_bf16 v[150:153], v[54:57], a[96:99], v[150:153]\n"
"s_waitcnt lgkmcnt(4)\n"
"ds_read_b128 v[78:81], v3 offset:55296\n"
"v_rcp_f32 v222, v222\n"
"v_mfma_f32_16x16x32_bf16 v[154:157], v[54:57], a[100:103], v[154:157]\n"
"s_add_u32 m0, s59, 94208\n"
"v_exp_f32 v224, v224\n"
"v_mfma_f32_16x16x32_bf16 v[158:161], v[58:61], a[96:99], v[158:161]\n"
"s_waitcnt lgkmcnt(4)\n"
"ds_read_b128 v[82:85], v3 offset:56320\n"
"v_mul_f32 v138, v138, v222\n"
"v_fma_f32 v224, v224, v229, v229\n"
"v_mfma_f32_16x16x32_bf16 v[162:165], v[58:61], a[100:103], v[162:165]\n"
"buffer_load_dwordx4 v1, s[20:23], m0 offen sc1 lds\n"
"v_mfma_f32_16x16x32_bf16 v[150:153], v[62:65], a[104:107], v[150:153]\n"
"s_waitcnt lgkmcnt(4)\n"
"ds_read_b128 v[54:57], v3 offset:57344\n"
"v_mfma_f32_16x16x32_bf16 v[154:157], v[62:65], a[108:111], v[154:157]\n"
"s_add_u32 m0, s59, 98304\n"
"v_rcp_f32 v223, v223\n"
"v_mfma_f32_16x16x32_bf16 v[158:161], v[66:69], a[104:107], v[158:161]\n"
"s_waitcnt lgkmcnt(4)\n"
"ds_read_b128 v[58:61], v3 offset:58368\n"
"v_mul_f32 v139, v139, v223\n"
"v_cvt_pk_bf16_f32 v144, v138, v139\n"
"v_mfma_f32_16x16x32_bf16 v[162:165], v[66:69], a[108:111], v[162:165]\n"
"v_exp_f32 v225, v225\n"
"v_mfma_f32_16x16x32_bf16 v[150:153], v[70:73], a[112:115], v[150:153]\n"
"s_waitcnt lgkmcnt(4)\n"
"ds_read_b128 v[62:65], v3 offset:59392\n"
"v_fma_f32 v225, v225, v229, v229\n"
"v_mfma_f32_16x16x32_bf16 v[154:157], v[70:73], a[116:119], v[154:157]\n"
"buffer_load_dwordx4 v1, s[20:23], m0 offen sc1 lds\n"
"v_mfma_f32_16x16x32_bf16 v[158:161], v[74:77], a[112:115], v[158:161]\n"
"ds_read_b128 v[66:69], v3 offset:60416\n"
"v_rcp_f32 v224, v224\n"
"v_mfma_f32_16x16x32_bf16 v[162:165], v[74:77], a[116:119], v[162:165]\n"
"s_waitcnt vmcnt(8)\n"
"v_cmp_ne_u64 s[18:19], v[254:255], v[8:9]\n"
"s_nop 0\n"
"s_and_b32 s18, s18, 65535\n"
"s_cbranch_scc0 L11_%=\n"
"s_mov_b32 s72, 0\n"
"L12_%=:\n"
"buffer_load_dwordx2 v[254:255], v11, s[20:23], s63 offen sc1\n"
"s_add_u32 s72, s72, 1\n"
"s_waitcnt vmcnt(0)\n"
"v_cmp_ne_u64 s[18:19], v[254:255], v[8:9]\n"
"s_nop 0\n"
"s_and_b32 s18, s18, 65535\n"
"s_cbranch_scc0 L11_%=\n"
"s_cmp_lt_u32 s72, 4194304\n"
"s_cbranch_scc1 L12_%=\n"
"L11_%=:\n"
"s_waitcnt lgkmcnt(5)\n"
"v_mfma_f32_16x16x32_bf16 v[150:153], v[78:81], a[120:123], v[150:153]\n"
"s_waitcnt lgkmcnt(4)\n"
"ds_read_b128 v[70:73], v3 offset:61440\n"
"v_mul_f32 v140, v140, v224\n"
"v_mfma_f32_16x16x32_bf16 v[154:157], v[78:81], a[124:127], v[154:157]\n"
"v_rcp_f32 v225, v225\n"
"v_mfma_f32_16x16x32_bf16 v[158:161], v[82:85], a[120:123], v[158:161]\n"
"s_waitcnt lgkmcnt(4)\n"
"ds_read_b128 v[74:77], v3 offset:62464\n"
"v_mul_f32 v141, v141, v225\n"
"v_mfma_f32_16x16x32_bf16 v[162:165], v[82:85], a[124:127], v[162:165]\n"
"s_add_u32 s62, s67, 106496\n"
"v_cvt_pk_bf16_f32 v145, v140, v141\n"
"v_permlane16_swap_b32 v142, v144\n"
"v_mfma_f32_16x16x32_bf16 v[150:153], v[54:57], a[128:131], v[150:153]\n"
"s_waitcnt lgkmcnt(4)\n"
"ds_read_b128 v[78:81], v3 offset:63488\n"
"v_mfma_f32_16x16x32_bf16 v[154:157], v[54:57], a[132:135], v[154:157]\n"
"buffer_load_dwordx4 a[192:195], v5, s[20:23], s62 offen sc1\n"
"v_mfma_f32_16x16x32_bf16 v[158:161], v[58:61], a[128:131], v[158:161]\n"
"s_waitcnt lgkmcnt(4)\n"
"ds_read_b128 v[82:85], v3 offset:64512\n"
"v_permlane16_swap_b32 v143, v145\n"
"v_mfma_f32_16x16x32_bf16 v[162:165], v[58:61], a[132:135], v[162:165]\n"
"buffer_load_dwordx4 a[196:199], v5, s[20:23], s62 offen offset:2048 sc1\n"
"v_mfma_f32_16x16x32_bf16 v[150:153], v[62:65], a[136:139], v[150:153]\n"
"s_waitcnt lgkmcnt(4)\n"
"ds_read_b128 v[54:57], v4\n"
"v_mfma_f32_16x16x32_bf16 v[154:157], v[62:65], a[140:143], v[154:157]\n"
"s_add_u32 s62, s67, 110592\n"
"v_mfma_f32_16x16x32_bf16 v[158:161], v[66:69], a[136:139], v[158:161]\n"
"s_waitcnt lgkmcnt(4)\n"
"ds_read_b128 v[58:61], v4 offset:1024\n"
"buffer_store_dwordx4 v[142:145], v39, s[96:99], s83 offen offset:512 sc1\n"
"v_mfma_f32_16x16x32_bf16 v[162:165], v[66:69], a[140:143], v[162:165]\n"
"buffer_load_dwordx4 a[200:203], v5, s[20:23], s62 offen sc1\n"
"v_mfma_f32_16x16x32_bf16 v[150:153], v[70:73], a[144:147], v[150:153]\n"
"s_waitcnt lgkmcnt(4)\n"
"ds_read_b128 v[62:65], v4 offset:2048\n"
"v_mfma_f32_16x16x32_bf16 v[154:157], v[70:73], a[148:151], v[154:157]\n"
"v_mfma_f32_16x16x32_bf16 v[158:161], v[74:77], a[144:147], v[158:161]\n"
"s_waitcnt lgkmcnt(4)\n"
"ds_read_b128 v[66:69], v4 offset:3072\n"
"v_mfma_f32_16x16x32_bf16 v[162:165], v[74:77], a[148:151], v[162:165]\n"
"buffer_load_dwordx4 a[204:207], v5, s[20:23], s62 offen offset:2048 sc1\n"
"v_mfma_f32_16x16x32_bf16 v[150:153], v[78:81], a[152:155], v[150:153]\n"
"s_waitcnt lgkmcnt(4)\n"
"ds_read_b128 v[70:73], v2\n"
"v_mfma_f32_16x16x32_bf16 v[154:157], v[78:81], a[156:159], v[154:157]\n"
"s_add_u32 s62, s67, 114688\n"
"v_mfma_f32_16x16x32_bf16 v[158:161], v[82:85], a[152:155], v[158:161]\n"
"s_waitcnt lgkmcnt(4)\n"
"ds_read_b128 v[74:77], v2 offset:1024\n"
"v_mfma_f32_16x16x32_bf16 v[162:165], v[82:85], a[156:159], v[162:165]\n"
"buffer_load_dwordx4 a[208:211], v5, s[20:23], s62 offen sc1\n"
"v_mfma_f32_16x16x32_bf16 v[166:169], v[54:57], a[0:3], v[166:169]\n"
"s_waitcnt lgkmcnt(4)\n"
"ds_read_b128 v[78:81], v2 offset:2048\n"
"v_mfma_f32_16x16x32_bf16 v[170:173], v[54:57], a[4:7], v[170:173]\n"
"v_mfma_f32_16x16x32_bf16 v[174:177], v[58:61], a[0:3], v[174:177]\n"
"ds_read_b128 v[82:85], v2 offset:3072\n"
"v_mfma_f32_16x16x32_bf16 v[178:181], v[58:61], a[4:7], v[178:181]\n"
"s_barrier\n"
"v_mul_f32 v218, v230, v150\n"
"v_exp_f32 v218, v218\n"
"s_waitcnt lgkmcnt(5)\n"
"v_mfma_f32_16x16x32_bf16 v[166:169], v[62:65], a[8:11], v[166:169]\n"
"s_waitcnt lgkmcnt(4)\n"
"ds_read_b128 v[54:57], v2 offset:4096\n"
"v_mul_f32 v219, v230, v151\n"
"v_mfma_f32_16x16x32_bf16 v[170:173], v[62:65], a[12:15], v[170:173]\n"
"buffer_load_dwordx4 a[212:215], v5, s[20:23], s62 offen offset:2048 sc1\n"
"v_mfma_f32_16x16x32_bf16 v[174:177], v[66:69], a[8:11], v[174:177]\n"
"s_waitcnt lgkmcnt(4)\n"
"ds_read_b128 v[58:61], v2 offset:5120\n"
"v_fma_f32 v218, v218, v228, v228\n"
"v_mfma_f32_16x16x32_bf16 v[178:181], v[66:69], a[12:15], v[178:181]\n"
"buffer_load_dwordx4 v[86:89], v32, s[96:99], 0 offen sc1\n"
"v_mfma_f32_16x16x32_bf16 v[166:169], v[70:73], a[16:19], v[166:169]\n"
"s_waitcnt lgkmcnt(4)\n"
"ds_read_b128 v[62:65], v2 offset:6144\n"
"v_mul_f32 v150, v150, v158\n"
"v_mul_f32 v220, v230, v152\n"
"v_mfma_f32_16x16x32_bf16 v[170:173], v[70:73], a[20:23], v[170:173]\n"
"v_exp_f32 v219, v219\n"
"v_mfma_f32_16x16x32_bf16 v[174:177], v[74:77], a[16:19], v[174:177]\n"
"s_waitcnt lgkmcnt(4)\n"
"ds_read_b128 v[66:69], v2 offset:7168\n"
"v_fma_f32 v219, v219, v228, v228\n"
"v_mfma_f32_16x16x32_bf16 v[178:181], v[74:77], a[20:23], v[178:181]\n"
"buffer_load_dwordx4 v[90:93], v33, s[96:99], 0 offen sc1\n"
"v_mfma_f32_16x16x32_bf16 v[166:169], v[78:81], a[24:27], v[166:169]\n"
"s_waitcnt lgkmcnt(4)\n"
"ds_read_b128 v[70:73], v2 offset:8192\n"
"v_mul_f32 v151, v151, v159\n"
"v_mfma_f32_16x16x32_bf16 v[170:173], v[78:81], a[28:31], v[170:173]\n"
"s_add_u32 m0, s59, 102400\n"
"v_rcp_f32 v218, v218\n"
"v_mul_f32 v221, v230, v153\n"
"v_mfma_f32_16x16x32_bf16 v[174:177], v[82:85], a[24:27], v[174:177]\n"
"s_waitcnt lgkmcnt(4)\n"
"ds_read_b128 v[74:77], v2 offset:9216\n"
"v_mul_f32 v150, v150, v218\n"
"v_mfma_f32_16x16x32_bf16 v[178:181], v[82:85], a[28:31], v[178:181]\n"
"v_exp_f32 v220, v220\n"
"v_mfma_f32_16x16x32_bf16 v[166:169], v[54:57], a[32:35], v[166:169]\n"
"s_waitcnt lgkmcnt(4)\n"
"ds_read_b128 v[78:81], v2 offset:10240\n"
"v_fma_f32 v220, v220, v228, v228\n"
"v_mfma_f32_16x16x32_bf16 v[170:173], v[54:57], a[36:39], v[170:173]\n"
"buffer_load_dwordx4 v1, s[20:23], m0 offen sc1 lds\n"
"v_mul_f32 v152, v152, v160\n"
"v_mfma_f32_16x16x32_bf16 v[174:177], v[58:61], a[32:35], v[174:177]\n"
"s_waitcnt lgkmcnt(4)\n"
"ds_read_b128 v[82:85], v2 offset:11264\n"
"v_mul_f32 v222, v231, v154\n"
"v_mfma_f32_16x16x32_bf16 v[178:181], v[58:61], a[36:39], v[178:181]\n"
"v_rcp_f32 v219, v219\n"
"v_mfma_f32_16x16x32_bf16 v[166:169], v[62:65], a[40:43], v[166:169]\n"
"s_waitcnt lgkmcnt(4)\n"
"ds_read_b128 v[54:57], v2 offset:12288\n"
"v_mul_f32 v151, v151, v219\n"
"v_mfma_f32_16x16x32_bf16 v[170:173], v[62:65], a[44:47], v[170:173]\n"
"buffer_load_dwordx4 v[94:97], v32, s[96:99], 0 offen offset:2048 sc1\n"
"v_mfma_f32_16x16x32_bf16 v[174:177], v[66:69], a[40:43], v[174:177]\n"
"s_waitcnt lgkmcnt(4)\n"
"ds_read_b128 v[58:61], v2 offset:13312\n"
"v_cvt_pk_bf16_f32 v158, v150, v151\n"
"v_mul_f32 v153, v153, v161\n"
"v_mfma_f32_16x16x32_bf16 v[178:181], v[66:69], a[44:47], v[178:181]\n"
"v_exp_f32 v221, v221\n"
"v_mfma_f32_16x16x32_bf16 v[166:169], v[70:73], a[48:51], v[166:169]\n"
"s_waitcnt lgkmcnt(4)\n"
"ds_read_b128 v[62:65], v2 offset:14336\n"
"v_fma_f32 v221, v221, v228, v228\n"
"v_mfma_f32_16x16x32_bf16 v[170:173], v[70:73], a[52:55], v[170:173]\n"
"buffer_load_dwordx4 v[98:101], v33, s[96:99], 0 offen offset:2048 sc1\n"
"v_mfma_f32_16x16x32_bf16 v[174:177], v[74:77], a[48:51], v[174:177]\n"
"s_waitcnt lgkmcnt(4)\n"
"ds_read_b128 v[66:69], v2 offset:15360\n"
"v_mul_f32 v223, v231, v155\n"
"v_mul_f32 v154, v154, v162\n"
"v_mfma_f32_16x16x32_bf16 v[178:181], v[74:77], a[52:55], v[178:181]\n"
"s_add_u32 m0, s59, 106496\n"
"v_rcp_f32 v220, v220\n"
"v_mfma_f32_16x16x32_bf16 v[166:169], v[78:81], a[56:59], v[166:169]\n"
"ds_read_b128 v[70:73], v2 offset:16384\n"
"s_mov_b32 s3, 655360\n"
"s_waitcnt vmcnt(9)\n"
"v_mfma_f32_16x16x32_bf16 v[170:173], v[78:81], a[60:63], v[170:173]\n"
"s_waitcnt lgkmcnt(5)\n"
"buffer_load_dwordx4 v1, s[20:23], m0 offen sc1 lds\n"
"v_mfma_f32_16x16x32_bf16 v[174:177], v[82:85], a[56:59], v[174:177]\n"
"s_waitcnt lgkmcnt(4)\n"
"ds_read_b128 v[74:77], v2 offset:17408\n"
"v_mul_f32 v152, v152, v220\n"
"v_mfma_f32_16x16x32_bf16 v[178:181], v[82:85], a[60:63], v[178:181]\n"
"buffer_store_dwordx2 v[8:9], v14, s[20:23], s3 offen sc1\n"
"v_mul_f32 v224, v231, v156\n"
"v_mfma_f32_16x16x32_bf16 v[166:169], v[54:57], a[64:67], v[166:169]\n"
"s_waitcnt lgkmcnt(4)\n"
"ds_read_b128 v[78:81], v2 offset:18432\n"
"v_mul_f32 v155, v155, v163\n"
"v_mfma_f32_16x16x32_bf16 v[170:173], v[54:57], a[68:71], v[170:173]\n"
"buffer_load_dwordx4 v[102:105], v32, s[96:99], s80 offen sc1\n"
"v_mfma_f32_16x16x32_bf16 v[174:177], v[58:61], a[64:67], v[174:177]\n"
"s_waitcnt lgkmcnt(4)\n"
"ds_read_b128 v[82:85], v2 offset:19456\n"
"v_mul_f32 v225, v231, v157\n"
"v_mfma_f32_16x16x32_bf16 v[178:181], v[58:61], a[68:71], v[178:181]\n"
"v_exp_f32 v222, v222\n"
"v_mfma_f32_16x16x32_bf16 v[166:169], v[62:65], a[72:75], v[166:169]\n"
"s_waitcnt lgkmcnt(4)\n"
"ds_read_b128 v[54:57], v4 offset:4096\n"
"v_fma_f32 v222, v222, v229, v229\n"
"v_mul_f32 v156, v156, v164\n"
"v_mfma_f32_16x16x32_bf16 v[170:173], v[62:65], a[76:79], v[170:173]\n"
"buffer_load_dwordx4 v[106:109], v33, s[96:99], s80 offen sc1\n"
"v_mfma_f32_16x16x32_bf16 v[174:177], v[66:69], a[72:75], v[174:177]\n"
"s_waitcnt lgkmcnt(4)\n"
"ds_read_b128 v[58:61], v4 offset:5120\n"
"v_mul_f32 v157, v157, v165\n"
"v_mfma_f32_16x16x32_bf16 v[178:181], v[66:69], a[76:79], v[178:181]\n"
"s_add_u32 m0, s59, 110592\n"
"v_rcp_f32 v221, v221\n"
"v_mfma_f32_16x16x32_bf16 v[166:169], v[70:73], a[80:83], v[166:169]\n"
"s_waitcnt lgkmcnt(4)\n"
"ds_read_b128 v[62:65], v4 offset:6144\n"
"v_mul_f32 v153, v153, v221\n"
"v_cvt_pk_bf16_f32 v159, v152, v153\n"
"v_mfma_f32_16x16x32_bf16 v[170:173], v[70:73], a[84:87], v[170:173]\n"
"buffer_load_dwordx4 v1, s[20:23], m0 offen sc1 lds\n"
"v_mfma_f32_16x16x32_bf16 v[174:177], v[74:77], a[80:83], v[174:177]\n"
"s_waitcnt lgkmcnt(4)\n"
"ds_read_b128 v[66:69], v4 offset:7168\n"
"v_mfma_f32_16x16x32_bf16 v[178:181], v[74:77], a[84:87], v[178:181]\n"
"v_exp_f32 v223, v223\n"
"v_mfma_f32_16x16x32_bf16 v[166:169], v[78:81], a[88:91], v[166:169]\n"
"s_waitcnt lgkmcnt(4)\n"
"ds_read_b128 v[70:73], v4 offset:8192\n"
"v_fma_f32 v223, v223, v229, v229\n"
"v_mfma_f32_16x16x32_bf16 v[170:173], v[78:81], a[92:95], v[170:173]\n"
"buffer_load_dwordx4 v[110:113], v32, s[96:99], s80 offen offset:2048 sc1\n"
"v_mfma_f32_16x16x32_bf16 v[174:177], v[82:85], a[88:91], v[174:177]\n"
"s_waitcnt lgkmcnt(4)\n"
"ds_read_b128 v[74:77], v4 offset:9216\n"
"v_rcp_f32 v222, v222\n"
"v_mfma_f32_16x16x32_bf16 v[178:181], v[82:85], a[92:95], v[178:181]\n"
"v_exp_f32 v224, v224\n"
"v_mfma_f32_16x16x32_bf16 v[166:169], v[54:57], a[96:99], v[166:169]\n"
"s_waitcnt lgkmcnt(4)\n"
"ds_read_b128 v[78:81], v4 offset:10240\n"
"v_mul_f32 v154, v154, v222\n"
"v_mfma_f32_16x16x32_bf16 v[170:173], v[54:57], a[100:103], v[170:173]\n"
"buffer_load_dwordx4 v[114:117], v33, s[96:99], s80 offen offset:2048 sc1\n"
"v_mfma_f32_16x16x32_bf16 v[174:177], v[58:61], a[96:99], v[174:177]\n"
"ds_read_b128 v[82:85], v4 offset:11264\n"
"v_fma_f32 v224, v224, v229, v229\n"
"v_mfma_f32_16x16x32_bf16 v[178:181], v[58:61], a[100:103], v[178:181]\n"
"s_barrier\n"
"s_waitcnt lgkmcnt(5)\n"
"v_mfma_f32_16x16x32_bf16 v[166:169], v[62:65], a[104:107], v[166:169]\n"
"s_waitcnt lgkmcnt(4)\n"
"ds_read_b128 v[54:57], v4 offset:12288\n"
"v_rcp_f32 v223, v223\n"
"v_mfma_f32_16x16x32_bf16 v[170:173], v[62:65], a[108:111], v[170:173]\n"
"s_add_u32 m0, s59, 114688\n"
"v_exp_f32 v225, v225\n"
"v_mfma_f32_16x16x32_bf16 v[174:177], v[66:69], a[104:107], v[174:177]\n"
"s_waitcnt lgkmcnt(4)\n"
"ds_read_b128 v[58:61], v4 offset:13312\n"
"v_mul_f32 v155, v155, v223\n"
"v_cvt_pk_bf16_f32 v160, v154, v155\n"
"v_mfma_f32_16x16x32_bf16 v[178:181], v[66:69], a[108:111], v[178:181]\n"
"buffer_load_dwordx4 v1, s[20:23], m0 offen sc1 lds\n"
"v_mfma_f32_16x16x32_bf16 v[166:169], v[70:73], a[112:115], v[166:169]\n"
"s_waitcnt lgkmcnt(4)\n"
"ds_read_b128 v[62:65], v4 offset:14336\n"
"v_fma_f32 v225, v225, v229, v229\n"
"v_mfma_f32_16x16x32_bf16 v[170:173], v[70:73], a[116:119], v[170:173]\n"
"buffer_load_dwordx4 v[118:121], v32, s[96:99], s81 offen sc1\n"
"v_mfma_f32_16x16x32_bf16 v[174:177], v[74:77], a[112:115], v[174:177]\n"
"s_waitcnt lgkmcnt(4)\n"
"ds_read_b128 v[66:69], v4 offset:15360\n"
"v_mfma_f32_16x16x32_bf16 v[178:181], v[74:77], a[116:119], v[178:181]\n"
"s_add_u32 m0, s59, 118784\n"
"v_rcp_f32 v224, v224\n"
"v_mfma_f32_16x16x32_bf16 v[166:169], v[78:81], a[120:123], v[166:169]\n"
"s_waitcnt lgkmcnt(4)\n"
"ds_read_b128 v[70:73], v4 offset:16384\n"
"v_mul_f32 v156, v156, v224\n"
"v_mfma_f32_16x16x32_bf16 v[170:173], v[78:81], a[124:127], v[170:173]\n"
"buffer_load_dwordx4 v1, s[20:23], m0 offen sc1 lds\n"
"v_mfma_f32_16x16x32_bf16 v[174:177], v[82:85], a[120:123], v[174:177]\n"
"s_waitcnt lgkmcnt(4)\n"
"ds_read_b128 v[74:77], v4 offset:17408\n"
"v_rcp_f32 v225, v225\n"
"v_mfma_f32_16x16x32_bf16 v[178:181], v[82:85], a[124:127], v[178:181]\n"
"v_permlane16_swap_b32 v158, v160\n"
"v_mul_f32 v157, v157, v225\n"
"v_mfma_f32_16x16x32_bf16 v[166:169], v[54:57], a[128:131], v[166:169]\n"
"s_waitcnt lgkmcnt(4)\n"
"ds_read_b128 v[78:81], v4 offset:18432\n"
"v_cvt_pk_bf16_f32 v161, v156, v157\n"
"v_mfma_f32_16x16x32_bf16 v[170:173], v[54:57], a[132:135], v[170:173]\n"
"buffer_load_dwordx4 v[122:125], v33, s[96:99], s81 offen sc1\n"
"v_mfma_f32_16x16x32_bf16 v[174:177], v[58:61], a[128:131], v[174:177]\n"
"s_waitcnt lgkmcnt(4)\n"
"ds_read_b128 v[82:85], v4 offset:19456\n"
"v_mfma_f32_16x16x32_bf16 v[178:181], v[58:61], a[132:135], v[178:181]\n"
"v_permlane16_swap_b32 v159, v161\n"
"v_mfma_f32_16x16x32_bf16 v[166:169], v[62:65], a[136:139], v[166:169]\n"
"s_waitcnt lgkmcnt(4)\n"
"ds_read_b128 v[54:57], v2 offset:20480\n"
"buffer_store_dwordx4 v[158:161], v39, s[96:99], s87 offen sc1\n"
"v_mfma_f32_16x16x32_bf16 v[170:173], v[62:65], a[140:143], v[170:173]\n"
"s_mov_b32 s63, 655360\n"
"v_mfma_f32_16x16x32_bf16 v[174:177], v[66:69], a[136:139], v[174:177]\n"
"s_waitcnt lgkmcnt(4)\n"
"ds_read_b128 v[58:61], v2 offset:21504\n"
"v_mfma_f32_16x16x32_bf16 v[178:181], v[66:69], a[140:143], v[178:181]\n"
"buffer_load_dwordx2 v[254:255], v11, s[20:23], s63 offen sc1\n"
"v_mfma_f32_16x16x32_bf16 v[166:169], v[70:73], a[144:147], v[166:169]\n"
"s_waitcnt lgkmcnt(4)\n"
"ds_read_b128 v[62:65], v2 offset:22528\n"
"v_mfma_f32_16x16x32_bf16 v[170:173], v[70:73], a[148:151], v[170:173]\n"
"buffer_load_dwordx4 v[126:129], v32, s[96:99], s81 offen offset:2048 sc1\n"
"v_mfma_f32_16x16x32_bf16 v[174:177], v[74:77], a[144:147], v[174:177]\n"
"s_waitcnt lgkmcnt(4)\n"
"ds_read_b128 v[66:69], v2 offset:23552\n"
"s_add_u32 m0, s59, 122880\n"
"v_mfma_f32_16x16x32_bf16 v[178:181], v[74:77], a[148:151], v[178:181]\n"
"buffer_load_dwordx4 v1, s[20:23], m0 offen sc1 lds\n"
"v_mfma_f32_16x16x32_bf16 v[166:169], v[78:81], a[152:155], v[166:169]\n"
"s_waitcnt lgkmcnt(4)\n"
"ds_read_b128 v[70:73], v2 offset:24576\n"
"v_mfma_f32_16x16x32_bf16 v[170:173], v[78:81], a[156:159], v[170:173]\n"
"v_mfma_f32_16x16x32_bf16 v[174:177], v[82:85], a[152:155], v[174:177]\n"
"s_waitcnt lgkmcnt(4)\n"
"ds_read_b128 v[74:77], v2 offset:25600\n"
"v_mfma_f32_16x16x32_bf16 v[178:181], v[82:85], a[156:159], v[178:181]\n"
"buffer_load_dwordx4 v[130:133], v33, s[96:99], s81 offen offset:2048 sc1\n"
"v_mfma_f32_16x16x32_bf16 v[182:185], v[54:57], a[0:3], v[182:185]\n"
"s_waitcnt lgkmcnt(4)\n"
"ds_read_b128 v[78:81], v2 offset:26624\n"
"v_mfma_f32_16x16x32_bf16 v[186:189], v[54:57], a[4:7], v[186:189]\n"
"v_mfma_f32_16x16x32_bf16 v[190:193], v[58:61], a[0:3], v[190:193]\n"
"s_waitcnt lgkmcnt(4)\n"
"ds_read_b128 v[82:85], v2 offset:27648\n"
"v_mfma_f32_16x16x32_bf16 v[194:197], v[58:61], a[4:7], v[194:197]\n"
"buffer_load_dwordx4 v[134:137], v32, s[96:99], s82 offen sc1\n"
"v_mul_f32 v218, v230, v166\n"
"v_exp_f32 v218, v218\n"
"v_mfma_f32_16x16x32_bf16 v[182:185], v[62:65], a[8:11], v[182:185]\n"
"s_waitcnt lgkmcnt(4)\n"
"ds_read_b128 v[54:57], v2 offset:28672\n"
"v_mul_f32 v219, v230, v167\n"
"v_mfma_f32_16x16x32_bf16 v[186:189], v[62:65], a[12:15], v[186:189]\n"
"s_add_u32 m0, s59, 126976\n"
"v_exp_f32 v219, v219\n"
"v_mfma_f32_16x16x32_bf16 v[190:193], v[66:69], a[8:11], v[190:193]\n"
"s_waitcnt lgkmcnt(4)\n"
"ds_read_b128 v[58:61], v2 offset:29696\n"
"v_fma_f32 v218, v218, v228, v228\n"
"v_mul_f32 v166, v166, v174\n"
"v_mfma_f32_16x16x32_bf16 v[194:197], v[66:69], a[12:15], v[194:197]\n"
"buffer_load_dwordx4 v1, s[20:23], m0 offen sc1 lds\n"
"v_mfma_f32_16x16x32_bf16 v[182:185], v[70:73], a[16:19], v[182:185]\n"
"s_waitcnt lgkmcnt(4)\n"
"ds_read_b128 v[62:65], v2 offset:30720\n"
"v_mul_f32 v220, v230, v168\n"
"v_fma_f32 v219, v219, v228, v228\n"
"v_mfma_f32_16x16x32_bf16 v[186:189], v[70:73], a[20:23], v[186:189]\n"
"v_rcp_f32 v218, v218\n"
"v_mfma_f32_16x16x32_bf16 v[190:193], v[74:77], a[16:19], v[190:193]\n"
"s_waitcnt lgkmcnt(4)\n"
"ds_read_b128 v[66:69], v2 offset:31744\n"
"v_mul_f32 v167, v167, v175\n"
"v_mul_f32 v166, v166, v218\n"
"v_mfma_f32_16x16x32_bf16 v[194:197], v[74:77], a[20:23], v[194:197]\n"
"buffer_load_dwordx4 v[138:141], v33, s[96:99], s82 offen sc1\n"
"v_mfma_f32_16x16x32_bf16 v[182:185], v[78:81], a[24:27], v[182:185]\n"
"s_waitcnt lgkmcnt(4)\n"
"ds_read_b128 v[70:73], v2 offset:32768\n"
"v_mul_f32 v221, v230, v169\n"
"v_mul_f32 v168, v168, v176\n"
"v_mfma_f32_16x16x32_bf16 v[186:189], v[78:81], a[28:31], v[186:189]\n"
"v_exp_f32 v220, v220\n"
"v_mfma_f32_16x16x32_bf16 v[190:193], v[82:85], a[24:27], v[190:193]\n"
"s_waitcnt lgkmcnt(4)\n"
"ds_read_b128 v[74:77], v2 offset:33792\n"
"v_fma_f32 v220, v220, v228, v228\n"
"v_mfma_f32_16x16x32_bf16 v[194:197], v[82:85], a[28:31], v[194:197]\n"
"buffer_load_dwordx4 v[142:145], v32, s[96:99], s82 offen offset:2048 sc1\n"
"v_mul_f32 v222, v231, v170\n"
"v_mfma_f32_16x16x32_bf16 v[182:185], v[54:57], a[32:35], v[182:185]\n"
"s_waitcnt lgkmcnt(4)\n"
"ds_read_b128 v[78:81], v2 offset:34816\n"
"v_mul_f32 v169, v169, v177\n"
"v_mfma_f32_16x16x32_bf16 v[186:189], v[54:57], a[36:39], v[186:189]\n"
"s_add_u32 m0, s59, 131072\n"
"v_rcp_f32 v219, v219\n"
"v_mul_f32 v223, v231, v171\n"
"v_mfma_f32_16x16x32_bf16 v[190:193], v[58:61], a[32:35], v[190:193]\n"
"ds_read_b128 v[82:85], v2 offset:35840\n"
"v_mul_f32 v167, v167, v219\n"
"v_mfma_f32_16x16x32_bf16 v[194:197], v[58:61], a[36:39], v[194:197]\n"
"s_barrier\n"
"v_cvt_pk_bf16_f32 v174, v166, v167\n"
"s_waitcnt lgkmcnt(5)\n"
"v_mfma_f32_16x16x32_bf16 v[182:185], v[62:65], a[40:43], v[182:185]\n"
"s_waitcnt lgkmcnt(4)\n"
"ds_read_b128 v[54:57], v2 offset:36864\n"
"v_mul_f32 v170, v170, v178\n"
"v_mfma_f32_16x16x32_bf16 v[186:189], v[62:65], a[44:47], v[186:189]\n"
"buffer_load_dwordx4 v1, s[20:23], m0 offen sc1 lds\n"
"v_mfma_f32_16x16x32_bf16 v[190:193], v[66:69], a[40:43], v[190:193]\n"
"s_waitcnt lgkmcnt(4)\n"
"ds_read_b128 v[58:61], v2 offset:37888\n"
"v_mul_f32 v224, v231, v172\n"
"v_mul_f32 v171, v171, v179\n"
"v_mfma_f32_16x16x32_bf16 v[194:197], v[66:69], a[44:47], v[194:197]\n"
"buffer_load_dwordx4 v[146:149], v33, s[96:99], s82 offen offset:2048 sc1\n"
"v_mfma_f32_16x16x32_bf16 v[182:185], v[70:73], a[48:51], v[182:185]\n"
"s_waitcnt lgkmcnt(4)\n"
"ds_read_b128 v[62:65], v2 offset:38912\n"
"v_mul_f32 v225, v231, v173\n"
"v_mul_f32 v172, v172, v180\n"
"v_mfma_f32_16x16x32_bf16 v[186:189], v[70:73], a[52:55], v[186:189]\n"
"s_add_u32 m0, s59, 135168\n"
"v_exp_f32 v221, v221\n"
"v_mfma_f32_16x16x32_bf16 v[190:193], v[74:77], a[48:51], v[190:193]\n"
"s_waitcnt lgkmcnt(4)\n"
"ds_read_b128 v[66:69], v2 offset:39936\n"
"v_fma_f32 v221, v221, v228, v228\n"
"v_mfma_f32_16x16x32_bf16 v[194:197], v[74:77], a[52:55], v[194:197]\n"
"buffer_load_dwordx4 v1, s[20:23], m0 offen sc1 lds\n"
"v_mfma_f32_16x16x32_bf16 v[182:185], v[78:81], a[56:59], v[182:185]\n"
"ds_read_b128 v[70:73], v2 offset:40960\n"
"v_mul_f32 v173, v173, v181\n"
"v_rcp_f32 v220, v220\n"
"v_mfma_f32_16x16x32_bf16 v[186:189], v[78:81], a[60:63], v[186:189]\n"
"s_waitcnt vmcnt(10)\n"
"v_cmp_ne_u64 s[18:19], v[254:255], v[8:9]\n"
"s_nop 0\n"
"s_and_b32 s18, s18, 65535\n"
"s_cbranch_scc0 L13_%=\n"
"s_mov_b32 s72, 0\n"
"L14_%=:\n"
"buffer_load_dwordx2 v[254:255], v11, s[20:23], s63 offen sc1\n"
"s_add_u32 s72, s72, 1\n"
"s_waitcnt vmcnt(0)\n"
"v_cmp_ne_u64 s[18:19], v[254:255], v[8:9]\n"
"s_nop 0\n"
"s_and_b32 s18, s18, 65535\n"
"s_cbranch_scc0 L13_%=\n"
"s_cmp_lt_u32 s72, 4194304\n"
"s_cbranch_scc1 L14_%=\n"
"L13_%=:\n"
"s_waitcnt lgkmcnt(5)\n"
"v_mfma_f32_16x16x32_bf16 v[190:193], v[82:85], a[56:59], v[190:193]\n"
"s_waitcnt lgkmcnt(4)\n"
"ds_read_b128 v[74:77], v2 offset:41984\n"
"v_mul_f32 v168, v168, v220\n"
"v_mfma_f32_16x16x32_bf16 v[194:197], v[82:85], a[60:63], v[194:197]\n"
"v_exp_f32 v222, v222\n"
"v_mfma_f32_16x16x32_bf16 v[182:185], v[54:57], a[64:67], v[182:185]\n"
"s_waitcnt lgkmcnt(4)\n"
"ds_read_b128 v[78:81], v2 offset:43008\n"
"v_fma_f32 v222, v222, v229, v229\n"
"v_mfma_f32_16x16x32_bf16 v[186:189], v[54:57], a[68:71], v[186:189]\n"
"buffer_load_dwordx4 a[0:3], v32, s[96:99], s83 offen sc1\n"
"v_rcp_f32 v221, v221\n"
"v_mfma_f32_16x16x32_bf16 v[190:193], v[58:61], a[64:67], v[190:193]\n"
"s_waitcnt lgkmcnt(4)\n"
"ds_read_b128 v[82:85], v2 offset:44032\n"
"v_mul_f32 v169, v169, v221\n"
"v_mfma_f32_16x16x32_bf16 v[194:197], v[58:61], a[68:71], v[194:197]\n"
"s_add_u32 m0, s59, 139264\n"
"v_exp_f32 v223, v223\n"
"v_mfma_f32_16x16x32_bf16 v[182:185], v[62:65], a[72:75], v[182:185]\n"
"s_waitcnt lgkmcnt(4)\n"
"ds_read_b128 v[54:57], v4 offset:20480\n"
"v_cvt_pk_bf16_f32 v175, v168, v169\n"
"v_fma_f32 v223, v223, v229, v229\n"
"v_mfma_f32_16x16x32_bf16 v[186:189], v[62:65], a[76:79], v[186:189]\n"
"buffer_load_dwordx4 v1, s[20:23], m0 offen sc1 lds\n"
"v_mfma_f32_16x16x32_bf16 v[190:193], v[66:69], a[72:75], v[190:193]\n"
"s_waitcnt lgkmcnt(4)\n"
"ds_read_b128 v[58:61], v4 offset:21504\n"
"v_rcp_f32 v222, v222\n"
"v_mfma_f32_16x16x32_bf16 v[194:197], v[66:69], a[76:79], v[194:197]\n"
"s_add_u32 m0, s59, 143360\n"
"v_exp_f32 v224, v224\n"
"v_mfma_f32_16x16x32_bf16 v[182:185], v[70:73], a[80:83], v[182:185]\n"
"s_waitcnt lgkmcnt(4)\n"
"ds_read_b128 v[62:65], v4 offset:22528\n"
"v_mul_f32 v170, v170, v222\n"
"v_mfma_f32_16x16x32_bf16 v[186:189], v[70:73], a[84:87], v[186:189]\n"
"buffer_load_dwordx4 v1, s[20:23], m0 offen sc1 lds\n"
"v_fma_f32 v224, v224, v229, v229\n"
"v_mfma_f32_16x16x32_bf16 v[190:193], v[74:77], a[80:83], v[190:193]\n"
"s_waitcnt lgkmcnt(4)\n"
"ds_read_b128 v[66:69], v4 offset:23552\n"
"v_mfma_f32_16x16x32_bf16 v[194:197], v[74:77], a[84:87], v[194:197]\n"
"v_rcp_f32 v223, v223\n"
"v_mfma_f32_16x16x32_bf16 v[182:185], v[78:81], a[88:91], v[182:185]\n"
"s_waitcnt lgkmcnt(4)\n"
"ds_read_b128 v[70:73], v2 offset:45056\n"
"v_mul_f32 v171, v171, v223\n"
"v_cvt_pk_bf16_f32 v176, v170, v171\n"
"v_mfma_f32_16x16x32_bf16 v[186:189], v[78:81], a[92:95], v[186:189]\n"
"buffer_load_dwordx4 a[4:7], v33, s[96:99], s83 offen sc1\n"
"v_mfma_f32_16x16x32_bf16 v[190:193], v[82:85], a[88:91], v[190:193]\n"
"s_waitcnt lgkmcnt(4)\n"
"ds_read_b128 v[74:77], v2 offset:46080\n"
"v_exp_f32 v225, v225\n"
"v_mfma_f32_16x16x32_bf16 v[194:197], v[82:85], a[92:95], v[194:197]\n"
"s_add_u32 m0, s59, 147456\n"
"v_add_u32 v1, 28672, v1\n"
"v_fma_f32 v225, v225, v229, v229\n"
"v_mfma_f32_16x16x32_bf16 v[182:185], v[54:57], a[96:99], v[182:185]\n"
"s_waitcnt lgkmcnt(4)\n"
"ds_read_b128 v[78:81], v2 offset:47104\n"
"v_rcp_f32 v224, v224\n"
"v_mfma_f32_16x16x32_bf16 v[186:189], v[54:57], a[100:103], v[186:189]\n"
"buffer_load_dwordx4 v1, s[20:23], m0 offen sc1 lds\n"
"v_mfma_f32_16x16x32_bf16 v[190:193], v[58:61], a[96:99], v[190:193]\n"
"s_waitcnt lgkmcnt(4)\n"
"ds_read_b128 v[82:85], v2 offset:48128\n"
"v_mul_f32 v172, v172, v224\n"
"v_mfma_f32_16x16x32_bf16 v[194:197], v[58:61], a[100:103], v[194:197]\n"
"s_add_u32 m0, s59, 0\n"
"v_add_u32 v1, 151552, v1\n"
"v_rcp_f32 v225, v225\n"
"v_mfma_f32_16x16x32_bf16 v[182:185], v[62:65], a[104:107], v[182:185]\n"
"s_waitcnt lgkmcnt(4)\n"
"ds_read_b128 v[54:57], v2 offset:49152\n"
"v_mul_f32 v173, v173, v225\n"
"v_cvt_pk_bf16_f32 v177, v172, v173\n"
"v_mfma_f32_16x16x32_bf16 v[186:189], v[62:65], a[108:111], v[186:189]\n"
"buffer_load_dwordx4 v1, s[20:23], m0 offen sc1 lds\n"
"v_mfma_f32_16x16x32_bf16 v[190:193], v[66:69], a[104:107], v[190:193]\n"
"s_waitcnt lgkmcnt(4)\n"
"ds_read_b128 v[58:61], v2 offset:50176\n"
"v_mfma_f32_16x16x32_bf16 v[194:197], v[66:69], a[108:111], v[194:197]\n"
"v_permlane16_swap_b32 v174, v176\n"
"v_mfma_f32_16x16x32_bf16 v[182:185], v[70:73], a[112:115], v[182:185]\n"
"s_waitcnt lgkmcnt(4)\n"
"ds_read_b128 v[62:65], v2 offset:51200\n"
"v_permlane16_swap_b32 v175, v177\n"
"v_mfma_f32_16x16x32_bf16 v[186:189], v[70:73], a[116:119], v[186:189]\n"
"buffer_load_dwordx4 a[8:11], v32, s[96:99], s83 offen offset:2048 sc1\n"
"buffer_store_dwordx4 v[174:177], v39, s[96:99], s87 offen offset:512 sc1\n"
"v_mfma_f32_16x16x32_bf16 v[190:193], v[74:77], a[112:115], v[190:193]\n"
"s_waitcnt lgkmcnt(4)\n"
"ds_read_b128 v[66:69], v2 offset:52224\n"
"v_mfma_f32_16x16x32_bf16 v[194:197], v[74:77], a[116:119], v[194:197]\n"
"s_add_u32 m0, s59, 4096\n"
"v_mfma_f32_16x16x32_bf16 v[182:185], v[78:81], a[120:123], v[182:185]\n"
"s_waitcnt lgkmcnt(4)\n"
"ds_read_b128 v[70:73], v2 offset:53248\n"
"v_mfma_f32_16x16x32_bf16 v[186:189], v[78:81], a[124:127], v[186:189]\n"
"buffer_load_dwordx4 v1, s[20:23], m0 offen sc1 lds\n"
"v_mfma_f32_16x16x32_bf16 v[190:193], v[82:85], a[120:123], v[190:193]\n"
"s_waitcnt lgkmcnt(4)\n"
"ds_read_b128 v[74:77], v2 offset:54272\n"
"v_mfma_f32_16x16x32_bf16 v[194:197], v[82:85], a[124:127], v[194:197]\n"
"s_add_u32 m0, s59, 8192\n"
"v_mfma_f32_16x16x32_bf16 v[182:185], v[54:57], a[128:131], v[182:185]\n"
"s_waitcnt lgkmcnt(4)\n"
"ds_read_b128 v[78:81], v2 offset:55296\n"
"v_mfma_f32_16x16x32_bf16 v[186:189], v[54:57], a[132:135], v[186:189]\n"
"buffer_load_dwordx4 v1, s[20:23], m0 offen sc1 lds\n"
"v_mfma_f32_16x16x32_bf16 v[190:193], v[58:61], a[128:131], v[190:193]\n"
"s_waitcnt lgkmcnt(4)\n"
"ds_read_b128 v[82:85], v2 offset:56320\n"
"v_mfma_f32_16x16x32_bf16 v[194:197], v[58:61], a[132:135], v[194:197]\n"
"v_mfma_f32_16x16x32_bf16 v[182:185], v[62:65], a[136:139], v[182:185]\n"
"v_mfma_f32_16x16x32_bf16 v[186:189], v[62:65], a[140:143], v[186:189]\n"
"s_waitcnt lgkmcnt(4)\n"
"buffer_load_dwordx4 a[12:15], v33, s[96:99], s83 offen offset:2048 sc1\n"
"v_mfma_f32_16x16x32_bf16 v[190:193], v[66:69], a[136:139], v[190:193]\n"
"s_add_u32 m0, s59, 12288\n"
"v_mfma_f32_16x16x32_bf16 v[194:197], v[66:69], a[140:143], v[194:197]\n"
"s_waitcnt lgkmcnt(3)\n"
"v_mfma_f32_16x16x32_bf16 v[182:185], v[70:73], a[144:147], v[182:185]\n"
"v_mfma_f32_16x16x32_bf16 v[186:189], v[70:73], a[148:151], v[186:189]\n"
"s_waitcnt lgkmcnt(2)\n"
"buffer_load_dwordx4 v1, s[20:23], m0 offen sc1 lds\n"
"v_mfma_f32_16x16x32_bf16 v[190:193], v[74:77], a[144:147], v[190:193]\n"
"s_add_u32 m0, s59, 16384\n"
"v_mfma_f32_16x16x32_bf16 v[194:197], v[74:77], a[148:151], v[194:197]\n"
"s_waitcnt lgkmcnt(1)\n"
"v_mfma_f32_16x16x32_bf16 v[182:185], v[78:81], a[152:155], v[182:185]\n"
"v_mfma_f32_16x16x32_bf16 v[186:189], v[78:81], a[156:159], v[186:189]\n"
"s_waitcnt lgkmcnt(0)\n"
"buffer_load_dwordx4 v1, s[20:23], m0 offen sc1 lds\n"
"v_mfma_f32_16x16x32_bf16 v[190:193], v[82:85], a[152:155], v[190:193]\n"
"v_mfma_f32_16x16x32_bf16 v[194:197], v[82:85], a[156:159], v[194:197]\n"
"buffer_load_dwordx4 a[16:19], v32, s[96:99], s84 offen sc1\n"
"s_nop 1\n"
"v_pk_mul_f32 v[218:219], v[182:183], v[230:231] op_sel_hi:[1,0]\n"
"v_exp_f32 v218, v218\n"
"v_exp_f32 v219, v219\n"
"s_nop 0\n"
"v_pk_mul_f32 v[182:183], v[182:183], v[190:191]\n"
"v_pk_fma_f32 v[218:219], v[218:219], v[228:229], v[228:229] op_sel_hi:[1,0,0]\n"
"v_rcp_f32 v218, v218\n"
"buffer_load_dwordx4 a[20:23], v33, s[96:99], s84 offen sc1\n"
"v_rcp_f32 v219, v219\n"
"s_nop 0\n"
"v_pk_mul_f32 v[182:183], v[182:183], v[218:219]\n"
"v_cvt_pk_bf16_f32 v190, v182, v183\n"
"v_pk_mul_f32 v[220:221], v[184:185], v[230:231] op_sel_hi:[1,0]\n"
"v_exp_f32 v220, v220\n"
"v_exp_f32 v221, v221\n"
"v_pk_mul_f32 v[184:185], v[184:185], v[192:193]\n"
"buffer_load_dwordx4 a[24:27], v32, s[96:99], s84 offen offset:2048 sc1\n"
"v_pk_fma_f32 v[220:221], v[220:221], v[228:229], v[228:229] op_sel_hi:[1,0,0]\n"
"v_rcp_f32 v220, v220\n"
"v_rcp_f32 v221, v221\n"
"s_nop 0\n"
"v_pk_mul_f32 v[184:185], v[184:185], v[220:221]\n"
"v_cvt_pk_bf16_f32 v191, v184, v185\n"
"v_pk_mul_f32 v[218:219], v[186:187], v[230:231] op_sel:[0,1]\n"
"buffer_load_dwordx4 a[28:31], v33, s[96:99], s84 offen offset:2048 sc1\n"
"v_exp_f32 v218, v218\n"
"v_exp_f32 v219, v219\n"
"v_pk_mul_f32 v[186:187], v[186:187], v[194:195]\n"
"v_pk_fma_f32 v[218:219], v[218:219], v[228:229], v[228:229] op_sel:[0,1,1]\n"
"v_rcp_f32 v218, v218\n"
"v_rcp_f32 v219, v219\n"
"s_nop 0\n"
"v_pk_mul_f32 v[186:187], v[186:187], v[218:219]\n"
"buffer_load_dwordx4 a[32:35], v32, s[96:99], s85 offen sc1\n"
"v_cvt_pk_bf16_f32 v192, v186, v187\n"
"v_pk_mul_f32 v[220:221], v[188:189], v[230:231] op_sel:[0,1]\n"
"v_exp_f32 v220, v220\n"
"v_exp_f32 v221, v221\n"
"v_pk_mul_f32 v[188:189], v[188:189], v[196:197]\n"
"v_pk_fma_f32 v[220:221], v[220:221], v[228:229], v[228:229] op_sel:[0,1,1]\n"
"buffer_load_dwordx4 a[36:39], v33, s[96:99], s85 offen sc1\n"
"v_rcp_f32 v220, v220\n"
"v_rcp_f32 v221, v221\n"
"s_nop 0\n"
"v_pk_mul_f32 v[188:189], v[188:189], v[220:221]\n"
"v_cvt_pk_bf16_f32 v193, v188, v189\n"
"v_permlane16_swap_b32 v190, v192\n"
"s_nop 0\n"
"v_permlane16_swap_b32 v191, v193\n"
"buffer_store_dwordx4 v[190:193], v42, s[96:99], 0 offen sc1\n"
"; P5 layer 0\n"
"s_barrier\n"
"ds_read_b128 v[54:57], v5 offset:57344\n"
"ds_read_b128 v[58:61], v5 offset:59392\n"
"ds_read_b128 v[62:65], v5 offset:61440\n"
"ds_read_b128 v[66:69], v5 offset:63488\n"
"ds_read_b128 v[70:73], v6\n"
"ds_read_b128 v[74:77], v6 offset:2048\n"
"s_waitcnt lgkmcnt(5)\n"
"v_mfma_f32_16x16x32_bf16 v[150:153], v[54:57], v[86:89], v[198:201]\n"
"s_waitcnt lgkmcnt(4)\n"
"ds_read_b128 v[78:81], v6 offset:4096\n"
"v_mfma_f32_16x16x32_bf16 v[154:157], v[54:57], v[90:93], 0\n"
"buffer_load_dwordx4 a[40:43], v32, s[96:99], s85 offen offset:2048 sc1\n"
"v_mfma_f32_16x16x32_bf16 v[158:161], v[58:61], v[86:89], v[202:205]\n"
"ds_read_b128 v[82:85], v6 offset:6144\n"
"s_mov_b32 s3, 786432\n"
"s_waitcnt vmcnt(13)\n"
"v_mfma_f32_16x16x32_bf16 v[162:165], v[58:61], v[90:93], 0\n"
"s_waitcnt lgkmcnt(5)\n"
"v_mfma_f32_16x16x32_bf16 v[166:169], v[62:65], v[86:89], v[206:209]\n"
"s_waitcnt lgkmcnt(4)\n"
"ds_read_b128 v[54:57], v6 offset:8192\n"
"v_mfma_f32_16x16x32_bf16 v[170:173], v[62:65], v[90:93], 0\n"
"buffer_store_dwordx2 v[8:9], v14, s[20:23], s3 offen sc1\n"
"v_mfma_f32_16x16x32_bf16 v[174:177], v[66:69], v[86:89], v[210:213]\n"
"s_waitcnt lgkmcnt(4)\n"
"ds_read_b128 v[58:61], v6 offset:10240\n"
"v_mfma_f32_16x16x32_bf16 v[178:181], v[66:69], v[90:93], 0\n"
"buffer_load_dwordx4 a[44:47], v33, s[96:99], s85 offen offset:2048 sc1\n"
"v_mfma_f32_16x16x32_bf16 v[182:185], v[70:73], v[86:89], v[214:217]\n"
"s_waitcnt lgkmcnt(4)\n"
"ds_read_b128 v[62:65], v6 offset:12288\n"
"v_mfma_f32_16x16x32_bf16 v[186:189], v[70:73], v[90:93], 0\n"
"buffer_load_dwordx4 a[48:51], v32, s[96:99], s86 offen sc1\n"
"v_mfma_f32_16x16x32_bf16 v[150:153], v[74:77], v[94:97], v[150:153]\n"
"s_waitcnt lgkmcnt(4)\n"
"ds_read_b128 v[66:69], v6 offset:14336\n"
"v_mfma_f32_16x16x32_bf16 v[154:157], v[74:77], v[98:101], v[154:157]\n"
"buffer_load_dwordx4 a[52:55], v33, s[96:99], s86 offen sc1\n"
"v_mfma_f32_16x16x32_bf16 v[158:161], v[78:81], v[94:97], v[158:161]\n"
"s_waitcnt lgkmcnt(4)\n"
"ds_read_b128 v[70:73], v6 offset:16384\n"
"v_mfma_f32_16x16x32_bf16 v[162:165], v[78:81], v[98:101], v[162:165]\n"
"v_mfma_f32_16x16x32_bf16 v[166:169], v[82:85], v[94:97], v[166:169]\n"
"s_waitcnt lgkmcnt(4)\n"
"ds_read_b128 v[74:77], v6 offset:18432\n"
"v_mfma_f32_16x16x32_bf16 v[170:173], v[82:85], v[98:101], v[170:173]\n"
"buffer_load_dwordx4 a[56:59], v32, s[96:99], s86 offen offset:2048 sc1\n"
"v_mfma_f32_16x16x32_bf16 v[174:177], v[54:57], v[94:97], v[174:177]\n"
"s_waitcnt lgkmcnt(4)\n"
"ds_read_b128 v[78:81], v6 offset:20480\n"
"v_mfma_f32_16x16x32_bf16 v[178:181], v[54:57], v[98:101], v[178:181]\n"
"v_mfma_f32_16x16x32_bf16 v[182:185], v[58:61], v[94:97], v[182:185]\n"
"s_waitcnt lgkmcnt(4)\n"
"ds_read_b128 v[82:85], v6 offset:22528\n"
"v_mfma_f32_16x16x32_bf16 v[186:189], v[58:61], v[98:101], v[186:189]\n"
"buffer_load_dwordx4 a[60:63], v33, s[96:99], s86 offen offset:2048 sc1\n"
"v_mfma_f32_16x16x32_bf16 v[150:153], v[62:65], v[102:105], v[150:153]\n"
"s_waitcnt lgkmcnt(4)\n"
"ds_read_b128 v[54:57], v6 offset:24576\n"
"v_mfma_f32_16x16x32_bf16 v[154:157], v[62:65], v[106:109], v[154:157]\n"
"s_add_u32 m0, s59, 151552\n"
"v_add_u32 v1, 4294836224, v1\n"
"v_mfma_f32_16x16x32_bf16 v[158:161], v[66:69], v[102:105], v[158:161]\n"
"s_waitcnt lgkmcnt(4)\n"
"ds_read_b128 v[58:61], v6 offset:26624\n"
"v_mfma_f32_16x16x32_bf16 v[162:165], v[66:69], v[106:109], v[162:165]\n"
"buffer_load_dwordx4 v1, s[20:23], m0 offen sc1 lds\n"
"v_mfma_f32_16x16x32_bf16 v[166:169], v[70:73], v[102:105], v[166:169]\n"
"s_waitcnt lgkmcnt(4)\n"
"ds_read_b128 v[62:65], v6 offset:28672\n"
"v_mfma_f32_16x16x32_bf16 v[170:173], v[70:73], v[106:109], v[170:173]\n"
"s_add_u32 m0, s59, 20480\n"
"v_add_u32 v1, 135168, v1\n"
"v_mfma_f32_16x16x32_bf16 v[174:177], v[74:77], v[102:105], v[174:177]\n"
"s_waitcnt lgkmcnt(4)\n"
"ds_read_b128 v[66:69], v6 offset:30720\n"
"v_mfma_f32_16x16x32_bf16 v[178:181], v[74:77], v[106:109], v[178:181]\n"
"buffer_load_dwordx4 v1, s[20:23], m0 offen sc1 lds\n"
"v_mfma_f32_16x16x32_bf16 v[182:185], v[78:81], v[102:105], v[182:185]\n"
"s_waitcnt lgkmcnt(4)\n"
"ds_read_b128 v[70:73], v6 offset:32768\n"
"v_mfma_f32_16x16x32_bf16 v[186:189], v[78:81], v[106:109], v[186:189]\n"
"s_add_u32 m0, s59, 24576\n"
"v_mfma_f32_16x16x32_bf16 v[150:153], v[82:85], v[110:113], v[150:153]\n"
"s_waitcnt lgkmcnt(4)\n"
"ds_read_b128 v[74:77], v6 offset:34816\n"
"v_mfma_f32_16x16x32_bf16 v[154:157], v[82:85], v[114:117], v[154:157]\n"
"buffer_load_dwordx4 v1, s[20:23], m0 offen sc1 lds\n"
"v_mfma_f32_16x16x32_bf16 v[158:161], v[54:57], v[110:113], v[158:161]\n"
"s_waitcnt lgkmcnt(4)\n"
"ds_read_b128 v[78:81], v6 offset:36864\n"
"s_add_u32 m0, s59, 28672\n"
"v_mfma_f32_16x16x32_bf16 v[162:165], v[54:57], v[114:117], v[162:165]\n"
"buffer_load_dwordx4 v1, s[20:23], m0 offen sc1 lds\n"
"v_mfma_f32_16x16x32_bf16 v[166:169], v[58:61], v[110:113], v[166:169]\n"
"ds_read_b128 v[82:85], v6 offset:38912\n"
"s_add_u32 m0, s59, 32768\n"
"v_mfma_f32_16x16x32_bf16 v[170:173], v[58:61], v[114:117], v[170:173]\n"
"s_barrier\n"
"s_waitcnt lgkmcnt(5)\n"
"v_mfma_f32_16x16x32_bf16 v[174:177], v[62:65], v[110:113], v[174:177]\n"
"s_waitcnt lgkmcnt(4)\n"
"ds_read_b128 v[54:57], v6 offset:40960\n"
"v_mfma_f32_16x16x32_bf16 v[178:181], v[62:65], v[114:117], v[178:181]\n"
"buffer_load_dwordx4 v1, s[20:23], m0 offen sc1 lds\n"
"v_mfma_f32_16x16x32_bf16 v[182:185], v[66:69], v[110:113], v[182:185]\n"
"s_waitcnt lgkmcnt(4)\n"
"ds_read_b128 v[58:61], v6 offset:43008\n"
"v_mfma_f32_16x16x32_bf16 v[186:189], v[66:69], v[114:117], v[186:189]\n"
"s_add_u32 m0, s59, 36864\n"
"v_mfma_f32_16x16x32_bf16 v[150:153], v[70:73], v[118:121], v[150:153]\n"
"s_waitcnt lgkmcnt(4)\n"
"ds_read_b128 v[62:65], v6 offset:45056\n"
"v_mfma_f32_16x16x32_bf16 v[154:157], v[70:73], v[122:125], v[154:157]\n"
"buffer_load_dwordx4 v1, s[20:23], m0 offen sc1 lds\n"
"v_mfma_f32_16x16x32_bf16 v[158:161], v[74:77], v[118:121], v[158:161]\n"
"ds_read_b128 v[66:69], v6 offset:47104\n"
"s_mov_b32 s3, 917504\n"
"s_waitcnt vmcnt(13)\n"
"v_mfma_f32_16x16x32_bf16 v[162:165], v[74:77], v[122:125], v[162:165]\n"
"s_waitcnt lgkmcnt(5)\n"
"s_add_u32 m0, s59, 40960\n"
"v_mfma_f32_16x16x32_bf16 v[166:169], v[78:81], v[118:121], v[166:169]\n"
"s_waitcnt lgkmcnt(4)\n"
"ds_read_b128 v[70:73], v6 offset:49152\n"
"v_mfma_f32_16x16x32_bf16 v[170:173], v[78:81], v[122:125], v[170:173]\n"
"buffer_store_dwordx2 v[8:9], v14, s[20:23], s3 offen sc1\n"
"v_mfma_f32_16x16x32_bf16 v[174:177], v[82:85], v[118:121], v[174:177]\n"
"s_waitcnt lgkmcnt(4)\n"
"ds_read_b128 v[74:77], v6 offset:51200\n"
"v_mfma_f32_16x16x32_bf16 v[178:181], v[82:85], v[122:125], v[178:181]\n"
"buffer_load_dwordx4 v1, s[20:23], m0 offen sc1 lds\n"
"v_mfma_f32_16x16x32_bf16 v[182:185], v[54:57], v[118:121], v[182:185]\n"
"s_waitcnt lgkmcnt(4)\n"
"ds_read_b128 v[78:81], v6 offset:53248\n"
"s_add_u32 m0, s59, 45056\n"
"v_mfma_f32_16x16x32_bf16 v[186:189], v[54:57], v[122:125], v[186:189]\n"
"s_mov_b32 s63, 786432\n"
"buffer_load_dwordx4 v1, s[20:23], m0 offen sc1 lds\n"
"v_mfma_f32_16x16x32_bf16 v[150:153], v[58:61], v[126:129], v[150:153]\n"
"s_waitcnt lgkmcnt(4)\n"
"ds_read_b128 v[82:85], v6 offset:55296\n"
"s_add_u32 m0, s59, 49152\n"
"v_mfma_f32_16x16x32_bf16 v[154:157], v[58:61], v[130:133], v[154:157]\n"
"buffer_load_dwordx2 v[254:255], v11, s[20:23], s63 offen sc1\n"
"v_mfma_f32_16x16x32_bf16 v[158:161], v[62:65], v[126:129], v[158:161]\n"
"s_waitcnt lgkmcnt(4)\n"
"ds_read_b128 v[54:57], v6 offset:57344\n"
"v_mfma_f32_16x16x32_bf16 v[162:165], v[62:65], v[130:133], v[162:165]\n"
"buffer_load_dwordx4 v1, s[20:23], m0 offen sc1 lds\n"
"v_mfma_f32_16x16x32_bf16 v[166:169], v[66:69], v[126:129], v[166:169]\n"
"s_waitcnt lgkmcnt(4)\n"
"ds_read_b128 v[58:61], v6 offset:59392\n"
"s_add_u32 m0, s59, 53248\n"
"v_mfma_f32_16x16x32_bf16 v[170:173], v[66:69], v[130:133], v[170:173]\n"
"buffer_load_dwordx4 v1, s[20:23], m0 offen sc1 lds\n"
"v_mfma_f32_16x16x32_bf16 v[174:177], v[70:73], v[126:129], v[174:177]\n"
"s_waitcnt lgkmcnt(4)\n"
"ds_read_b128 v[62:65], v6 offset:61440\n"
"v_mfma_f32_16x16x32_bf16 v[178:181], v[70:73], v[130:133], v[178:181]\n"
"s_add_u32 m0, s59, 57344\n"
"v_mfma_f32_16x16x32_bf16 v[182:185], v[74:77], v[126:129], v[182:185]\n"
"s_waitcnt lgkmcnt(4)\n"
"ds_read_b128 v[66:69], v6 offset:63488\n"
"v_mfma_f32_16x16x32_bf16 v[186:189], v[74:77], v[130:133], v[186:189]\n"
"buffer_load_dwordx4 v1, s[20:23], m0 offen sc1 lds\n"
"v_mfma_f32_16x16x32_bf16 v[150:153], v[78:81], v[134:137], v[150:153]\n"
"s_waitcnt lgkmcnt(4)\n"
"ds_read_b128 v[70:73], v7\n"
"s_add_u32 m0, s59, 61440\n"
"v_mfma_f32_16x16x32_bf16 v[154:157], v[78:81], v[138:141], v[154:157]\n"
"buffer_load_dwordx4 v1, s[20:23], m0 offen sc1 lds\n"
"v_mfma_f32_16x16x32_bf16 v[158:161], v[82:85], v[134:137], v[158:161]\n"
"s_waitcnt lgkmcnt(4)\n"
"ds_read_b128 v[74:77], v7 offset:2048\n"
"v_mfma_f32_16x16x32_bf16 v[162:165], v[82:85], v[138:141], v[162:165]\n"
"s_add_u32 m0, s59, 65536\n"
"v_mfma_f32_16x16x32_bf16 v[166:169], v[54:57], v[134:137], v[166:169]\n"
"s_waitcnt lgkmcnt(4)\n"
"ds_read_b128 v[78:81], v7 offset:4096\n"
"v_mfma_f32_16x16x32_bf16 v[170:173], v[54:57], v[138:141], v[170:173]\n"
"buffer_load_dwordx4 v1, s[20:23], m0 offen sc1 lds\n"
"v_mfma_f32_16x16x32_bf16 v[174:177], v[58:61], v[134:137], v[174:177]\n"
"s_waitcnt lgkmcnt(4)\n"
"ds_read_b128 v[82:85], v7 offset:6144\n"
"v_mfma_f32_16x16x32_bf16 v[178:181], v[58:61], v[138:141], v[178:181]\n"
"s_add_u32 m0, s59, 69632\n"
"v_mfma_f32_16x16x32_bf16 v[182:185], v[62:65], v[134:137], v[182:185]\n"
"s_waitcnt lgkmcnt(4)\n"
"ds_read_b128 v[54:57], v7 offset:8192\n"
"v_mfma_f32_16x16x32_bf16 v[186:189], v[62:65], v[138:141], v[186:189]\n"
"buffer_load_dwordx4 v1, s[20:23], m0 offen sc1 lds\n"
"v_mfma_f32_16x16x32_bf16 v[150:153], v[66:69], v[142:145], v[150:153]\n"
"s_waitcnt lgkmcnt(4)\n"
"ds_read_b128 v[58:61], v7 offset:10240\n"
"v_mfma_f32_16x16x32_bf16 v[154:157], v[66:69], v[146:149], v[154:157]\n"
"s_add_u32 m0, s59, 73728\n"
"v_mfma_f32_16x16x32_bf16 v[158:161], v[70:73], v[142:145], v[158:161]\n"
"s_waitcnt lgkmcnt(4)\n"
"ds_read_b128 v[62:65], v7 offset:12288\n"
"v_mfma_f32_16x16x32_bf16 v[162:165], v[70:73], v[146:149], v[162:165]\n"
"buffer_load_dwordx4 v1, s[20:23], m0 offen sc1 lds\n"
"v_mfma_f32_16x16x32_bf16 v[166:169], v[74:77], v[142:145], v[166:169]\n"
"s_waitcnt lgkmcnt(4)\n"
"ds_read_b128 v[66:69], v7 offset:14336\n"
"v_mfma_f32_16x16x32_bf16 v[170:173], v[74:77], v[146:149], v[170:173]\n"
"s_add_u32 m0, s59, 77824\n"
"v_mfma_f32_16x16x32_bf16 v[174:177], v[78:81], v[142:145], v[174:177]\n"
"buffer_load_dwordx4 v1, s[20:23], m0 offen sc1 lds\n"
"v_mfma_f32_16x16x32_bf16 v[178:181], v[78:81], v[146:149], v[178:181]\n"
"s_waitcnt lgkmcnt(4)\n"
"s_add_u32 m0, s59, 81920\n"
"v_mfma_f32_16x16x32_bf16 v[182:185], v[82:85], v[142:145], v[182:185]\n"
"buffer_load_dwordx4 v1, s[20:23], m0 offen sc1 lds\n"
"v_mfma_f32_16x16x32_bf16 v[186:189], v[82:85], v[146:149], v[186:189]\n"
"s_waitcnt lgkmcnt(3)\n"
"s_add_u32 m0, s59, 86016\n"
"v_mfma_f32_16x16x32_bf16 v[150:153], v[54:57], a[0:3], v[150:153]\n"
"v_mfma_f32_16x16x32_bf16 v[154:157], v[54:57], a[4:7], v[154:157]\n"
"s_waitcnt lgkmcnt(2)\n"
"v_mfma_f32_16x16x32_bf16 v[158:161], v[58:61], a[0:3], v[158:161]\n"
"buffer_load_dwordx4 v1, s[20:23], m0 offen sc1 lds\n"
"v_mfma_f32_16x16x32_bf16 v[162:165], v[58:61], a[4:7], v[162:165]\n"
"s_waitcnt lgkmcnt(1)\n"
"s_add_u32 m0, s59, 90112\n"
"v_mfma_f32_16x16x32_bf16 v[166:169], v[62:65], a[0:3], v[166:169]\n"
"v_mfma_f32_16x16x32_bf16 v[170:173], v[62:65], a[4:7], v[170:173]\n"
"s_waitcnt lgkmcnt(0)\n"
"buffer_load_dwordx4 v1, s[20:23], m0 offen sc1 lds\n"
"v_mfma_f32_16x16x32_bf16 v[174:177], v[66:69], a[0:3], v[174:177]\n"
"v_mfma_f32_16x16x32_bf16 v[178:181], v[66:69], a[4:7], v[178:181]\n"
"v_mfma_f32_16x16x32_bf16 v[182:185], a[160:163], a[0:3], v[182:185]\n"
"v_mfma_f32_16x16x32_bf16 v[186:189], a[160:163], a[4:7], v[186:189]\n"
"s_waitcnt vmcnt(11)\n"
"v_cmp_ne_u64 s[18:19], v[254:255], v[8:9]\n"
"s_nop 0\n"
"s_and_b32 s18, s18, 65535\n"
"s_cbranch_scc0 L15_%=\n"
"s_mov_b32 s72, 0\n"
"L16_%=:\n"
"buffer_load_dwordx2 v[254:255], v11, s[20:23], s63 offen sc1\n"
"s_add_u32 s72, s72, 1\n"
"s_waitcnt vmcnt(0)\n"
"v_cmp_ne_u64 s[18:19], v[254:255], v[8:9]\n"
"s_nop 0\n"
"s_and_b32 s18, s18, 65535\n"
"s_cbranch_scc0 L15_%=\n"
"s_cmp_lt_u32 s72, 4194304\n"
"s_cbranch_scc1 L16_%=\n"
"L15_%=:\n"
"v_mfma_f32_16x16x32_bf16 v[150:153], a[164:167], a[8:11], v[150:153]\n"
"v_mfma_f32_16x16x32_bf16 v[154:157], a[164:167], a[12:15], v[154:157]\n"
"s_barrier\n"
"v_mfma_f32_16x16x32_bf16 v[158:161], a[168:171], a[8:11], v[158:161]\n"
"buffer_load_dwordx4 a[64:67], v32, s[96:99], s87 offen sc1\n"
"v_mfma_f32_16x16x32_bf16 v[162:165], a[168:171], a[12:15], v[162:165]\n"
"v_mfma_f32_16x16x32_bf16 v[166:169], a[172:175], a[8:11], v[166:169]\n"
"v_mfma_f32_16x16x32_bf16 v[170:173], a[172:175], a[12:15], v[170:173]\n"
"buffer_load_dwordx4 a[68:71], v33, s[96:99], s87 offen sc1\n"
"v_mfma_f32_16x16x32_bf16 v[174:177], a[176:179], a[8:11], v[174:177]\n"
"s_add_u32 m0, s59, 135168\n"
"v_add_u32 v1, 4294926336, v1\n"
"v_mfma_f32_16x16x32_bf16 v[178:181], a[176:179], a[12:15], v[178:181]\n"
"v_mfma_f32_16x16x32_bf16 v[182:185], a[180:183], a[8:11], v[182:185]\n"
"s_mov_b32 s63, 917504\n"
"v_mfma_f32_16x16x32_bf16 v[186:189], a[180:183], a[12:15], v[186:189]\n"
"buffer_load_dwordx2 v[254:255], v11, s[20:23], s63 offen sc1\n"
"v_mfma_f32_16x16x32_bf16 v[150:153], a[184:187], a[16:19], v[150:153]\n"
"buffer_load_dwordx4 v1, s[20:23], m0 offen sc1 lds\n"
"v_mfma_f32_16x16x32_bf16 v[154:157], a[184:187], a[20:23], v[154:157]\n"
"buffer_load_dwordx4 a[72:75], v32, s[96:99], s87 offen offset:2048 sc1\n"
"v_mfma_f32_16x16x32_bf16 v[158:161], a[188:191], a[16:19], v[158:161]\n"
"v_mfma_f32_16x16x32_bf16 v[162:165], a[188:191], a[20:23], v[162:165]\n"
"v_mfma_f32_16x16x32_bf16 v[166:169], a[192:195], a[16:19], v[166:169]\n"
"ds_read_b128 v[62:65], v7 offset:16384\n"
"v_mfma_f32_16x16x32_bf16 v[170:173], a[192:195], a[20:23], v[170:173]\n"
"buffer_load_dwordx4 a[76:79], v33, s[96:99], s87 offen offset:2048 sc1\n"
"v_mfma_f32_16x16x32_bf16 v[174:177], a[196:199], a[16:19], v[174:177]\n"
"ds_read_b128 v[66:69], v7 offset:18432\n"
"v_mfma_f32_16x16x32_bf16 v[178:181], a[196:199], a[20:23], v[178:181]\n"
"s_add_u32 m0, s59, 94208\n"
"v_add_u32 v1, 45056, v1\n"
"v_mfma_f32_16x16x32_bf16 v[182:185], a[200:203], a[16:19], v[182:185]\n"
"ds_read_b128 v[70:73], v5\n"
"v_mfma_f32_16x16x32_bf16 v[186:189], a[200:203], a[20:23], v[186:189]\n"
"buffer_load_dwordx4 v1, s[20:23], m0 offen sc1 lds\n"
"v_mfma_f32_16x16x32_bf16 v[150:153], a[204:207], a[24:27], v[150:153]\n"
"ds_read_b128 v[74:77], v5 offset:2048\n"
"v_mfma_f32_16x16x32_bf16 v[154:157], a[204:207], a[28:31], v[154:157]\n"
"buffer_load_dwordx4 a[80:83], v32, s[96:99], s88 offen sc1\n"
"v_mfma_f32_16x16x32_bf16 v[158:161], a[208:211], a[24:27], v[158:161]\n"
"ds_read_b128 v[78:81], v5 offset:4096\n"
"v_mfma_f32_16x16x32_bf16 v[162:165], a[208:211], a[28:31], v[162:165]\n"
"buffer_load_dwordx4 a[84:87], v33, s[96:99], s88 offen sc1\n"
"v_mfma_f32_16x16x32_bf16 v[166:169], a[212:215], a[24:27], v[166:169]\n"
"s_waitcnt lgkmcnt(4)\n"
"ds_read_b128 v[82:85], v5 offset:6144\n"
"v_mfma_f32_16x16x32_bf16 v[170:173], a[212:215], a[28:31], v[170:173]\n"
"s_add_u32 m0, s59, 98304\n"
"v_mfma_f32_16x16x32_bf16 v[174:177], v[62:65], a[24:27], v[174:177]\n"
"s_waitcnt lgkmcnt(4)\n"
"ds_read_b128 v[54:57], v5 offset:8192\n"
"v_mfma_f32_16x16x32_bf16 v[178:181], v[62:65], a[28:31], v[178:181]\n"
"buffer_load_dwordx4 v1, s[20:23], m0 offen sc1 lds\n"
"v_mfma_f32_16x16x32_bf16 v[182:185], v[66:69], a[24:27], v[182:185]\n"
"s_waitcnt lgkmcnt(4)\n"
"ds_read_b128 v[58:61], v5 offset:10240\n"
"v_mfma_f32_16x16x32_bf16 v[186:189], v[66:69], a[28:31], v[186:189]\n"
"buffer_load_dwordx4 a[88:91], v32, s[96:99], s88 offen offset:2048 sc1\n"
"v_mfma_f32_16x16x32_bf16 v[150:153], v[70:73], a[32:35], v[150:153]\n"
"s_waitcnt lgkmcnt(4)\n"
"ds_read_b128 v[62:65], v5 offset:12288\n"
"v_mfma_f32_16x16x32_bf16 v[154:157], v[70:73], a[36:39], v[154:157]\n"
"buffer_load_dwordx4 a[92:95], v33, s[96:99], s88 offen offset:2048 sc1\n"
"v_mfma_f32_16x16x32_bf16 v[158:161], v[74:77], a[32:35], v[158:161]\n"
"s_waitcnt lgkmcnt(4)\n"
"ds_read_b128 v[66:69], v5 offset:14336\n"
"v_mfma_f32_16x16x32_bf16 v[162:165], v[74:77], a[36:39], v[162:165]\n"
"s_add_u32 m0, s59, 102400\n"
"v_mfma_f32_16x16x32_bf16 v[166:169], v[78:81], a[32:35], v[166:169]\n"
"s_waitcnt lgkmcnt(4)\n"
"ds_read_b128 v[70:73], v5 offset:16384\n"
"v_mfma_f32_16x16x32_bf16 v[170:173], v[78:81], a[36:39], v[170:173]\n"
"buffer_load_dwordx4 v1, s[20:23], m0 offen sc1 lds\n"
"v_mfma_f32_16x16x32_bf16 v[174:177], v[82:85], a[32:35], v[174:177]\n"
"s_waitcnt lgkmcnt(4)\n"
"ds_read_b128 v[74:77], v5 offset:18432\n"
"v_mfma_f32_16x16x32_bf16 v[178:181], v[82:85], a[36:39], v[178:181]\n"
"buffer_load_dwordx4 a[96:99], v32, s[96:99], s89 offen sc1\n"
"v_mfma_f32_16x16x32_bf16 v[182:185], v[54:57], a[32:35], v[182:185]\n"
"s_waitcnt lgkmcnt(4)\n"
"ds_read_b128 v[78:81], v7 offset:20480\n"
"v_mfma_f32_16x16x32_bf16 v[186:189], v[54:57], a[36:39], v[186:189]\n"
"v_mfma_f32_16x16x32_bf16 v[150:153], v[58:61], a[40:43], v[150:153]\n"
"s_waitcnt lgkmcnt(4)\n"
"ds_read_b128 v[82:85], v7 offset:22528\n"
"v_mfma_f32_16x16x32_bf16 v[154:157], v[58:61], a[44:47], v[154:157]\n"
"buffer_load_dwordx4 a[100:103], v33, s[96:99], s89 offen sc1\n"
"v_mfma_f32_16x16x32_bf16 v[158:161], v[62:65], a[40:43], v[158:161]\n"
"s_waitcnt lgkmcnt(4)\n"
"ds_read_b128 v[54:57], v5 offset:20480\n"
"v_mfma_f32_16x16x32_bf16 v[162:165], v[62:65], a[44:47], v[162:165]\n"
"s_add_u32 m0, s59, 106496\n"
"v_mfma_f32_16x16x32_bf16 v[166:169], v[66:69], a[40:43], v[166:169]\n"
"s_waitcnt lgkmcnt(4)\n"
"ds_read_b128 v[58:61], v5 offset:22528\n"
"v_mfma_f32_16x16x32_bf16 v[170:173], v[66:69], a[44:47], v[170:173]\n"
"buffer_load_dwordx4 v1, s[20:23], m0 offen sc1 lds\n"
"v_mfma_f32_16x16x32_bf16 v[174:177], v[70:73], a[40:43], v[174:177]\n"
"s_waitcnt lgkmcnt(4)\n"
"ds_read_b128 v[62:65], v5 offset:24576\n"
"v_mfma_f32_16x16x32_bf16 v[178:181], v[70:73], a[44:47], v[178:181]\n"
"buffer_load_dwordx4 a[104:107], v32, s[96:99], s89 offen offset:2048 sc1\n"
"v_mfma_f32_16x16x32_bf16 v[182:185], v[74:77], a[40:43], v[182:185]\n"
"s_waitcnt lgkmcnt(4)\n"
"ds_read_b128 v[66:69], v5 offset:26624\n"
"v_mfma_f32_16x16x32_bf16 v[186:189], v[74:77], a[44:47], v[186:189]\n"
"buffer_load_dwordx4 a[108:111], v33, s[96:99], s89 offen offset:2048 sc1\n"
"v_mfma_f32_16x16x32_bf16 v[150:153], v[78:81], a[48:51], v[150:153]\n"
"s_waitcnt lgkmcnt(4)\n"
"ds_read_b128 v[70:73], v5 offset:28672\n"
"v_mfma_f32_16x16x32_bf16 v[154:157], v[78:81], a[52:55], v[154:157]\n"
"s_add_u32 m0, s59, 110592\n"
"v_mfma_f32_16x16x32_bf16 v[158:161], v[82:85], a[48:51], v[158:161]\n"
"s_waitcnt lgkmcnt(4)\n"
"ds_read_b128 v[74:77], v5 offset:30720\n"
"v_mfma_f32_16x16x32_bf16 v[162:165], v[82:85], a[52:55], v[162:165]\n"
"buffer_load_dwordx4 v1, s[20:23], m0 offen sc1 lds\n"
"v_mfma_f32_16x16x32_bf16 v[166:169], v[54:57], a[48:51], v[166:169]\n"
"s_waitcnt lgkmcnt(4)\n"
"ds_read_b128 v[78:81], v5 offset:32768\n"
"v_mfma_f32_16x16x32_bf16 v[170:173], v[54:57], a[52:55], v[170:173]\n"
"buffer_load_dwordx4 a[112:115], v32, s[96:99], s90 offen sc1\n"
"v_mfma_f32_16x16x32_bf16 v[174:177], v[58:61], a[48:51], v[174:177]\n"
"s_waitcnt lgkmcnt(4)\n"
"ds_read_b128 v[82:85], v5 offset:34816\n"
"v_mfma_f32_16x16x32_bf16 v[178:181], v[58:61], a[52:55], v[178:181]\n"
"buffer_load_dwordx4 a[116:119], v33, s[96:99], s90 offen sc1\n"
"v_mfma_f32_16x16x32_bf16 v[182:185], v[62:65], a[48:51], v[182:185]\n"
"s_waitcnt lgkmcnt(4)\n"
"ds_read_b128 v[54:57], v5 offset:36864\n"
"v_mfma_f32_16x16x32_bf16 v[186:189], v[62:65], a[52:55], v[186:189]\n"
"s_add_u32 m0, s59, 114688\n"
"v_mfma_f32_16x16x32_bf16 v[150:153], v[66:69], a[56:59], v[150:153]\n"
"s_waitcnt lgkmcnt(4)\n"
"ds_read_b128 v[58:61], v5 offset:38912\n"
"v_mfma_f32_16x16x32_bf16 v[154:157], v[66:69], a[60:63], v[154:157]\n"
"buffer_load_dwordx4 v1, s[20:23], m0 offen sc1 lds\n"
"v_mfma_f32_16x16x32_bf16 v[158:161], v[70:73], a[56:59], v[158:161]\n"
"s_waitcnt lgkmcnt(4)\n"
"ds_read_b128 v[62:65], v5 offset:40960\n"
"v_mfma_f32_16x16x32_bf16 v[162:165], v[70:73], a[60:63], v[162:165]\n"
"buffer_load_dwordx4 a[120:123], v32, s[96:99], s90 offen offset:2048 sc1\n"
"v_mfma_f32_16x16x32_bf16 v[166:169], v[74:77], a[56:59], v[166:169]\n"
"ds_read_b128 v[66:69], v5 offset:43008\n"
"v_mfma_f32_16x16x32_bf16 v[170:173], v[74:77], a[60:63], v[170:173]\n"
"s_waitcnt vmcnt(26)\n"
"s_barrier\n"
"s_waitcnt lgkmcnt(5)\n"
"v_mfma_f32_16x16x32_bf16 v[174:177], v[78:81], a[56:59], v[174:177]\n"
"s_waitcnt lgkmcnt(4)\n"
"ds_read_b128 v[70:73], v5 offset:45056\n"
"v_mfma_f32_16x16x32_bf16 v[178:181], v[78:81], a[60:63], v[178:181]\n"
"buffer_load_dwordx4 a[124:127], v33, s[96:99], s90 offen offset:2048 sc1\n"
"v_mfma_f32_16x16x32_bf16 v[182:185], v[82:85], a[56:59], v[182:185]\n"
"ds_read_b128 v[74:77], v5 offset:47104\n"
"s_add_u32 m0, s59, 118784\n"
"v_mfma_f32_16x16x32_bf16 v[186:189], v[82:85], a[60:63], v[186:189]\n"
"s_waitcnt vmcnt(21)\n"
"v_cmp_ne_u64 s[18:19], v[254:255], v[8:9]\n"
"s_nop 0\n"
"s_and_b32 s18, s18, 65535\n"
"s_cbranch_scc0 L17_%=\n"
"s_mov_b32 s72, 0\n"
"L18_%=:\n"
"buffer_load_dwordx2 v[254:255], v11, s[20:23], s63 offen sc1\n"
"s_add_u32 s72, s72, 1\n"
"s_waitcnt vmcnt(0)\n"
"v_cmp_ne_u64 s[18:19], v[254:255], v[8:9]\n"
"s_nop 0\n"
"s_and_b32 s18, s18, 65535\n"
"s_cbranch_scc0 L17_%=\n"
"s_cmp_lt_u32 s72, 4194304\n"
"s_cbranch_scc1 L18_%=\n"
"L17_%=:\n"
"s_waitcnt lgkmcnt(5)\n"
"v_mfma_f32_16x16x32_bf16 v[150:153], v[54:57], a[64:67], v[150:153]\n"
"s_waitcnt lgkmcnt(4)\n"
"ds_read_b128 v[78:81], v5 offset:49152\n"
"v_mfma_f32_16x16x32_bf16 v[154:157], v[54:57], a[68:71], v[154:157]\n"
"buffer_load_dwordx4 v1, s[20:23], m0 offen sc1 lds\n"
"v_mfma_f32_16x16x32_bf16 v[158:161], v[58:61], a[64:67], v[158:161]\n"
"s_waitcnt lgkmcnt(4)\n"
"ds_read_b128 v[82:85], v5 offset:51200\n"
"v_mfma_f32_16x16x32_bf16 v[162:165], v[58:61], a[68:71], v[162:165]\n"
"buffer_load_dwordx4 a[128:131], v32, s[96:99], s91 offen sc1\n"
"v_mfma_f32_16x16x32_bf16 v[166:169], v[62:65], a[64:67], v[166:169]\n"
"s_waitcnt lgkmcnt(4)\n"
"ds_read_b128 v[54:57], v5 offset:53248\n"
"v_mfma_f32_16x16x32_bf16 v[170:173], v[62:65], a[68:71], v[170:173]\n"
"v_mfma_f32_16x16x32_bf16 v[174:177], v[66:69], a[64:67], v[174:177]\n"
"s_waitcnt lgkmcnt(4)\n"
"ds_read_b128 v[58:61], v5 offset:55296\n"
"v_mfma_f32_16x16x32_bf16 v[178:181], v[66:69], a[68:71], v[178:181]\n"
"buffer_load_dwordx4 a[132:135], v33, s[96:99], s91 offen sc1\n"
"v_mfma_f32_16x16x32_bf16 v[182:185], v[70:73], a[64:67], v[182:185]\n"
"s_waitcnt vmcnt(22) lgkmcnt(4)\n"
"ds_read_b128 v[62:65], v5 offset:57344\n"
"v_mfma_f32_16x16x32_bf16 v[186:189], v[70:73], a[68:71], v[186:189]\n"
"v_mfma_f32_16x16x32_bf16 v[150:153], v[74:77], a[72:75], v[150:153]\n"
"s_waitcnt vmcnt(21) lgkmcnt(4)\n"
"ds_read_b128 v[66:69], v5 offset:59392\n"
"v_mfma_f32_16x16x32_bf16 v[154:157], v[74:77], a[76:79], v[154:157]\n"
"buffer_load_dwordx4 a[136:139], v32, s[96:99], s91 offen offset:2048 sc1\n"
"v_mfma_f32_16x16x32_bf16 v[158:161], v[78:81], a[72:75], v[158:161]\n"
"s_waitcnt lgkmcnt(4)\n"
"ds_read_b128 v[70:73], v5 offset:61440\n"
"v_mfma_f32_16x16x32_bf16 v[162:165], v[78:81], a[76:79], v[162:165]\n"
"v_mfma_f32_16x16x32_bf16 v[166:169], v[82:85], a[72:75], v[166:169]\n"
"s_waitcnt lgkmcnt(4)\n"
"ds_read_b128 v[74:77], v5 offset:63488\n"
"v_mfma_f32_16x16x32_bf16 v[170:173], v[82:85], a[76:79], v[170:173]\n"
"buffer_load_dwordx4 a[140:143], v33, s[96:99], s91 offen offset:2048 sc1\n"
"v_mfma_f32_16x16x32_bf16 v[174:177], v[54:57], a[72:75], v[174:177]\n"
"s_waitcnt lgkmcnt(4)\n"
"ds_read_b128 v[78:81], v6\n"
"v_mfma_f32_16x16x32_bf16 v[178:181], v[54:57], a[76:79], v[178:181]\n"
"v_mfma_f32_16x16x32_bf16 v[182:185], v[58:61], a[72:75], v[182:185]\n"
"s_waitcnt vmcnt(21) lgkmcnt(4)\n"
"ds_read_b128 v[82:85], v6 offset:2048\n"
"v_mfma_f32_16x16x32_bf16 v[186:189], v[58:61], a[76:79], v[186:189]\n"
"buffer_load_dwordx4 a[144:147], v32, s[96:99], s92 offen sc1\n"
"v_mfma_f32_16x16x32_bf16 v[150:153], v[62:65], a[80:83], v[150:153]\n"
"s_waitcnt vmcnt(21) lgkmcnt(4)\n"
"ds_read_b128 v[54:57], v6 offset:4096\n"
"v_mfma_f32_16x16x32_bf16 v[154:157], v[62:65], a[84:87], v[154:157]\n"
"v_mfma_f32_16x16x32_bf16 v[158:161], v[66:69], a[80:83], v[158:161]\n"
"s_waitcnt lgkmcnt(4)\n"
"ds_read_b128 v[58:61], v6 offset:6144\n"
"v_mfma_f32_16x16x32_bf16 v[162:165], v[66:69], a[84:87], v[162:165]\n"
"buffer_load_dwordx4 a[148:151], v33, s[96:99], s92 offen sc1\n"
"v_mfma_f32_16x16x32_bf16 v[182:185], v[70:73], a[80:83], v[182:185]\n"
"s_waitcnt vmcnt(20) lgkmcnt(4)\n"
"ds_read_b128 v[62:65], v6 offset:8192\n"
"v_mfma_f32_16x16x32_bf16 v[186:189], v[70:73], a[84:87], v[186:189]\n"
"s_add_u32 m0, s59, 122880\n"
"v_mfma_f32_16x16x32_bf16 v[150:153], v[74:77], a[88:91], v[150:153]\n"
"s_waitcnt vmcnt(19) lgkmcnt(4)\n"
"ds_read_b128 v[66:69], v6 offset:10240\n"
"v_mfma_f32_16x16x32_bf16 v[154:157], v[74:77], a[92:95], v[154:157]\n"
"buffer_load_dwordx4 v1, s[20:23], m0 offen sc1 lds\n"
"v_mfma_f32_16x16x32_bf16 v[158:161], v[78:81], a[88:91], v[158:161]\n"
"s_waitcnt lgkmcnt(4)\n"
"ds_read_b128 v[70:73], v6 offset:12288\n"
"v_mfma_f32_16x16x32_bf16 v[162:165], v[78:81], a[92:95], v[162:165]\n"
"s_add_u32 m0, s59, 126976\n"
"v_mfma_f32_16x16x32_bf16 v[182:185], v[82:85], a[88:91], v[182:185]\n"
"ds_read_b128 v[74:77], v6 offset:14336\n"
"v_mfma_f32_16x16x32_bf16 v[186:189], v[82:85], a[92:95], v[186:189]\n"
"s_waitcnt vmcnt(16)\n"
"s_barrier\n"
"s_waitcnt lgkmcnt(5)\n"
"v_mfma_f32_16x16x32_bf16 v[150:153], v[54:57], a[96:99], v[150:153]\n"
"s_waitcnt lgkmcnt(4)\n"
"ds_read_b128 v[78:81], v6 offset:16384\n"
"v_mfma_f32_16x16x32_bf16 v[154:157], v[54:57], a[100:103], v[154:157]\n"
"buffer_load_dwordx4 v1, s[20:23], m0 offen sc1 lds\n"
"v_mfma_f32_16x16x32_bf16 v[158:161], v[58:61], a[96:99], v[158:161]\n"
"s_waitcnt lgkmcnt(4)\n"
"ds_read_b128 v[82:85], v6 offset:18432\n"
"s_add_u32 m0, s59, 131072\n"
"v_mfma_f32_16x16x32_bf16 v[162:165], v[58:61], a[100:103], v[162:165]\n"
"buffer_load_dwordx4 v1, s[20:23], m0 offen sc1 lds\n"
"v_mfma_f32_16x16x32_bf16 v[182:185], v[62:65], a[96:99], v[182:185]\n"
"s_waitcnt vmcnt(17) lgkmcnt(4)\n"
"ds_read_b128 v[54:57], v6 offset:20480\n"
"v_mfma_f32_16x16x32_bf16 v[186:189], v[62:65], a[100:103], v[186:189]\n"
"s_add_u32 m0, s59, 0\n"
"v_add_u32 v1, 135168, v1\n"
"v_mfma_f32_16x16x32_bf16 v[150:153], v[66:69], a[104:107], v[150:153]\n"
"s_waitcnt vmcnt(16) lgkmcnt(4)\n"
"ds_read_b128 v[58:61], v6 offset:22528\n"
"v_mfma_f32_16x16x32_bf16 v[154:157], v[66:69], a[108:111], v[154:157]\n"
"buffer_load_dwordx4 v1, s[20:23], m0 offen sc1 lds\n"
"v_mfma_f32_16x16x32_bf16 v[158:161], v[70:73], a[104:107], v[158:161]\n"
"s_waitcnt lgkmcnt(4)\n"
"ds_read_b128 v[62:65], v6 offset:24576\n"
"v_mfma_f32_16x16x32_bf16 v[162:165], v[70:73], a[108:111], v[162:165]\n"
"s_add_u32 m0, s59, 4096\n"
"v_mfma_f32_16x16x32_bf16 v[182:185], v[74:77], a[104:107], v[182:185]\n"
"s_waitcnt vmcnt(15) lgkmcnt(4)\n"
"ds_read_b128 v[66:69], v6 offset:26624\n"
"v_mfma_f32_16x16x32_bf16 v[186:189], v[74:77], a[108:111], v[186:189]\n"
"buffer_load_dwordx4 v1, s[20:23], m0 offen sc1 lds\n"
"v_mfma_f32_16x16x32_bf16 v[150:153], v[78:81], a[112:115], v[150:153]\n"
"s_waitcnt vmcnt(15) lgkmcnt(4)\n"
"ds_read_b128 v[70:73], v7 offset:4096\n"
"v_mfma_f32_16x16x32_bf16 v[154:157], v[78:81], a[116:119], v[154:157]\n"
"s_add_u32 m0, s59, 8192\n"
"v_mfma_f32_16x16x32_bf16 v[158:161], v[82:85], a[112:115], v[158:161]\n"
"s_waitcnt lgkmcnt(4)\n"
"ds_read_b128 v[74:77], v7 offset:6144\n"
"v_mfma_f32_16x16x32_bf16 v[162:165], v[82:85], a[116:119], v[162:165]\n"
"buffer_load_dwordx4 v1, s[20:23], m0 offen sc1 lds\n"
"v_mfma_f32_16x16x32_bf16 v[182:185], v[54:57], a[112:115], v[182:185]\n"
"s_waitcnt vmcnt(14) lgkmcnt(4)\n"
"ds_read_b128 v[78:81], v6 offset:28672\n"
"v_mfma_f32_16x16x32_bf16 v[186:189], v[54:57], a[116:119], v[186:189]\n"
"s_add_u32 m0, s59, 12288\n"
"s_add_u32 s60, s64, 4294955008\n"
"v_add_u32 v1, s60, v2\n"
"v_mfma_f32_16x16x32_bf16 v[150:153], v[58:61], a[120:123], v[150:153]\n"
"s_waitcnt vmcnt(13) lgkmcnt(4)\n"
"ds_read_b128 v[82:85], v6 offset:30720\n"
"v_mfma_f32_16x16x32_bf16 v[154:157], v[58:61], a[124:127], v[154:157]\n"
"buffer_load_dwordx4 v1, s[20:23], m0 offen sc1 lds\n"
"v_mfma_f32_16x16x32_bf16 v[158:161], v[62:65], a[120:123], v[158:161]\n"
"s_waitcnt lgkmcnt(4)\n"
"ds_read_b128 v[54:57], v6 offset:32768\n"
"v_mfma_f32_16x16x32_bf16 v[162:165], v[62:65], a[124:127], v[162:165]\n"
"s_add_u32 m0, s59, 16384\n"
"v_mfma_f32_16x16x32_bf16 v[182:185], v[66:69], a[120:123], v[182:185]\n"
"s_waitcnt vmcnt(12) lgkmcnt(4)\n"
"ds_read_b128 v[58:61], v6 offset:34816\n"
"v_mfma_f32_16x16x32_bf16 v[186:189], v[66:69], a[124:127], v[186:189]\n"
"buffer_load_dwordx4 v1, s[20:23], m0 offen sc1 lds\n"
"v_mfma_f32_16x16x32_bf16 v[150:153], v[70:73], a[128:131], v[150:153]\n"
"s_waitcnt vmcnt(12) lgkmcnt(4)\n"
"ds_read_b128 v[62:65], v6 offset:36864\n"
"v_mfma_f32_16x16x32_bf16 v[154:157], v[70:73], a[132:135], v[154:157]\n"
"s_add_u32 m0, s59, 20480\n"
"v_mfma_f32_16x16x32_bf16 v[158:161], v[74:77], a[128:131], v[158:161]\n"
"s_waitcnt lgkmcnt(4)\n"
"ds_read_b128 v[66:69], v6 offset:38912\n"
"v_mfma_f32_16x16x32_bf16 v[162:165], v[74:77], a[132:135], v[162:165]\n"
"buffer_load_dwordx4 v1, s[20:23], m0 offen sc1 lds\n"
"v_mfma_f32_16x16x32_bf16 v[182:185], v[78:81], a[128:131], v[182:185]\n"
"ds_read_b128 v[70:73], v6 offset:40960\n"
"v_mfma_f32_16x16x32_bf16 v[186:189], v[78:81], a[132:135], v[186:189]\n"
"s_waitcnt vmcnt(7)\n"
"s_barrier\n"
"s_waitcnt lgkmcnt(5)\n"
"v_mfma_f32_16x16x32_bf16 v[150:153], v[82:85], a[136:139], v[150:153]\n"
"s_waitcnt lgkmcnt(4)\n"
"ds_read_b128 v[74:77], v6 offset:43008\n"
"v_mfma_f32_16x16x32_bf16 v[154:157], v[82:85], a[140:143], v[154:157]\n"
"s_add_u32 m0, s59, 24576\n"
"v_mfma_f32_16x16x32_bf16 v[158:161], v[54:57], a[136:139], v[158:161]\n"
"s_waitcnt lgkmcnt(4)\n"
"ds_read_b128 v[78:81], v6 offset:45056\n"
"v_mfma_f32_16x16x32_bf16 v[162:165], v[54:57], a[140:143], v[162:165]\n"
"buffer_load_dwordx4 v1, s[20:23], m0 offen sc1 lds\n"
"v_mfma_f32_16x16x32_bf16 v[182:185], v[58:61], a[136:139], v[182:185]\n"
"s_waitcnt lgkmcnt(4)\n"
"ds_read_b128 v[82:85], v6 offset:47104\n"
"v_mfma_f32_16x16x32_bf16 v[186:189], v[58:61], a[140:143], v[186:189]\n"
"s_add_u32 m0, s59, 28672\n"
"v_mfma_f32_16x16x32_bf16 v[150:153], v[62:65], a[144:147], v[150:153]\n"
"s_waitcnt lgkmcnt(4)\n"
"ds_read_b128 v[54:57], v6 offset:49152\n"
"v_mfma_f32_16x16x32_bf16 v[154:157], v[62:65], a[148:151], v[154:157]\n"
"v_mfma_f32_16x16x32_bf16 v[158:161], v[66:69], a[144:147], v[158:161]\n"
"s_waitcnt lgkmcnt(4)\n"
"ds_read_b128 v[58:61], v6 offset:51200\n"
"v_mfma_f32_16x16x32_bf16 v[162:165], v[66:69], a[148:151], v[162:165]\n"
"buffer_load_dwordx4 v1, s[20:23], m0 offen sc1 lds\n"
"v_mfma_f32_16x16x32_bf16 v[182:185], v[70:73], a[144:147], v[182:185]\n"
"s_waitcnt lgkmcnt(4)\n"
"ds_read_b128 v[62:65], v6 offset:53248\n"
"v_mfma_f32_16x16x32_bf16 v[186:189], v[70:73], a[148:151], v[186:189]\n"
"v_mfma_f32_16x16x32_bf16 v[166:169], v[74:77], a[80:83], v[166:169]\n"
"s_waitcnt lgkmcnt(4)\n"
"ds_read_b128 v[66:69], v6 offset:55296\n"
"v_mfma_f32_16x16x32_bf16 v[170:173], v[74:77], a[84:87], v[170:173]\n"
"v_mfma_f32_16x16x32_bf16 v[174:177], v[78:81], a[80:83], v[174:177]\n"
"s_waitcnt lgkmcnt(4)\n"
"ds_read_b128 v[70:73], v6 offset:57344\n"
"v_mfma_f32_16x16x32_bf16 v[178:181], v[78:81], a[84:87], v[178:181]\n"
"ds_write_b128 v43, v[154:157]\n"
"v_mfma_f32_16x16x32_bf16 v[166:169], v[82:85], a[88:91], v[166:169]\n"
"s_waitcnt lgkmcnt(5)\n"
"ds_read_b128 v[74:77], v6 offset:59392\n"
"v_mfma_f32_16x16x32_bf16 v[170:173], v[82:85], a[92:95], v[170:173]\n"
"ds_write_b128 v43, v[162:165] offset:1024\n"
"v_mfma_f32_16x16x32_bf16 v[174:177], v[54:57], a[88:91], v[174:177]\n"
"ds_read_b128 v[78:81], v6 offset:61440\n"
"ds_write_b128 v43, v[186:189] offset:4096\n"
"v_mfma_f32_16x16x32_bf16 v[178:181], v[54:57], a[92:95], v[178:181]\n"
"s_waitcnt vmcnt(5) lgkmcnt(0)\n"
"s_barrier\n"
"v_mfma_f32_16x16x32_bf16 v[166:169], v[58:61], a[96:99], v[166:169]\n"
"ds_read_b128 v[82:85], v6 offset:63488\n"
"ds_read_b128 v[86:89], v44\n"
"v_mfma_f32_16x16x32_bf16 v[170:173], v[58:61], a[100:103], v[170:173]\n"
"s_add_u32 m0, s59, 32768\n"
"v_mfma_f32_16x16x32_bf16 v[174:177], v[62:65], a[96:99], v[174:177]\n"
"ds_read_b128 v[54:57], v7\n"
"ds_read_b128 v[90:93], v44 offset:1024\n"
"ds_read_b128 v[102:105], v44 offset:4096\n"
"v_mfma_f32_16x16x32_bf16 v[178:181], v[62:65], a[100:103], v[178:181]\n"
"buffer_load_dwordx4 v1, s[20:23], m0 offen sc1 lds\n"
"v_mfma_f32_16x16x32_bf16 v[166:169], v[66:69], a[104:107], v[166:169]\n"
"ds_read_b128 v[58:61], v7 offset:2048\n"
"v_mfma_f32_16x16x32_bf16 v[170:173], v[66:69], a[108:111], v[170:173]\n"
"s_add_u32 m0, s59, 36864\n"
"v_mfma_f32_16x16x32_bf16 v[174:177], v[70:73], a[104:107], v[174:177]\n"
"s_waitcnt lgkmcnt(4)\n"
"ds_read_b128 v[62:65], v5\n"
"v_add_f32 v198, v150, v86\n"
"v_add_f32 v199, v151, v87\n"
"v_add_f32 v200, v152, v88\n"
"v_add_f32 v201, v153, v89\n"
"v_mfma_f32_16x16x32_bf16 v[178:181], v[70:73], a[108:111], v[178:181]\n"
"buffer_load_dwordx4 v1, s[20:23], m0 offen sc1 lds\n"
"v_cvt_pk_bf16_f32 v218, v198, v199\n"
"v_mfma_f32_16x16x32_bf16 v[166:169], v[74:77], a[112:115], v[166:169]\n"
"s_waitcnt lgkmcnt(3)\n"
"ds_read_b128 v[66:69], v5 offset:2048\n"
"v_cvt_pk_bf16_f32 v219, v200, v201\n"
"v_add_f32 v202, v158, v90\n"
"v_mfma_f32_16x16x32_bf16 v[170:173], v[74:77], a[116:119], v[170:173]\n"
"s_add_u32 m0, s59, 40960\n"
"v_add_f32 v203, v159, v91\n"
"v_add_f32 v204, v160, v92\n"
"v_add_f32 v205, v161, v93\n"
"v_mfma_f32_16x16x32_bf16 v[174:177], v[78:81], a[112:115], v[174:177]\n"
"s_waitcnt lgkmcnt(3)\n"
"ds_read_b128 v[70:73], v5 offset:4096\n"
"v_cvt_pk_bf16_f32 v220, v202, v203\n"
"v_cvt_pk_bf16_f32 v221, v204, v205\n"
"v_mfma_f32_16x16x32_bf16 v[178:181], v[78:81], a[116:119], v[178:181]\n"
"buffer_load_dwordx4 v1, s[20:23], m0 offen sc1 lds\n"
"v_add_f32 v214, v182, v102\n"
"v_mfma_f32_16x16x32_bf16 v[166:169], v[82:85], a[120:123], v[166:169]\n"
"ds_read_b128 v[74:77], v5 offset:6144\n"
"v_add_f32 v215, v183, v103\n"
"v_add_f32 v216, v184, v104\n"
"v_mfma_f32_16x16x32_bf16 v[170:173], v[82:85], a[124:127], v[170:173]\n"
"s_add_u32 m0, s59, 45056\n"
"v_add_f32 v217, v185, v105\n"
"v_cvt_pk_bf16_f32 v226, v214, v215\n"
"v_cvt_pk_bf16_f32 v227, v216, v217\n"
"v_mfma_f32_16x16x32_bf16 v[174:177], v[54:57], a[120:123], v[174:177]\n"
"s_waitcnt lgkmcnt(4)\n"
"ds_read_b128 v[78:81], v5 offset:8192\n"
"v_mov_b32 v228, v226\n"
"v_mov_b32 v229, v227\n"
"v_mfma_f32_16x16x32_bf16 v[178:181], v[54:57], a[124:127], v[178:181]\n"
"buffer_load_dwordx4 v1, s[20:23], m0 offen sc1 lds\n"
"v_add_f32 v230, v198, v200\n"
"v_mfma_f32_16x16x32_bf16 v[166:169], v[58:61], a[128:131], v[166:169]\n"
"s_add_u32 m0, s59, 49152\n"
"v_add_f32 v231, v199, v201\n"
"v_mul_f32 v232, v198, v198\n"
"v_mul_f32 v233, v199, v199\n"
"v_mfma_f32_16x16x32_bf16 v[170:173], v[58:61], a[132:135], v[170:173]\n"
"s_waitcnt lgkmcnt(4)\n"
"buffer_store_dwordx4 v[218:221], v34, s[96:99], s79 offen sc1\n"
"v_permlane16_swap_b32 v226, v228\n"
"v_mfma_f32_16x16x32_bf16 v[174:177], v[62:65], a[128:131], v[174:177]\n"
"v_permlane16_swap_b32 v227, v229\n"
"v_mfma_f32_16x16x32_bf16 v[178:181], v[62:65], a[132:135], v[178:181]\n"
"s_waitcnt lgkmcnt(3)\n"
"buffer_store_dwordx4 v[226:229], v35, s[96:99], s79 offen sc1\n"
"v_fmac_f32 v232, v200, v200\n"
"v_fmac_f32 v233, v201, v201\n"
"v_mfma_f32_16x16x32_bf16 v[166:169], v[66:69], a[136:139], v[166:169]\n"
"buffer_load_dwordx4 v1, s[20:23], m0 offen sc1 lds\n"
"v_add_f32 v230, v230, v202\n"
"v_mfma_f32_16x16x32_bf16 v[170:173], v[66:69], a[140:143], v[170:173]\n"
"s_waitcnt lgkmcnt(2)\n"
"s_add_u32 m0, s59, 53248\n"
"v_fmac_f32 v232, v202, v202\n"
"v_add_f32 v231, v231, v203\n"
"v_mfma_f32_16x16x32_bf16 v[174:177], v[70:73], a[136:139], v[174:177]\n"
"v_fmac_f32 v233, v203, v203\n"
"v_add_f32 v230, v230, v204\n"
"v_fmac_f32 v232, v204, v204\n"
"v_add_f32 v231, v231, v205\n"
"v_mfma_f32_16x16x32_bf16 v[178:181], v[70:73], a[140:143], v[178:181]\n"
"s_waitcnt lgkmcnt(1)\n"
"v_fmac_f32 v233, v205, v205\n"
"v_add_f32 v230, v230, v214\n"
"v_mfma_f32_16x16x32_bf16 v[166:169], v[74:77], a[144:147], v[166:169]\n"
"buffer_load_dwordx4 v1, s[20:23], m0 offen sc1 lds\n"
"v_fmac_f32 v232, v214, v214\n"
"v_add_f32 v231, v231, v215\n"
"v_mfma_f32_16x16x32_bf16 v[170:173], v[74:77], a[148:151], v[170:173]\n"
"s_waitcnt lgkmcnt(0)\n"
"v_fmac_f32 v233, v215, v215\n"
"v_add_f32 v230, v230, v216\n"
"v_mfma_f32_16x16x32_bf16 v[174:177], v[78:81], a[144:147], v[174:177]\n"
"v_fmac_f32 v232, v216, v216\n"
"v_add_f32 v231, v231, v217\n"
"v_fmac_f32 v233, v217, v217\n"
"v_mfma_f32_16x16x32_bf16 v[178:181], v[78:81], a[148:151], v[178:181]\n"
"ds_write_b128 v43, v[170:173] offset:2048\n"
"s_nop 6\n"
"ds_write_b128 v43, v[178:181] offset:3072\n"
"s_waitcnt lgkmcnt(0)\n"
"s_barrier\n"
"ds_read_b128 v[94:97], v44 offset:2048\n"
"ds_read_b128 v[98:101], v44 offset:3072\n"
"s_waitcnt lgkmcnt(1)\n"
"v_pk_add_f32 v[206:207], v[166:167], v[94:95]\n"
"v_pk_add_f32 v[208:209], v[168:169], v[96:97]\n"
"v_cvt_pk_bf16_f32 v222, v206, v207\n"
"v_cvt_pk_bf16_f32 v223, v208, v209\n"
"s_waitcnt lgkmcnt(0)\n"
"v_pk_add_f32 v[210:211], v[174:175], v[98:99]\n"
"v_pk_add_f32 v[212:213], v[176:177], v[100:101]\n"
"v_cvt_pk_bf16_f32 v224, v210, v211\n"
"v_cvt_pk_bf16_f32 v225, v212, v213\n"
"buffer_store_dwordx4 v[222:225], v34, s[96:99], s79 offen offset:1024 sc1\n"
"v_pk_add_f32 v[230:231], v[230:231], v[206:207]\n"
"v_pk_fma_f32 v[232:233], v[206:207], v[206:207], v[232:233]\n"
"v_pk_add_f32 v[230:231], v[230:231], v[208:209]\n"
"v_pk_fma_f32 v[232:233], v[208:209], v[208:209], v[232:233]\n"
"v_pk_add_f32 v[230:231], v[230:231], v[210:211]\n"
"v_pk_fma_f32 v[232:233], v[210:211], v[210:211], v[232:233]\n"
"v_pk_add_f32 v[230:231], v[230:231], v[212:213]\n"
"v_pk_fma_f32 v[232:233], v[212:213], v[212:213], v[232:233]\n"
"v_add_f32 v230, v230, v231\n"
"v_add_f32 v232, v232, v233\n"
"s_nop 1\n"
"v_permlane32_swap_b32 v230, v232\n"
"v_add_f32 v230, v230, v232\n"
"v_mov_b32 v232, v230\n"
"s_nop 1\n"
"v_permlane16_swap_b32 v230, v232\n"
"v_add_f32 v230, v230, v232\n"
"buffer_store_dword v230, v36, s[96:99], s95 offen sc1\n"
"s_add_u32 m0, s59, 57344\n"
"s_nop 0\n"
"buffer_load_dwordx4 v1, s[20:23], m0 offen sc1 lds\n"
"s_add_u32 m0, s59, 61440\n"
"s_nop 0\n"
"buffer_load_dwordx4 v1, s[20:23], m0 offen sc1 lds\n"
"s_add_u32 m0, s59, 65536\n"
"s_nop 0\n"
"buffer_load_dwordx4 v1, s[20:23], m0 offen sc1 lds\n"
"s_mov_b32 s3, 1048576\n"
"s_waitcnt vmcnt(3)\n"
"buffer_store_dwordx2 v[8:9], v14, s[20:23], s3 offen sc1\n"
"s_barrier\n"
"s_add_u32 m0, s59, 135168\n"
"v_add_u32 v1, 4294901760, v1\n"
"buffer_load_dwordx4 v1, s[20:23], m0 offen sc1 lds\n"
"s_add_u32 m0, s59, 139264\n"
"s_nop 0\n"
"buffer_load_dwordx4 v1, s[20:23], m0 offen sc1 lds\n"
"s_add_u32 m0, s59, 143360\n"
"s_nop 0\n"
"buffer_load_dwordx4 v1, s[20:23], m0 offen sc1 lds\n"
"s_add_u32 m0, s59, 147456\n"
"s_nop 0\n"
"buffer_load_dwordx4 v1, s[20:23], m0 offen sc1 lds\n"
"s_add_u32 m0, s59, 0\n"
"v_add_u32 v1, 151552, v1\n"
"buffer_load_dwordx4 v1, s[20:23], m0 offen sc1 lds\n"
"s_add_u32 m0, s59, 4096\n"
"s_nop 0\n"
"buffer_load_dwordx4 v1, s[20:23], m0 offen sc1 lds\n"
"s_add_u32 m0, s59, 8192\n"
"s_nop 0\n"
"buffer_load_dwordx4 v1, s[20:23], m0 offen sc1 lds\n"
"s_add_u32 m0, s59, 69632\n"
"v_add_u32 v1, 4294909952, v1\n"
"buffer_load_dwordx4 v1, s[20:23], m0 offen sc1 lds\n"
"s_add_u32 m0, s59, 73728\n"
"s_nop 0\n"
"buffer_load_dwordx4 v1, s[20:23], m0 offen sc1 lds\n"
"s_add_u32 m0, s59, 77824\n"
"s_nop 0\n"
"buffer_load_dwordx4 v1, s[20:23], m0 offen sc1 lds\n"
"s_add_u32 m0, s59, 81920\n"
"s_nop 0\n"
"buffer_load_dwordx4 v1, s[20:23], m0 offen sc1 lds\n"
"s_add_u32 m0, s59, 86016\n"
"s_nop 0\n"
"buffer_load_dwordx4 v1, s[20:23], m0 offen sc1 lds\n"
"s_add_u32 m0, s59, 90112\n"
"s_nop 0\n"
"buffer_load_dwordx4 v1, s[20:23], m0 offen sc1 lds\n"
"s_add_u32 m0, s59, 94208\n"
"s_nop 0\n"
"buffer_load_dwordx4 v1, s[20:23], m0 offen sc1 lds\n"
"s_add_u32 s96, s96, 16252928\n"
"s_addc_u32 s97, s97, 0\n"
"s_sub_u32 s98, s98, 16252928\n"
"s_barrier\n"
"s_mov_b32 s63, 1048576\n"
"s_mov_b32 s72, 0\n"
"L20_%=:\n"
"buffer_load_dwordx2 v[254:255], v12, s[20:23], s63 offen sc1\n"
"s_add_u32 s72, s72, 1\n"
"s_waitcnt vmcnt(0)\n"
"v_cmp_ne_u64 s[18:19], v[254:255], v[8:9]\n"
"s_nop 0\n"
"s_and_b32 s18, s18, 65535\n"
"s_cbranch_scc0 L19_%=\n"
"s_cmp_lt_u32 s72, 4194304\n"
"s_cbranch_scc1 L20_%=\n"
"L19_%=:\n"
"; P1 layer 1\n"
"buffer_load_dwordx4 a[0:3], v27, s[96:99], 0 offen sc1\n"
"buffer_load_dwordx4 a[4:7], v27, s[96:99], s84 offen sc1\n"
"buffer_load_dwordx4 a[8:11], v27, s[96:99], 0 offen offset:1024 sc1\n"
"buffer_load_dwordx4 a[12:15], v27, s[96:99], s84 offen offset:1024 sc1\n"
"buffer_load_dwordx4 a[16:19], v27, s[96:99], 0 offen offset:2048 sc1\n"
"buffer_load_dwordx4 a[20:23], v27, s[96:99], s84 offen offset:2048 sc1\n"
"buffer_load_dwordx4 a[24:27], v27, s[96:99], 0 offen offset:3072 sc1\n"
"buffer_load_dwordx4 a[28:31], v27, s[96:99], s84 offen offset:3072 sc1\n"
"buffer_load_dwordx4 a[32:35], v27, s[96:99], s80 offen sc1\n"
"buffer_load_dwordx4 a[36:39], v27, s[96:99], s85 offen sc1\n"
"buffer_load_dwordx4 a[40:43], v27, s[96:99], s80 offen offset:1024 sc1\n"
"buffer_load_dwordx4 a[44:47], v27, s[96:99], s85 offen offset:1024 sc1\n"
"ds_read_b128 v[54:57], v2 offset:12288\n"
"ds_read_b128 v[58:61], v2 offset:13312\n"
"ds_read_b128 v[62:65], v2 offset:14336\n"
"ds_read_b128 v[66:69], v2 offset:15360\n"
"ds_read_b128 v[70:73], v2 offset:16384\n"
"ds_read_b128 v[74:77], v2 offset:17408\n"
"s_waitcnt vmcnt(11) lgkmcnt(5)\n"
"v_mfma_f32_16x16x32_bf16 v[110:113], a[0:3], v[54:57], 0\n"
"s_waitcnt vmcnt(10) lgkmcnt(4)\n"
"ds_read_b128 v[78:81], v2 offset:18432\n"
"v_mfma_f32_16x16x32_bf16 v[114:117], a[4:7], v[54:57], 0\n"
"buffer_load_dwordx4 a[48:51], v27, s[96:99], s80 offen offset:2048 sc1\n"
"v_mfma_f32_16x16x32_bf16 v[118:121], a[0:3], v[58:61], 0\n"
"s_waitcnt lgkmcnt(4)\n"
"ds_read_b128 v[82:85], v2 offset:19456\n"
"v_mfma_f32_16x16x32_bf16 v[122:125], a[4:7], v[58:61], 0\n"
"v_mfma_f32_16x16x32_bf16 v[126:129], a[0:3], v[62:65], 0\n"
"s_waitcnt lgkmcnt(4)\n"
"ds_read_b128 v[54:57], v2 offset:20480\n"
"v_mfma_f32_16x16x32_bf16 v[130:133], a[4:7], v[62:65], 0\n"
"buffer_load_dwordx4 a[52:55], v27, s[96:99], s85 offen offset:2048 sc1\n"
"v_mfma_f32_16x16x32_bf16 v[134:137], a[0:3], v[66:69], 0\n"
"s_waitcnt lgkmcnt(4)\n"
"ds_read_b128 v[58:61], v2 offset:21504\n"
"v_mfma_f32_16x16x32_bf16 v[138:141], a[4:7], v[66:69], 0\n"
"v_mfma_f32_16x16x32_bf16 v[142:145], a[0:3], v[70:73], 0\n"
"s_waitcnt vmcnt(11) lgkmcnt(4)\n"
"ds_read_b128 v[62:65], v2 offset:22528\n"
"v_mfma_f32_16x16x32_bf16 v[146:149], a[4:7], v[70:73], 0\n"
"buffer_load_dwordx4 a[56:59], v27, s[96:99], s80 offen offset:3072 sc1\n"
"v_mfma_f32_16x16x32_bf16 v[110:113], a[8:11], v[74:77], v[110:113]\n"
"s_waitcnt vmcnt(11) lgkmcnt(4)\n"
"ds_read_b128 v[66:69], v2 offset:23552\n"
"v_mfma_f32_16x16x32_bf16 v[114:117], a[12:15], v[74:77], v[114:117]\n"
"v_mfma_f32_16x16x32_bf16 v[118:121], a[8:11], v[78:81], v[118:121]\n"
"s_waitcnt lgkmcnt(4)\n"
"ds_read_b128 v[70:73], v2 offset:24576\n"
"v_mfma_f32_16x16x32_bf16 v[122:125], a[12:15], v[78:81], v[122:125]\n"
"buffer_load_dwordx4 a[60:63], v27, s[96:99], s85 offen offset:3072 sc1\n"
"v_mfma_f32_16x16x32_bf16 v[126:129], a[8:11], v[82:85], v[126:129]\n"
"s_waitcnt lgkmcnt(4)\n"
"ds_read_b128 v[74:77], v2 offset:25600\n"
"v_mfma_f32_16x16x32_bf16 v[130:133], a[12:15], v[82:85], v[130:133]\n"
"v_mfma_f32_16x16x32_bf16 v[134:137], a[8:11], v[54:57], v[134:137]\n"
"s_waitcnt lgkmcnt(4)\n"
"ds_read_b128 v[78:81], v2 offset:26624\n"
"v_mfma_f32_16x16x32_bf16 v[138:141], a[12:15], v[54:57], v[138:141]\n"
"v_mfma_f32_16x16x32_bf16 v[142:145], a[8:11], v[58:61], v[142:145]\n"
"s_waitcnt vmcnt(11) lgkmcnt(4)\n"
"ds_read_b128 v[82:85], v2 offset:27648\n"
"v_mfma_f32_16x16x32_bf16 v[146:149], a[12:15], v[58:61], v[146:149]\n"
"buffer_load_dwordx4 a[64:67], v27, s[96:99], s81 offen sc1\n"
"v_mfma_f32_16x16x32_bf16 v[110:113], a[16:19], v[62:65], v[110:113]\n"
"s_waitcnt vmcnt(11) lgkmcnt(4)\n"
"ds_read_b128 v[54:57], v2 offset:28672\n"
"v_mfma_f32_16x16x32_bf16 v[114:117], a[20:23], v[62:65], v[114:117]\n"
"v_mfma_f32_16x16x32_bf16 v[118:121], a[16:19], v[66:69], v[118:121]\n"
"s_waitcnt lgkmcnt(4)\n"
"ds_read_b128 v[58:61], v2 offset:29696\n"
"v_mfma_f32_16x16x32_bf16 v[122:125], a[20:23], v[66:69], v[122:125]\n"
"buffer_load_dwordx4 a[68:71], v27, s[96:99], s86 offen sc1\n"
"v_mfma_f32_16x16x32_bf16 v[126:129], a[16:19], v[70:73], v[126:129]\n"
"s_waitcnt lgkmcnt(4)\n"
"ds_read_b128 v[62:65], v2 offset:30720\n"
"v_mfma_f32_16x16x32_bf16 v[130:133], a[20:23], v[70:73], v[130:133]\n"
"v_mfma_f32_16x16x32_bf16 v[134:137], a[16:19], v[74:77], v[134:137]\n"
"s_waitcnt lgkmcnt(4)\n"
"ds_read_b128 v[66:69], v2 offset:31744\n"
"v_mfma_f32_16x16x32_bf16 v[138:141], a[20:23], v[74:77], v[138:141]\n"
"buffer_load_dwordx4 a[72:75], v27, s[96:99], s81 offen offset:1024 sc1\n"
"v_mfma_f32_16x16x32_bf16 v[142:145], a[16:19], v[78:81], v[142:145]\n"
"s_waitcnt vmcnt(12) lgkmcnt(4)\n"
"ds_read_b128 v[70:73], v2 offset:32768\n"
"v_mfma_f32_16x16x32_bf16 v[146:149], a[20:23], v[78:81], v[146:149]\n"
"v_mfma_f32_16x16x32_bf16 v[110:113], a[24:27], v[82:85], v[110:113]\n"
"s_waitcnt vmcnt(11) lgkmcnt(4)\n"
"ds_read_b128 v[74:77], v2 offset:33792\n"
"v_mfma_f32_16x16x32_bf16 v[114:117], a[28:31], v[82:85], v[114:117]\n"
"buffer_load_dwordx4 a[76:79], v27, s[96:99], s86 offen offset:1024 sc1\n"
"v_mfma_f32_16x16x32_bf16 v[118:121], a[24:27], v[54:57], v[118:121]\n"
"s_waitcnt lgkmcnt(4)\n"
"ds_read_b128 v[78:81], v2 offset:34816\n"
"v_mfma_f32_16x16x32_bf16 v[122:125], a[28:31], v[54:57], v[122:125]\n"
"v_mfma_f32_16x16x32_bf16 v[126:129], a[24:27], v[58:61], v[126:129]\n"
"s_waitcnt lgkmcnt(4)\n"
"ds_read_b128 v[82:85], v2 offset:35840\n"
"v_mfma_f32_16x16x32_bf16 v[130:133], a[28:31], v[58:61], v[130:133]\n"
"buffer_load_dwordx4 a[80:83], v27, s[96:99], s81 offen offset:2048 sc1\n"
"v_mfma_f32_16x16x32_bf16 v[134:137], a[24:27], v[62:65], v[134:137]\n"
"s_waitcnt lgkmcnt(4)\n"
"ds_read_b128 v[54:57], v2 offset:36864\n"
"v_mfma_f32_16x16x32_bf16 v[138:141], a[28:31], v[62:65], v[138:141]\n"
"v_mfma_f32_16x16x32_bf16 v[142:145], a[24:27], v[66:69], v[142:145]\n"
"s_waitcnt vmcnt(12) lgkmcnt(4)\n"
"ds_read_b128 v[58:61], v2 offset:37888\n"
"v_mfma_f32_16x16x32_bf16 v[146:149], a[28:31], v[66:69], v[146:149]\n"
"buffer_load_dwordx4 a[84:87], v27, s[96:99], s86 offen offset:2048 sc1\n"
"v_mfma_f32_16x16x32_bf16 v[110:113], a[32:35], v[70:73], v[110:113]\n"
"s_waitcnt vmcnt(12) lgkmcnt(4)\n"
"ds_read_b128 v[62:65], v2 offset:38912\n"
"v_mfma_f32_16x16x32_bf16 v[114:117], a[36:39], v[70:73], v[114:117]\n"
"v_mfma_f32_16x16x32_bf16 v[118:121], a[32:35], v[74:77], v[118:121]\n"
"s_waitcnt lgkmcnt(4)\n"
"ds_read_b128 v[66:69], v2 offset:39936\n"
"v_mfma_f32_16x16x32_bf16 v[122:125], a[36:39], v[74:77], v[122:125]\n"
"buffer_load_dwordx4 v[218:221], v38, s[96:99], 0 offen sc1\n"
"v_mfma_f32_16x16x32_bf16 v[126:129], a[32:35], v[78:81], v[126:129]\n"
"s_waitcnt lgkmcnt(4)\n"
"ds_read_b128 v[70:73], v2 offset:40960\n"
"v_mfma_f32_16x16x32_bf16 v[130:133], a[36:39], v[78:81], v[130:133]\n"
"v_mfma_f32_16x16x32_bf16 v[134:137], a[32:35], v[82:85], v[134:137]\n"
"s_waitcnt lgkmcnt(4)\n"
"ds_read_b128 v[74:77], v2 offset:41984\n"
"v_mfma_f32_16x16x32_bf16 v[138:141], a[36:39], v[82:85], v[138:141]\n"
"buffer_load_dwordx4 v[222:225], v38, s[96:99], s80 offen sc1\n"
"v_mfma_f32_16x16x32_bf16 v[142:145], a[32:35], v[54:57], v[142:145]\n"
"s_waitcnt vmcnt(13) lgkmcnt(4)\n"
"ds_read_b128 v[78:81], v2 offset:43008\n"
"v_mfma_f32_16x16x32_bf16 v[146:149], a[36:39], v[54:57], v[146:149]\n"
"v_mfma_f32_16x16x32_bf16 v[110:113], a[40:43], v[58:61], v[110:113]\n"
"s_waitcnt vmcnt(12) lgkmcnt(4)\n"
"ds_read_b128 v[82:85], v2 offset:44032\n"
"v_mfma_f32_16x16x32_bf16 v[114:117], a[44:47], v[58:61], v[114:117]\n"
"buffer_load_dwordx4 a[88:91], v27, s[96:99], s81 offen offset:3072 sc1\n"
"v_mfma_f32_16x16x32_bf16 v[118:121], a[40:43], v[62:65], v[118:121]\n"
"s_waitcnt lgkmcnt(4)\n"
"ds_read_b128 v[54:57], v2 offset:45056\n"
"v_mfma_f32_16x16x32_bf16 v[122:125], a[44:47], v[62:65], v[122:125]\n"
"v_mfma_f32_16x16x32_bf16 v[126:129], a[40:43], v[66:69], v[126:129]\n"
"s_waitcnt lgkmcnt(4)\n"
"ds_read_b128 v[58:61], v2 offset:46080\n"
"v_mfma_f32_16x16x32_bf16 v[130:133], a[44:47], v[66:69], v[130:133]\n"
"v_mfma_f32_16x16x32_bf16 v[134:137], a[40:43], v[70:73], v[134:137]\n"
"s_waitcnt lgkmcnt(4)\n"
"ds_read_b128 v[62:65], v2 offset:47104\n"
"v_mfma_f32_16x16x32_bf16 v[138:141], a[44:47], v[70:73], v[138:141]\n"
"buffer_load_dwordx4 a[92:95], v27, s[96:99], s86 offen offset:3072 sc1\n"
"v_mfma_f32_16x16x32_bf16 v[142:145], a[40:43], v[74:77], v[142:145]\n"
"s_waitcnt vmcnt(13) lgkmcnt(4)\n"
"ds_read_b128 v[66:69], v2 offset:48128\n"
"v_mfma_f32_16x16x32_bf16 v[146:149], a[44:47], v[74:77], v[146:149]\n"
"v_mfma_f32_16x16x32_bf16 v[110:113], a[48:51], v[78:81], v[110:113]\n"
"s_waitcnt vmcnt(12) lgkmcnt(4)\n"
"ds_read_b128 v[70:73], v2 offset:49152\n"
"v_mfma_f32_16x16x32_bf16 v[114:117], a[52:55], v[78:81], v[114:117]\n"
"buffer_load_dwordx4 a[96:99], v27, s[96:99], s82 offen sc1\n"
"v_mfma_f32_16x16x32_bf16 v[118:121], a[48:51], v[82:85], v[118:121]\n"
"s_waitcnt lgkmcnt(4)\n"
"ds_read_b128 v[74:77], v2 offset:50176\n"
"v_mfma_f32_16x16x32_bf16 v[122:125], a[52:55], v[82:85], v[122:125]\n"
"v_mfma_f32_16x16x32_bf16 v[126:129], a[48:51], v[54:57], v[126:129]\n"
"s_waitcnt lgkmcnt(4)\n"
"ds_read_b128 v[78:81], v2 offset:51200\n"
"v_mfma_f32_16x16x32_bf16 v[130:133], a[52:55], v[54:57], v[130:133]\n"
"buffer_load_dwordx4 a[100:103], v27, s[96:99], s87 offen sc1\n"
"v_mfma_f32_16x16x32_bf16 v[134:137], a[48:51], v[58:61], v[134:137]\n"
"ds_read_b128 v[82:85], v2 offset:52224\n"
"s_add_u32 m0, s59, 98304\n"
"v_mfma_f32_16x16x32_bf16 v[138:141], a[52:55], v[58:61], v[138:141]\n"
"s_barrier\n"
"s_waitcnt lgkmcnt(5)\n"
"v_mfma_f32_16x16x32_bf16 v[142:145], a[48:51], v[62:65], v[142:145]\n"
"s_waitcnt vmcnt(13) lgkmcnt(4)\n"
"ds_read_b128 v[54:57], v2 offset:53248\n"
"v_mfma_f32_16x16x32_bf16 v[146:149], a[52:55], v[62:65], v[146:149]\n"
"buffer_load_dwordx4 v1, s[20:23], m0 offen sc1 lds\n"
"v_mfma_f32_16x16x32_bf16 v[110:113], a[56:59], v[66:69], v[110:113]\n"
"s_waitcnt vmcnt(13) lgkmcnt(4)\n"
"ds_read_b128 v[58:61], v2 offset:54272\n"
"v_mfma_f32_16x16x32_bf16 v[114:117], a[60:63], v[66:69], v[114:117]\n"
"v_mfma_f32_16x16x32_bf16 v[118:121], a[56:59], v[70:73], v[118:121]\n"
"s_waitcnt lgkmcnt(4)\n"
"ds_read_b128 v[62:65], v2 offset:55296\n"
"v_mfma_f32_16x16x32_bf16 v[122:125], a[60:63], v[70:73], v[122:125]\n"
"buffer_load_dwordx4 a[104:107], v27, s[96:99], s82 offen offset:1024 sc1\n"
"v_mfma_f32_16x16x32_bf16 v[126:129], a[56:59], v[74:77], v[126:129]\n"
"s_waitcnt lgkmcnt(4)\n"
"ds_read_b128 v[66:69], v2 offset:56320\n"
"v_mfma_f32_16x16x32_bf16 v[130:133], a[60:63], v[74:77], v[130:133]\n"
"v_mfma_f32_16x16x32_bf16 v[134:137], a[56:59], v[78:81], v[134:137]\n"
"s_waitcnt lgkmcnt(4)\n"
"ds_read_b128 v[70:73], v2 offset:57344\n"
"v_mfma_f32_16x16x32_bf16 v[138:141], a[60:63], v[78:81], v[138:141]\n"
"buffer_load_dwordx4 a[108:111], v27, s[96:99], s87 offen offset:1024 sc1\n"
"v_mfma_f32_16x16x32_bf16 v[142:145], a[56:59], v[82:85], v[142:145]\n"
"s_waitcnt vmcnt(14) lgkmcnt(4)\n"
"ds_read_b128 v[74:77], v2 offset:58368\n"
"v_mfma_f32_16x16x32_bf16 v[146:149], a[60:63], v[82:85], v[146:149]\n"
"v_mfma_f32_16x16x32_bf16 v[110:113], a[64:67], v[54:57], v[110:113]\n"
"s_waitcnt vmcnt(13) lgkmcnt(4)\n"
"ds_read_b128 v[78:81], v2 offset:59392\n"
"v_mfma_f32_16x16x32_bf16 v[114:117], a[68:71], v[54:57], v[114:117]\n"
"buffer_load_dwordx4 a[112:115], v27, s[96:99], s82 offen offset:2048 sc1\n"
"v_mfma_f32_16x16x32_bf16 v[118:121], a[64:67], v[58:61], v[118:121]\n"
"s_waitcnt lgkmcnt(4)\n"
"ds_read_b128 v[82:85], v2 offset:60416\n"
"v_mfma_f32_16x16x32_bf16 v[122:125], a[68:71], v[58:61], v[122:125]\n"
"v_mfma_f32_16x16x32_bf16 v[126:129], a[64:67], v[62:65], v[126:129]\n"
"s_waitcnt lgkmcnt(4)\n"
"ds_read_b128 v[54:57], v2 offset:61440\n"
"v_mfma_f32_16x16x32_bf16 v[130:133], a[68:71], v[62:65], v[130:133]\n"
"buffer_load_dwordx4 a[116:119], v27, s[96:99], s87 offen offset:2048 sc1\n"
"v_mfma_f32_16x16x32_bf16 v[134:137], a[64:67], v[66:69], v[134:137]\n"
"s_waitcnt lgkmcnt(4)\n"
"ds_read_b128 v[58:61], v2 offset:62464\n"
"v_mfma_f32_16x16x32_bf16 v[138:141], a[68:71], v[66:69], v[138:141]\n"
"s_add_u32 m0, s59, 102400\n"
"v_mfma_f32_16x16x32_bf16 v[142:145], a[64:67], v[70:73], v[142:145]\n"
"s_waitcnt vmcnt(14) lgkmcnt(4)\n"
"ds_read_b128 v[62:65], v2 offset:63488\n"
"v_mfma_f32_16x16x32_bf16 v[146:149], a[68:71], v[70:73], v[146:149]\n"
"buffer_load_dwordx4 v1, s[20:23], m0 offen sc1 lds\n"
"v_mfma_f32_16x16x32_bf16 v[110:113], a[72:75], v[74:77], v[110:113]\n"
"s_waitcnt vmcnt(14) lgkmcnt(4)\n"
"ds_read_b128 v[66:69], v2 offset:64512\n"
"v_mfma_f32_16x16x32_bf16 v[114:117], a[76:79], v[74:77], v[114:117]\n"
"v_mfma_f32_16x16x32_bf16 v[118:121], a[72:75], v[78:81], v[118:121]\n"
"s_waitcnt lgkmcnt(4)\n"
"ds_read_b128 v[70:73], v3\n"
"v_mfma_f32_16x16x32_bf16 v[122:125], a[76:79], v[78:81], v[122:125]\n"
"v_mfma_f32_16x16x32_bf16 v[126:129], a[72:75], v[82:85], v[126:129]\n"
"s_waitcnt lgkmcnt(4)\n"
"ds_read_b128 v[74:77], v3 offset:1024\n"
"v_mfma_f32_16x16x32_bf16 v[130:133], a[76:79], v[82:85], v[130:133]\n"
"buffer_load_dwordx4 a[120:123], v27, s[96:99], s82 offen offset:3072 sc1\n"
"v_mfma_f32_16x16x32_bf16 v[134:137], a[72:75], v[54:57], v[134:137]\n"
"s_waitcnt lgkmcnt(4)\n"
"ds_read_b128 v[78:81], v3 offset:2048\n"
"v_mfma_f32_16x16x32_bf16 v[138:141], a[76:79], v[54:57], v[138:141]\n"
"v_mfma_f32_16x16x32_bf16 v[142:145], a[72:75], v[58:61], v[142:145]\n"
"s_waitcnt vmcnt(14) lgkmcnt(4)\n"
"ds_read_b128 v[82:85], v3 offset:3072\n"
"v_mfma_f32_16x16x32_bf16 v[146:149], a[76:79], v[58:61], v[146:149]\n"
"buffer_load_dwordx4 a[124:127], v27, s[96:99], s87 offen offset:3072 sc1\n"
"v_mfma_f32_16x16x32_bf16 v[110:113], a[80:83], v[62:65], v[110:113]\n"
"s_waitcnt vmcnt(14) lgkmcnt(4)\n"
"ds_read_b128 v[54:57], v4 offset:4096\n"
"v_mfma_f32_16x16x32_bf16 v[114:117], a[84:87], v[62:65], v[114:117]\n"
"v_mfma_f32_16x16x32_bf16 v[118:121], a[80:83], v[66:69], v[118:121]\n"
"s_waitcnt lgkmcnt(4)\n"
"ds_read_b128 v[58:61], v4 offset:5120\n"
"v_mfma_f32_16x16x32_bf16 v[122:125], a[84:87], v[66:69], v[122:125]\n"
"buffer_load_dwordx4 a[128:131], v27, s[96:99], s83 offen sc1\n"
"v_mfma_f32_16x16x32_bf16 v[126:129], a[80:83], v[70:73], v[126:129]\n"
"s_waitcnt lgkmcnt(4)\n"
"ds_read_b128 v[62:65], v4 offset:6144\n"
"v_mfma_f32_16x16x32_bf16 v[130:133], a[84:87], v[70:73], v[130:133]\n"
"s_add_u32 m0, s59, 106496\n"
"v_mfma_f32_16x16x32_bf16 v[134:137], a[80:83], v[74:77], v[134:137]\n"
"s_waitcnt lgkmcnt(4)\n"
"ds_read_b128 v[66:69], v4 offset:7168\n"
"v_mfma_f32_16x16x32_bf16 v[138:141], a[84:87], v[74:77], v[138:141]\n"
"buffer_load_dwordx4 v1, s[20:23], m0 offen sc1 lds\n"
"v_mfma_f32_16x16x32_bf16 v[142:145], a[80:83], v[78:81], v[142:145]\n"
"s_waitcnt vmcnt(13) lgkmcnt(4)\n"
"ds_read_b128 v[70:73], v4 offset:8192\n"
"v_mfma_f32_16x16x32_bf16 v[146:149], a[84:87], v[78:81], v[146:149]\n"
"v_mfma_f32_16x16x32_bf16 v[110:113], a[88:91], v[82:85], v[110:113]\n"
"s_waitcnt vmcnt(12) lgkmcnt(4)\n"
"ds_read_b128 v[74:77], v4 offset:9216\n"
"v_mfma_f32_16x16x32_bf16 v[114:117], a[92:95], v[82:85], v[114:117]\n"
"buffer_load_dwordx4 a[132:135], v27, s[96:99], s88 offen sc1\n"
"v_mfma_f32_16x16x32_bf16 v[118:121], a[88:91], v[54:57], v[118:121]\n"
"s_waitcnt lgkmcnt(4)\n"
"ds_read_b128 v[78:81], v4 offset:10240\n"
"v_mfma_f32_16x16x32_bf16 v[122:125], a[92:95], v[54:57], v[122:125]\n"
"v_mfma_f32_16x16x32_bf16 v[126:129], a[88:91], v[58:61], v[126:129]\n"
"s_waitcnt lgkmcnt(4)\n"
"ds_read_b128 v[82:85], v4 offset:11264\n"
"v_mfma_f32_16x16x32_bf16 v[130:133], a[92:95], v[58:61], v[130:133]\n"
"buffer_load_dwordx4 a[136:139], v27, s[96:99], s83 offen offset:1024 sc1\n"
"v_mfma_f32_16x16x32_bf16 v[134:137], a[88:91], v[62:65], v[134:137]\n"
"s_waitcnt lgkmcnt(4)\n"
"ds_read_b128 v[54:57], v4 offset:12288\n"
"v_mfma_f32_16x16x32_bf16 v[138:141], a[92:95], v[62:65], v[138:141]\n"
"v_mfma_f32_16x16x32_bf16 v[142:145], a[88:91], v[66:69], v[142:145]\n"
"s_waitcnt vmcnt(13) lgkmcnt(4)\n"
"ds_read_b128 v[58:61], v4 offset:13312\n"
"v_mfma_f32_16x16x32_bf16 v[146:149], a[92:95], v[66:69], v[146:149]\n"
"buffer_load_dwordx4 a[140:143], v27, s[96:99], s88 offen offset:1024 sc1\n"
"v_mfma_f32_16x16x32_bf16 v[110:113], a[96:99], v[70:73], v[110:113]\n"
"s_waitcnt vmcnt(13) lgkmcnt(4)\n"
"ds_read_b128 v[62:65], v4 offset:14336\n"
"v_mfma_f32_16x16x32_bf16 v[114:117], a[100:103], v[70:73], v[114:117]\n"
"v_mfma_f32_16x16x32_bf16 v[118:121], a[96:99], v[74:77], v[118:121]\n"
"s_waitcnt lgkmcnt(4)\n"
"ds_read_b128 v[66:69], v4 offset:15360\n"
"v_mfma_f32_16x16x32_bf16 v[122:125], a[100:103], v[74:77], v[122:125]\n"
"buffer_load_dwordx4 a[144:147], v27, s[96:99], s83 offen offset:2048 sc1\n"
"v_mfma_f32_16x16x32_bf16 v[126:129], a[96:99], v[78:81], v[126:129]\n"
"s_waitcnt lgkmcnt(4)\n"
"ds_read_b128 v[70:73], v4 offset:16384\n"
"v_mfma_f32_16x16x32_bf16 v[130:133], a[100:103], v[78:81], v[130:133]\n"
"s_add_u32 m0, s59, 110592\n"
"v_mfma_f32_16x16x32_bf16 v[134:137], a[96:99], v[82:85], v[134:137]\n"
"s_waitcnt lgkmcnt(4)\n"
"ds_read_b128 v[74:77], v4 offset:17408\n"
"v_mfma_f32_16x16x32_bf16 v[138:141], a[100:103], v[82:85], v[138:141]\n"
"buffer_load_dwordx4 v1, s[20:23], m0 offen sc1 lds\n"
"v_mfma_f32_16x16x32_bf16 v[142:145], a[96:99], v[54:57], v[142:145]\n"
"s_waitcnt vmcnt(13) lgkmcnt(4)\n"
"ds_read_b128 v[78:81], v4 offset:18432\n"
"v_mfma_f32_16x16x32_bf16 v[146:149], a[100:103], v[54:57], v[146:149]\n"
"v_mfma_f32_16x16x32_bf16 v[110:113], a[104:107], v[58:61], v[110:113]\n"
"s_waitcnt vmcnt(12) lgkmcnt(4)\n"
"ds_read_b128 v[82:85], v4 offset:19456\n"
"v_mfma_f32_16x16x32_bf16 v[114:117], a[108:111], v[58:61], v[114:117]\n"
"v_mfma_f32_16x16x32_bf16 v[118:121], a[104:107], v[62:65], v[118:121]\n"
"s_waitcnt lgkmcnt(4)\n"
"ds_read_b128 v[54:57], v2\n"
"v_mfma_f32_16x16x32_bf16 v[122:125], a[108:111], v[62:65], v[122:125]\n"
"buffer_load_dwordx4 a[148:151], v27, s[96:99], s88 offen offset:2048 sc1\n"
"v_mfma_f32_16x16x32_bf16 v[126:129], a[104:107], v[66:69], v[126:129]\n"
"s_waitcnt lgkmcnt(4)\n"
"ds_read_b128 v[58:61], v2 offset:1024\n"
"v_mfma_f32_16x16x32_bf16 v[130:133], a[108:111], v[66:69], v[130:133]\n"
"v_mfma_f32_16x16x32_bf16 v[134:137], a[104:107], v[70:73], v[134:137]\n"
"s_waitcnt lgkmcnt(4)\n"
"ds_read_b128 v[62:65], v2 offset:2048\n"
"v_mfma_f32_16x16x32_bf16 v[138:141], a[108:111], v[70:73], v[138:141]\n"
"buffer_load_dwordx4 a[152:155], v27, s[96:99], s83 offen offset:3072 sc1\n"
"v_mfma_f32_16x16x32_bf16 v[142:145], a[104:107], v[74:77], v[142:145]\n"
"s_waitcnt vmcnt(13) lgkmcnt(4)\n"
"ds_read_b128 v[66:69], v2 offset:3072\n"
"v_mfma_f32_16x16x32_bf16 v[146:149], a[108:111], v[74:77], v[146:149]\n"
"v_mfma_f32_16x16x32_bf16 v[110:113], a[112:115], v[78:81], v[110:113]\n"
"s_waitcnt vmcnt(12) lgkmcnt(4)\n"
"ds_read_b128 v[70:73], v2 offset:4096\n"
"v_mfma_f32_16x16x32_bf16 v[114:117], a[116:119], v[78:81], v[114:117]\n"
"buffer_load_dwordx4 a[156:159], v27, s[96:99], s88 offen offset:3072 sc1\n"
"v_mfma_f32_16x16x32_bf16 v[118:121], a[112:115], v[82:85], v[118:121]\n"
"s_waitcnt lgkmcnt(4)\n"
"ds_read_b128 v[74:77], v2 offset:5120\n"
"v_mfma_f32_16x16x32_bf16 v[122:125], a[116:119], v[82:85], v[122:125]\n"
"s_add_u32 m0, s59, 114688\n"
"v_mfma_f32_16x16x32_bf16 v[126:129], a[112:115], v[54:57], v[126:129]\n"
"s_waitcnt lgkmcnt(4)\n"
"ds_read_b128 v[78:81], v2 offset:6144\n"
"v_mfma_f32_16x16x32_bf16 v[130:133], a[116:119], v[54:57], v[130:133]\n"
"buffer_load_dwordx4 v1, s[20:23], m0 offen sc1 lds\n"
"v_mfma_f32_16x16x32_bf16 v[134:137], a[112:115], v[58:61], v[134:137]\n"
"ds_read_b128 v[82:85], v2 offset:7168\n"
"v_mfma_f32_16x16x32_bf16 v[138:141], a[116:119], v[58:61], v[138:141]\n"
"s_barrier\n"
"s_waitcnt lgkmcnt(5)\n"
"v_mfma_f32_16x16x32_bf16 v[142:145], a[112:115], v[62:65], v[142:145]\n"
"s_waitcnt vmcnt(12) lgkmcnt(4)\n"
"ds_read_b128 v[54:57], v2 offset:8192\n"
"s_add_u32 m0, s59, 118784\n"
"v_mfma_f32_16x16x32_bf16 v[146:149], a[116:119], v[62:65], v[146:149]\n"
"buffer_load_dwordx4 v1, s[20:23], m0 offen sc1 lds\n"
"v_mfma_f32_16x16x32_bf16 v[110:113], a[120:123], v[66:69], v[110:113]\n"
"s_waitcnt vmcnt(12) lgkmcnt(4)\n"
"ds_read_b128 v[58:61], v2 offset:9216\n"
"v_mfma_f32_16x16x32_bf16 v[114:117], a[124:127], v[66:69], v[114:117]\n"
"s_add_u32 m0, s59, 122880\n"
"v_mfma_f32_16x16x32_bf16 v[118:121], a[120:123], v[70:73], v[118:121]\n"
"s_waitcnt lgkmcnt(4)\n"
"ds_read_b128 v[62:65], v2 offset:10240\n"
"v_mfma_f32_16x16x32_bf16 v[122:125], a[124:127], v[70:73], v[122:125]\n"
"buffer_load_dwordx4 v1, s[20:23], m0 offen sc1 lds\n"
"v_mfma_f32_16x16x32_bf16 v[126:129], a[120:123], v[74:77], v[126:129]\n"
"s_waitcnt lgkmcnt(4)\n"
"ds_read_b128 v[66:69], v2 offset:11264\n"
"v_mfma_f32_16x16x32_bf16 v[130:133], a[124:127], v[74:77], v[130:133]\n"
"s_add_u32 m0, s59, 126976\n"
"v_mfma_f32_16x16x32_bf16 v[134:137], a[120:123], v[78:81], v[134:137]\n"
"s_waitcnt lgkmcnt(4)\n"
"ds_read_b128 v[70:73], v3 offset:4096\n"
"v_mfma_f32_16x16x32_bf16 v[138:141], a[124:127], v[78:81], v[138:141]\n"
"buffer_load_dwordx4 v1, s[20:23], m0 offen sc1 lds\n"
"v_mfma_f32_16x16x32_bf16 v[142:145], a[120:123], v[82:85], v[142:145]\n"
"s_waitcnt vmcnt(13) lgkmcnt(4)\n"
"ds_read_b128 v[74:77], v3 offset:5120\n"
"v_mfma_f32_16x16x32_bf16 v[146:149], a[124:127], v[82:85], v[146:149]\n"
"s_add_u32 m0, s59, 131072\n"
"v_mfma_f32_16x16x32_bf16 v[110:113], a[128:131], v[54:57], v[110:113]\n"
"s_waitcnt vmcnt(11) lgkmcnt(4)\n"
"ds_read_b128 v[78:81], v3 offset:6144\n"
"v_add_f32 v218, v218, v222\n"
"v_mfma_f32_16x16x32_bf16 v[114:117], a[132:135], v[54:57], v[114:117]\n"
"buffer_load_dwordx4 v1, s[20:23], m0 offen sc1 lds\n"
"v_mfma_f32_16x16x32_bf16 v[118:121], a[128:131], v[58:61], v[118:121]\n"
"s_waitcnt lgkmcnt(4)\n"
"ds_read_b128 v[82:85], v3 offset:7168\n"
"v_add_f32 v219, v219, v223\n"
"v_add_f32 v220, v220, v224\n"
"v_mfma_f32_16x16x32_bf16 v[122:125], a[132:135], v[58:61], v[122:125]\n"
"s_add_u32 m0, s59, 12288\n"
"v_add_u32 v1, 122880, v1\n"
"v_add_f32 v221, v221, v225\n"
"v_mfma_f32_16x16x32_bf16 v[126:129], a[128:131], v[62:65], v[126:129]\n"
"s_waitcnt lgkmcnt(4)\n"
"ds_read_b128 v[54:57], v3 offset:8192\n"
"v_permlane32_swap_b32 v218, v220\n"
"v_mfma_f32_16x16x32_bf16 v[130:133], a[132:135], v[62:65], v[130:133]\n"
"buffer_load_dwordx4 v1, s[20:23], m0 offen sc1 lds\n"
"v_mfma_f32_16x16x32_bf16 v[134:137], a[128:131], v[66:69], v[134:137]\n"
"s_waitcnt lgkmcnt(4)\n"
"ds_read_b128 v[58:61], v3 offset:9216\n"
"v_add_f32 v218, v218, v220\n"
"v_mov_b32 v220, v218\n"
"v_mfma_f32_16x16x32_bf16 v[138:141], a[132:135], v[66:69], v[138:141]\n"
"s_add_u32 m0, s59, 16384\n"
"v_permlane32_swap_b32 v219, v221\n"
"v_mfma_f32_16x16x32_bf16 v[142:145], a[128:131], v[70:73], v[142:145]\n"
"s_waitcnt vmcnt(12) lgkmcnt(4)\n"
"ds_read_b128 v[62:65], v3 offset:10240\n"
"v_add_f32 v219, v219, v221\n"
"v_mov_b32 v221, v219\n"
"v_mfma_f32_16x16x32_bf16 v[146:149], a[132:135], v[70:73], v[146:149]\n"
"buffer_load_dwordx4 v1, s[20:23], m0 offen sc1 lds\n"
"v_mfma_f32_16x16x32_bf16 v[110:113], a[136:139], v[74:77], v[110:113]\n"
"s_waitcnt vmcnt(12) lgkmcnt(4)\n"
"ds_read_b128 v[66:69], v3 offset:11264\n"
"v_permlane16_swap_b32 v218, v220\n"
"v_mfma_f32_16x16x32_bf16 v[114:117], a[140:143], v[74:77], v[114:117]\n"
"s_add_u32 m0, s59, 20480\n"
"v_add_f32 v218, v218, v220\n"
"v_mul_f32 v218, 986500301, v218\n"
"v_mfma_f32_16x16x32_bf16 v[118:121], a[136:139], v[78:81], v[118:121]\n"
"s_waitcnt lgkmcnt(4)\n"
"ds_read_b128 v[70:73], v3 offset:12288\n"
"v_permlane16_swap_b32 v219, v221\n"
"v_mfma_f32_16x16x32_bf16 v[122:125], a[140:143], v[78:81], v[122:125]\n"
"buffer_load_dwordx4 v1, s[20:23], m0 offen sc1 lds\n"
"v_mfma_f32_16x16x32_bf16 v[126:129], a[136:139], v[82:85], v[126:129]\n"
"s_waitcnt lgkmcnt(4)\n"
"ds_read_b128 v[74:77], v3 offset:13312\n"
"s_add_u32 m0, s59, 24576\n"
"v_add_f32 v219, v219, v221\n"
"v_mfma_f32_16x16x32_bf16 v[130:133], a[140:143], v[82:85], v[130:133]\n"
"s_add_u32 s60, s65, 4294942720\n"
"v_add_u32 v1, s60, v2\n"
"v_mul_f32 v219, 986500301, v219\n"
"v_mfma_f32_16x16x32_bf16 v[134:137], a[136:139], v[54:57], v[134:137]\n"
"s_waitcnt lgkmcnt(4)\n"
"ds_read_b128 v[78:81], v3 offset:14336\n"
"v_fma_f32 v219, -v218, v218, v219\n"
"v_mfma_f32_16x16x32_bf16 v[138:141], a[140:143], v[54:57], v[138:141]\n"
"buffer_load_dwordx4 v1, s[20:23], m0 offen sc1 lds\n"
"v_max_f32 v219, 0, v219\n"
"v_mfma_f32_16x16x32_bf16 v[142:145], a[136:139], v[58:61], v[142:145]\n"
"s_waitcnt vmcnt(13) lgkmcnt(4)\n"
"ds_read_b128 v[82:85], v3 offset:15360\n"
"s_add_u32 m0, s59, 28672\n"
"v_mfma_f32_16x16x32_bf16 v[146:149], a[140:143], v[58:61], v[146:149]\n"
"buffer_load_dwordx4 v1, s[20:23], m0 offen sc1 lds\n"
"v_add_f32 v219, 925353388, v219\n"
"v_mfma_f32_16x16x32_bf16 v[110:113], a[144:147], v[62:65], v[110:113]\n"
"s_waitcnt vmcnt(12) lgkmcnt(4)\n"
"ds_read_b128 v[54:57], v3 offset:16384\n"
"v_mfma_f32_16x16x32_bf16 v[114:117], a[148:151], v[62:65], v[114:117]\n"
"s_add_u32 m0, s59, 32768\n"
"v_rsq_f32 v219, v219\n"
"v_mfma_f32_16x16x32_bf16 v[118:121], a[144:147], v[66:69], v[118:121]\n"
"s_waitcnt lgkmcnt(4)\n"
"ds_read_b128 v[58:61], v3 offset:17408\n"
"ds_write_b32 v46, v218\n"
"v_mfma_f32_16x16x32_bf16 v[122:125], a[148:151], v[66:69], v[122:125]\n"
"buffer_load_dwordx4 v1, s[20:23], m0 offen sc1 lds\n"
"v_mfma_f32_16x16x32_bf16 v[126:129], a[144:147], v[70:73], v[126:129]\n"
"s_waitcnt lgkmcnt(5)\n"
"ds_read_b128 v[62:65], v3 offset:18432\n"
"ds_write_b32 v46, v219 offset:128\n"
"v_mfma_f32_16x16x32_bf16 v[130:133], a[148:151], v[70:73], v[130:133]\n"
"s_add_u32 m0, s59, 36864\n"
"v_mfma_f32_16x16x32_bf16 v[134:137], a[144:147], v[74:77], v[134:137]\n"
"s_waitcnt lgkmcnt(6)\n"
"ds_read_b128 v[66:69], v3 offset:19456\n"
"v_mfma_f32_16x16x32_bf16 v[138:141], a[148:151], v[74:77], v[138:141]\n"
"buffer_load_dwordx4 v1, s[20:23], m0 offen sc1 lds\n"
"v_mfma_f32_16x16x32_bf16 v[142:145], a[144:147], v[78:81], v[142:145]\n"
"s_add_u32 m0, s59, 40960\n"
"v_mfma_f32_16x16x32_bf16 v[146:149], a[148:151], v[78:81], v[146:149]\n"
"s_waitcnt vmcnt(13) lgkmcnt(6)\n"
"buffer_load_dwordx4 v1, s[20:23], m0 offen sc1 lds\n"
"v_mfma_f32_16x16x32_bf16 v[110:113], a[152:155], v[82:85], v[110:113]\n"
"s_waitcnt vmcnt(13)\n"
"s_add_u32 m0, s59, 45056\n"
"v_mfma_f32_16x16x32_bf16 v[114:117], a[156:159], v[82:85], v[114:117]\n"
"s_waitcnt lgkmcnt(5)\n"
"ds_read_b128 v[158:161], v47 offset:128\n"
"ds_read_b128 v[162:165], v47 offset:192\n"
"v_mfma_f32_16x16x32_bf16 v[118:121], a[152:155], v[54:57], v[118:121]\n"
"ds_read_b128 v[150:153], v47\n"
"ds_read_b128 v[154:157], v47 offset:64\n"
"v_mfma_f32_16x16x32_bf16 v[122:125], a[156:159], v[54:57], v[122:125]\n"
"s_waitcnt lgkmcnt(8)\n"
"buffer_load_dwordx4 v1, s[20:23], m0 offen sc1 lds\n"
"v_mfma_f32_16x16x32_bf16 v[126:129], a[152:155], v[58:61], v[126:129]\n"
"s_add_u32 m0, s59, 49152\n"
"v_mfma_f32_16x16x32_bf16 v[130:133], a[156:159], v[58:61], v[130:133]\n"
"s_waitcnt lgkmcnt(6)\n"
"ds_read_b32 v178, v49 offset:256\n"
"ds_read_b32 v176, v49 offset:128\n"
"v_mfma_f32_16x16x32_bf16 v[134:137], a[152:155], v[62:65], v[134:137]\n"
"ds_read_b32 v177, v49 offset:192\n"
"ds_read_b32 v174, v49\n"
"v_mfma_f32_16x16x32_bf16 v[138:141], a[156:159], v[62:65], v[138:141]\n"
"s_waitcnt lgkmcnt(8)\n"
"buffer_load_dwordx4 v1, s[20:23], m0 offen sc1 lds\n"
"v_mfma_f32_16x16x32_bf16 v[142:145], a[152:155], v[66:69], v[142:145]\n"
"ds_read_b32 v175, v49 offset:64\n"
"v_mfma_f32_16x16x32_bf16 v[146:149], a[156:159], v[66:69], v[146:149]\n"
"s_waitcnt lgkmcnt(8)\n"
"v_pk_mul_f32 v[166:167], v[158:159], s[70:71]\n"
"v_pk_mul_f32 v[168:169], v[160:161], s[70:71]\n"
"s_waitcnt lgkmcnt(7)\n"
"v_pk_mul_f32 v[170:171], v[162:163], s[70:71]\n"
"v_pk_mul_f32 v[172:173], v[164:165], s[70:71]\n"
"s_waitcnt lgkmcnt(4)\n"
"v_pk_fma_f32 v[142:143], v[150:151], v[178:179], v[142:143] op_sel_hi:[1,0,1] neg_lo:[1,0,0] neg_hi:[1,0,0]\n"
"v_pk_mul_f32 v[184:185], v[142:143], v[158:159]\n"
"v_pk_mul_f32 v[180:181], v[142:143], v[166:167]\n"
"v_pk_fma_f32 v[142:143], v[142:143], v[158:159], s[68:69]\n"
"v_exp_f32_e64 v180, v180 clamp\n"
"v_exp_f32_e64 v181, v181 clamp\n"
"v_max_f32 v142, s68, v142\n"
"v_max_f32 v143, s68, v143\n"
"v_pk_add_f32 v[142:143], v[142:143], v[180:181]\n"
"v_cvt_pk_bf16_f32 v234, v142, v143\n"
"v_pk_fma_f32 v[144:145], v[152:153], v[178:179], v[144:145] op_sel_hi:[1,0,1] neg_lo:[1,0,0] neg_hi:[1,0,0]\n"
"v_pk_mul_f32 v[186:187], v[144:145], v[160:161]\n"
"v_pk_mul_f32 v[182:183], v[144:145], v[168:169]\n"
"v_pk_fma_f32 v[144:145], v[144:145], v[160:161], s[68:69]\n"
"v_exp_f32_e64 v182, v182 clamp\n"
"v_exp_f32_e64 v183, v183 clamp\n"
"v_max_f32 v144, s68, v144\n"
"v_max_f32 v145, s68, v145\n"
"v_pk_add_f32 v[144:145], v[144:145], v[182:183]\n"
"v_cvt_pk_bf16_f32 v235, v144, v145\n"
"v_pk_fma_f32 v[146:147], v[154:155], v[178:179], v[146:147] op_sel_hi:[1,0,1] neg_lo:[1,0,0] neg_hi:[1,0,0]\n"
"v_pk_mul_f32 v[188:189], v[146:147], v[162:163]\n"
"v_pk_mul_f32 v[180:181], v[146:147], v[170:171]\n"
"v_pk_fma_f32 v[146:147], v[146:147], v[162:163], s[68:69]\n"
"v_exp_f32_e64 v180, v180 clamp\n"
"v_exp_f32_e64 v181, v181 clamp\n"
"v_max_f32 v146, s68, v146\n"
"v_max_f32 v147, s68, v147\n"
"v_pk_add_f32 v[146:147], v[146:147], v[180:181]\n"
"v_cvt_pk_bf16_f32 v236, v146, v147\n"
"v_pk_fma_f32 v[148:149], v[156:157], v[178:179], v[148:149] op_sel_hi:[1,0,1] neg_lo:[1,0,0] neg_hi:[1,0,0]\n"
"v_pk_mul_f32 v[190:191], v[148:149], v[164:165]\n"
"v_pk_mul_f32 v[182:183], v[148:149], v[172:173]\n"
"v_pk_fma_f32 v[148:149], v[148:149], v[164:165], s[68:69]\n"
"v_exp_f32_e64 v182, v182 clamp\n"
"v_exp_f32_e64 v183, v183 clamp\n"
"v_max_f32 v148, s68, v148\n"
"v_max_f32 v149, s68, v149\n"
"v_pk_add_f32 v[148:149], v[148:149], v[182:183]\n"
"v_cvt_pk_bf16_f32 v237, v148, v149\n"
"v_mov_b32 v192, 1065369472\n"
"v_cvt_pk_bf16_f32 v238, v184, v185\n"
"v_cndmask_b32 v238, v238, v192, s[74:75]\n"
"v_cvt_pk_bf16_f32 v239, v186, v187\n"
"v_cndmask_b32 v239, v239, v192, s[74:75]\n"
"v_cvt_pk_bf16_f32 v240, v188, v189\n"
"v_cndmask_b32 v240, v240, v192, s[74:75]\n"
"v_cvt_pk_bf16_f32 v241, v190, v191\n"
"v_cndmask_b32 v241, v241, v192, s[74:75]\n"
"s_waitcnt lgkmcnt(2)\n"
"v_pk_fma_f32 v[126:127], v[150:151], v[176:177], v[126:127] op_sel_hi:[1,0,1] neg_lo:[1,0,0] neg_hi:[1,0,0]\n"
"v_pk_mul_f32 v[126:127], v[126:127], v[158:159]\n"
"v_cvt_pk_bf16_f32 v226, v126, v127\n"
"v_pk_fma_f32 v[128:129], v[152:153], v[176:177], v[128:129] op_sel_hi:[1,0,1] neg_lo:[1,0,0] neg_hi:[1,0,0]\n"
"v_pk_mul_f32 v[128:129], v[128:129], v[160:161]\n"
"v_cvt_pk_bf16_f32 v227, v128, v129\n"
"v_pk_fma_f32 v[130:131], v[154:155], v[176:177], v[130:131] op_sel_hi:[1,0,1] neg_lo:[1,0,0] neg_hi:[1,0,0]\n"
"v_pk_mul_f32 v[130:131], v[130:131], v[162:163]\n"
"v_cvt_pk_bf16_f32 v228, v130, v131\n"
"v_pk_fma_f32 v[132:133], v[156:157], v[176:177], v[132:133] op_sel_hi:[1,0,1] neg_lo:[1,0,0] neg_hi:[1,0,0]\n"
"v_pk_mul_f32 v[132:133], v[132:133], v[164:165]\n"
"v_cvt_pk_bf16_f32 v229, v132, v133\n"
"v_pk_fma_f32 v[134:135], v[150:151], v[176:177], v[134:135] op_sel:[0,1,0] neg_lo:[1,0,0] neg_hi:[1,0,0]\n"
"v_pk_mul_f32 v[134:135], v[134:135], v[158:159]\n"
"v_cvt_pk_bf16_f32 v230, v134, v135\n"
"v_pk_fma_f32 v[136:137], v[152:153], v[176:177], v[136:137] op_sel:[0,1,0] neg_lo:[1,0,0] neg_hi:[1,0,0]\n"
"v_pk_mul_f32 v[136:137], v[136:137], v[160:161]\n"
"v_cvt_pk_bf16_f32 v231, v136, v137\n"
"v_pk_fma_f32 v[138:139], v[154:155], v[176:177], v[138:139] op_sel:[0,1,0] neg_lo:[1,0,0] neg_hi:[1,0,0]\n"
"v_pk_mul_f32 v[138:139], v[138:139], v[162:163]\n"
"v_cvt_pk_bf16_f32 v232, v138, v139\n"
"v_pk_fma_f32 v[140:141], v[156:157], v[176:177], v[140:141] op_sel:[0,1,0] neg_lo:[1,0,0] neg_hi:[1,0,0]\n"
"v_pk_mul_f32 v[140:141], v[140:141], v[164:165]\n"
"v_cvt_pk_bf16_f32 v233, v140, v141\n"
"v_mfma_f32_16x16x32_bf16 v[142:145], v[234:237], v[226:229], 0\n"
"s_nop 0\n"
"v_mfma_f32_16x16x32_bf16 v[146:149], v[234:237], v[230:233], 0\n"
"v_mfma_f32_16x16x32_bf16 v[126:129], v[234:237], v[238:241], 0\n"
"s_waitcnt lgkmcnt(0)\n"
"v_pk_fma_f32 v[110:111], v[150:151], v[174:175], v[110:111] op_sel_hi:[1,0,1] neg_lo:[1,0,0] neg_hi:[1,0,0]\n"
"v_pk_mul_f32 v[180:181], v[110:111], v[166:167]\n"
"v_pk_fma_f32 v[110:111], v[110:111], v[158:159], s[68:69]\n"
"v_exp_f32_e64 v180, v180 clamp\n"
"v_exp_f32_e64 v181, v181 clamp\n"
"v_max_f32 v110, s68, v110\n"
"v_max_f32 v111, s68, v111\n"
"v_pk_add_f32 v[110:111], v[110:111], v[180:181]\n"
"v_cvt_pk_bf16_f32 v218, v110, v111\n"
"v_pk_fma_f32 v[112:113], v[152:153], v[174:175], v[112:113] op_sel_hi:[1,0,1] neg_lo:[1,0,0] neg_hi:[1,0,0]\n"
"v_pk_mul_f32 v[182:183], v[112:113], v[168:169]\n"
"v_pk_fma_f32 v[112:113], v[112:113], v[160:161], s[68:69]\n"
"v_exp_f32_e64 v182, v182 clamp\n"
"v_exp_f32_e64 v183, v183 clamp\n"
"v_max_f32 v112, s68, v112\n"
"v_max_f32 v113, s68, v113\n"
"v_pk_add_f32 v[112:113], v[112:113], v[182:183]\n"
"v_cvt_pk_bf16_f32 v219, v112, v113\n"
"v_pk_fma_f32 v[114:115], v[154:155], v[174:175], v[114:115] op_sel_hi:[1,0,1] neg_lo:[1,0,0] neg_hi:[1,0,0]\n"
"v_pk_mul_f32 v[180:181], v[114:115], v[170:171]\n"
"v_pk_fma_f32 v[114:115], v[114:115], v[162:163], s[68:69]\n"
"v_exp_f32_e64 v180, v180 clamp\n"
"v_exp_f32_e64 v181, v181 clamp\n"
"v_max_f32 v114, s68, v114\n"
"v_max_f32 v115, s68, v115\n"
"v_pk_add_f32 v[114:115], v[114:115], v[180:181]\n"
"v_cvt_pk_bf16_f32 v220, v114, v115\n"
"v_pk_fma_f32 v[116:117], v[156:157], v[174:175], v[116:117] op_sel_hi:[1,0,1] neg_lo:[1,0,0] neg_hi:[1,0,0]\n"
"v_pk_mul_f32 v[182:183], v[116:117], v[172:173]\n"
"v_pk_fma_f32 v[116:117], v[116:117], v[164:165], s[68:69]\n"
"v_exp_f32_e64 v182, v182 clamp\n"
"v_exp_f32_e64 v183, v183 clamp\n"
"v_max_f32 v116, s68, v116\n"
"v_max_f32 v117, s68, v117\n"
"v_pk_add_f32 v[116:117], v[116:117], v[182:183]\n"
"v_cvt_pk_bf16_f32 v221, v116, v117\n"
"ds_write_b128 v18, v[142:145]\n"
"ds_write_b128 v19, v[146:149]\n"
"ds_write_b128 v20, v[126:129]\n"
"v_mfma_f32_16x16x32_bf16 v[130:133], v[218:221], v[226:229], 0\n"
"v_mfma_f32_16x16x32_bf16 v[134:137], v[218:221], v[238:241], 0\n"
"v_pk_fma_f32 v[118:119], v[150:151], v[174:175], v[118:119] op_sel:[0,1,0] neg_lo:[1,0,0] neg_hi:[1,0,0]\n"
"v_pk_mul_f32 v[180:181], v[118:119], v[166:167]\n"
"v_pk_fma_f32 v[118:119], v[118:119], v[158:159], s[68:69]\n"
"v_exp_f32_e64 v180, v180 clamp\n"
"v_exp_f32_e64 v181, v181 clamp\n"
"v_max_f32 v118, s68, v118\n"
"v_max_f32 v119, s68, v119\n"
"v_pk_add_f32 v[118:119], v[118:119], v[180:181]\n"
"v_cvt_pk_bf16_f32 v222, v118, v119\n"
"v_pk_fma_f32 v[120:121], v[152:153], v[174:175], v[120:121] op_sel:[0,1,0] neg_lo:[1,0,0] neg_hi:[1,0,0]\n"
"v_pk_mul_f32 v[182:183], v[120:121], v[168:169]\n"
"v_pk_fma_f32 v[120:121], v[120:121], v[160:161], s[68:69]\n"
"v_exp_f32_e64 v182, v182 clamp\n"
"v_exp_f32_e64 v183, v183 clamp\n"
"v_max_f32 v120, s68, v120\n"
"v_max_f32 v121, s68, v121\n"
"v_pk_add_f32 v[120:121], v[120:121], v[182:183]\n"
"v_cvt_pk_bf16_f32 v223, v120, v121\n"
"v_pk_fma_f32 v[122:123], v[154:155], v[174:175], v[122:123] op_sel:[0,1,0] neg_lo:[1,0,0] neg_hi:[1,0,0]\n"
"v_pk_mul_f32 v[180:181], v[122:123], v[170:171]\n"
"v_pk_fma_f32 v[122:123], v[122:123], v[162:163], s[68:69]\n"
"v_exp_f32_e64 v180, v180 clamp\n"
"v_exp_f32_e64 v181, v181 clamp\n"
"v_max_f32 v122, s68, v122\n"
"v_max_f32 v123, s68, v123\n"
"v_pk_add_f32 v[122:123], v[122:123], v[180:181]\n"
"v_cvt_pk_bf16_f32 v224, v122, v123\n"
"v_pk_fma_f32 v[124:125], v[156:157], v[174:175], v[124:125] op_sel:[0,1,0] neg_lo:[1,0,0] neg_hi:[1,0,0]\n"
"v_pk_mul_f32 v[182:183], v[124:125], v[172:173]\n"
"v_pk_fma_f32 v[124:125], v[124:125], v[164:165], s[68:69]\n"
"v_exp_f32_e64 v182, v182 clamp\n"
"v_exp_f32_e64 v183, v183 clamp\n"
"v_max_f32 v124, s68, v124\n"
"v_max_f32 v125, s68, v125\n"
"v_pk_add_f32 v[124:125], v[124:125], v[182:183]\n"
"v_cvt_pk_bf16_f32 v225, v124, v125\n"
"s_nop 1\n"
"v_mfma_f32_16x16x32_bf16 v[110:113], v[222:225], v[230:233], 0\n"
"v_mfma_f32_16x16x32_bf16 v[114:117], v[222:225], v[238:241], 0\n"
"ds_write_b128 v15, v[130:133]\n"
"ds_write_b128 v16, v[134:137]\n"
"s_nop 4\n"
"ds_write_b128 v15, v[110:113] offset:1680\n"
"ds_write_b128 v17, v[114:117]\n"
"s_waitcnt lgkmcnt(0)\n"
"s_barrier\n"
"ds_read_b128 v[218:221], v45\n"
"ds_read_b128 v[222:225], v45 offset:4096\n"
"ds_read_b128 v[226:229], v45 offset:8192\n"
"ds_read_b128 v[230:233], v45 offset:12288\n"
"ds_read_b128 v[54:57], v3 offset:20480\n"
"ds_read_b128 v[58:61], v3 offset:21504\n"
"ds_read_b128 v[62:65], v3 offset:22528\n"
"ds_read_b128 v[66:69], v3 offset:23552\n"
"ds_read_b128 v[70:73], v3 offset:24576\n"
"ds_read_b128 v[74:77], v3 offset:25600\n"
"s_waitcnt lgkmcnt(8)\n"
"v_pk_add_f32 v[218:219], v[218:219], v[222:223]\n"
"v_pk_add_f32 v[220:221], v[220:221], v[224:225]\n"
"s_waitcnt lgkmcnt(6)\n"
"v_pk_add_f32 v[226:227], v[226:227], v[230:231]\n"
"v_pk_add_f32 v[228:229], v[228:229], v[232:233]\n"
"v_pk_add_f32 v[218:219], v[218:219], v[226:227]\n"
"v_pk_add_f32 v[220:221], v[220:221], v[228:229]\n"
"v_cvt_pk_bf16_f32 v218, v218, v219\n"
"v_cvt_pk_bf16_f32 v219, v220, v221\n"
"buffer_store_dwordx2 v[218:219], v21, s[96:99], s78 offen sc1\n"
"s_waitcnt lgkmcnt(5)\n"
"v_mfma_f32_16x16x32_bf16 v[86:89], v[54:57], a[0:3], 0\n"
"s_waitcnt lgkmcnt(4)\n"
"ds_read_b128 v[78:81], v3 offset:26624\n"
"v_mfma_f32_16x16x32_bf16 v[90:93], v[54:57], a[4:7], 0\n"
"s_add_u32 m0, s59, 53248\n"
"v_add_u32 v1, 20480, v1\n"
"v_mfma_f32_16x16x32_bf16 v[94:97], v[58:61], a[0:3], 0\n"
"s_waitcnt lgkmcnt(4)\n"
"ds_read_b128 v[82:85], v3 offset:27648\n"
"v_and_b32 v110, 1, v0\n"
"v_mfma_f32_16x16x32_bf16 v[98:101], v[58:61], a[4:7], 0\n"
"buffer_load_dwordx4 v1, s[20:23], m0 offen sc1 lds\n"
"v_mfma_f32_16x16x32_bf16 v[102:105], v[62:65], a[0:3], 0\n"
"s_waitcnt lgkmcnt(4)\n"
"ds_read_b128 v[54:57], v3 offset:28672\n"
"v_cmp_eq_u32 vcc, 0, v110\n"
"v_mfma_f32_16x16x32_bf16 v[106:109], v[62:65], a[4:7], 0\n"
"s_add_u32 m0, s59, 57344\n"
"v_mov_b32 v111, 1065353216\n"
"v_mov_b32 v112, 16256\n"
"v_mfma_f32_16x16x32_bf16 v[86:89], v[66:69], a[8:11], v[86:89]\n"
"s_waitcnt lgkmcnt(4)\n"
"ds_read_b128 v[58:61], v3 offset:29696\n"
"v_cndmask_b32 v238, v111, v112, vcc\n"
"v_mfma_f32_16x16x32_bf16 v[90:93], v[66:69], a[12:15], v[90:93]\n"
"v_lshrrev_b32 v114, 2, v0\n"
"v_and_b32 v113, 4, v114\n"
"v_mfma_f32_16x16x32_bf16 v[94:97], v[70:73], a[8:11], v[94:97]\n"
"s_waitcnt lgkmcnt(4)\n"
"ds_read_b128 v[62:65], v3 offset:30720\n"
"v_and_b32 v116, 15, v0\n"
"v_mfma_f32_16x16x32_bf16 v[98:101], v[70:73], a[12:15], v[98:101]\n"
"v_lshrrev_b32 v115, 1, v116\n"
"v_xor_b32 v239, v113, v115\n"
"v_mfma_f32_16x16x32_bf16 v[102:105], v[74:77], a[8:11], v[102:105]\n"
"s_waitcnt lgkmcnt(4)\n"
"ds_read_b128 v[66:69], v3 offset:31744\n"
"v_cmp_eq_u32 vcc, 0, v239\n"
"v_mfma_f32_16x16x32_bf16 v[106:109], v[74:77], a[12:15], v[106:109]\n"
"buffer_load_dwordx4 v1, s[20:23], m0 offen sc1 lds\n"
"v_mfma_f32_16x16x32_bf16 v[86:89], v[78:81], a[16:19], v[86:89]\n"
"s_waitcnt lgkmcnt(4)\n"
"ds_read_b128 v[70:73], v3 offset:32768\n"
"v_cndmask_b32 v234, 0, v238, vcc\n"
"v_mfma_f32_16x16x32_bf16 v[90:93], v[78:81], a[20:23], v[90:93]\n"
"s_add_u32 m0, s59, 61440\n"
"v_cmp_eq_u32 vcc, 1, v239\n"
"v_mfma_f32_16x16x32_bf16 v[94:97], v[82:85], a[16:19], v[94:97]\n"
"s_waitcnt lgkmcnt(4)\n"
"ds_read_b128 v[74:77], v3 offset:33792\n"
"v_mfma_f32_16x16x32_bf16 v[98:101], v[82:85], a[20:23], v[98:101]\n"
"v_cndmask_b32 v235, 0, v238, vcc\n"
"v_cmp_eq_u32 vcc, 2, v239\n"
"v_mfma_f32_16x16x32_bf16 v[102:105], v[54:57], a[16:19], v[102:105]\n"
"s_waitcnt lgkmcnt(4)\n"
"ds_read_b128 v[78:81], v3 offset:34816\n"
"v_mfma_f32_16x16x32_bf16 v[106:109], v[54:57], a[20:23], v[106:109]\n"
"v_cndmask_b32 v236, 0, v238, vcc\n"
"v_cmp_eq_u32 vcc, 3, v239\n"
"v_mfma_f32_16x16x32_bf16 v[86:89], v[58:61], a[24:27], v[86:89]\n"
"s_waitcnt lgkmcnt(4)\n"
"ds_read_b128 v[82:85], v3 offset:35840\n"
"v_mfma_f32_16x16x32_bf16 v[90:93], v[58:61], a[28:31], v[90:93]\n"
"buffer_load_dwordx4 v1, s[20:23], m0 offen sc1 lds\n"
"v_mfma_f32_16x16x32_bf16 v[94:97], v[62:65], a[24:27], v[94:97]\n"
"s_waitcnt lgkmcnt(4)\n"
"ds_read_b128 v[54:57], v3 offset:36864\n"
"v_cndmask_b32 v237, 0, v238, vcc\n"
"v_mfma_f32_16x16x32_bf16 v[98:101], v[62:65], a[28:31], v[98:101]\n"
"v_mfma_f32_16x16x32_bf16 v[102:105], v[66:69], a[24:27], v[102:105]\n"
"s_waitcnt lgkmcnt(4)\n"
"ds_read_b128 v[58:61], v3 offset:37888\n"
"v_mfma_f32_16x16x32_bf16 v[106:109], v[66:69], a[28:31], v[106:109]\n"
"v_mfma_f32_16x16x32_bf16 v[86:89], v[70:73], a[32:35], v[86:89]\n"
"ds_read_b128 v[62:65], v3 offset:38912\n"
"s_mov_b32 s3, 1179648\n"
"s_waitcnt vmcnt(3)\n"
"v_mfma_f32_16x16x32_bf16 v[90:93], v[70:73], a[36:39], v[90:93]\n"
"s_waitcnt lgkmcnt(5)\n"
"v_mfma_f32_16x16x32_bf16 v[94:97], v[74:77], a[32:35], v[94:97]\n"
"ds_read_b128 v[66:69], v3 offset:39936\n"
"buffer_store_dwordx2 v[8:9], v14, s[20:23], s3 offen sc1\n"
"v_mfma_f32_16x16x32_bf16 v[98:101], v[74:77], a[36:39], v[98:101]\n"
"s_barrier\n"
"s_waitcnt lgkmcnt(5)\n"
"v_mfma_f32_16x16x32_bf16 v[102:105], v[78:81], a[32:35], v[102:105]\n"
"s_waitcnt lgkmcnt(4)\n"
"ds_read_b128 v[70:73], v3 offset:40960\n"
"v_mfma_f32_16x16x32_bf16 v[106:109], v[78:81], a[36:39], v[106:109]\n"
"s_add_u32 m0, s59, 135168\n"
"v_add_u32 v1, 4294864896, v1\n"
"v_mfma_f32_16x16x32_bf16 v[86:89], v[82:85], a[40:43], v[86:89]\n"
"s_waitcnt lgkmcnt(4)\n"
"ds_read_b128 v[74:77], v3 offset:41984\n"
"v_mfma_f32_16x16x32_bf16 v[90:93], v[82:85], a[44:47], v[90:93]\n"
"buffer_load_dwordx4 v1, s[20:23], m0 offen sc1 lds\n"
"v_mfma_f32_16x16x32_bf16 v[94:97], v[54:57], a[40:43], v[94:97]\n"
"s_waitcnt lgkmcnt(4)\n"
"ds_read_b128 v[78:81], v3 offset:43008\n"
"v_mfma_f32_16x16x32_bf16 v[98:101], v[54:57], a[44:47], v[98:101]\n"
"s_add_u32 m0, s59, 139264\n"
"v_mfma_f32_16x16x32_bf16 v[102:105], v[58:61], a[40:43], v[102:105]\n"
"s_waitcnt lgkmcnt(4)\n"
"ds_read_b128 v[82:85], v3 offset:44032\n"
"v_mfma_f32_16x16x32_bf16 v[106:109], v[58:61], a[44:47], v[106:109]\n"
"buffer_load_dwordx4 v1, s[20:23], m0 offen sc1 lds\n"
"v_mfma_f32_16x16x32_bf16 v[86:89], v[62:65], a[48:51], v[86:89]\n"
"s_waitcnt lgkmcnt(4)\n"
"ds_read_b128 v[54:57], v3 offset:45056\n"
"v_mfma_f32_16x16x32_bf16 v[90:93], v[62:65], a[52:55], v[90:93]\n"
"s_add_u32 m0, s59, 143360\n"
"v_mfma_f32_16x16x32_bf16 v[94:97], v[66:69], a[48:51], v[94:97]\n"
"s_waitcnt lgkmcnt(4)\n"
"ds_read_b128 v[58:61], v3 offset:46080\n"
"v_mfma_f32_16x16x32_bf16 v[98:101], v[66:69], a[52:55], v[98:101]\n"
"buffer_load_dwordx4 v1, s[20:23], m0 offen sc1 lds\n"
"v_mfma_f32_16x16x32_bf16 v[102:105], v[70:73], a[48:51], v[102:105]\n"
"s_waitcnt lgkmcnt(4)\n"
"ds_read_b128 v[62:65], v3 offset:47104\n"
"v_mfma_f32_16x16x32_bf16 v[106:109], v[70:73], a[52:55], v[106:109]\n"
"s_add_u32 m0, s59, 147456\n"
"v_mfma_f32_16x16x32_bf16 v[86:89], v[74:77], a[56:59], v[86:89]\n"
"s_waitcnt lgkmcnt(4)\n"
"ds_read_b128 v[66:69], v3 offset:48128\n"
"v_mfma_f32_16x16x32_bf16 v[90:93], v[74:77], a[60:63], v[90:93]\n"
"buffer_load_dwordx4 v1, s[20:23], m0 offen sc1 lds\n"
"v_mfma_f32_16x16x32_bf16 v[94:97], v[78:81], a[56:59], v[94:97]\n"
"s_waitcnt lgkmcnt(4)\n"
"ds_read_b128 v[70:73], v3 offset:49152\n"
"v_mfma_f32_16x16x32_bf16 v[98:101], v[78:81], a[60:63], v[98:101]\n"
"s_add_u32 m0, s59, 151552\n"
"v_mfma_f32_16x16x32_bf16 v[102:105], v[82:85], a[56:59], v[102:105]\n"
"s_waitcnt lgkmcnt(4)\n"
"ds_read_b128 v[74:77], v3 offset:50176\n"
"v_mfma_f32_16x16x32_bf16 v[106:109], v[82:85], a[60:63], v[106:109]\n"
"buffer_load_dwordx4 v1, s[20:23], m0 offen sc1 lds\n"
"v_mfma_f32_16x16x32_bf16 v[86:89], v[54:57], a[64:67], v[86:89]\n"
"s_waitcnt lgkmcnt(4)\n"
"ds_read_b128 v[78:81], v3 offset:51200\n"
"v_mfma_f32_16x16x32_bf16 v[90:93], v[54:57], a[68:71], v[90:93]\n"
"s_add_u32 m0, s59, 0\n"
"v_add_u32 v1, 167936, v1\n"
"v_mfma_f32_16x16x32_bf16 v[94:97], v[58:61], a[64:67], v[94:97]\n"
"s_waitcnt lgkmcnt(4)\n"
"ds_read_b128 v[82:85], v3 offset:52224\n"
"v_mfma_f32_16x16x32_bf16 v[98:101], v[58:61], a[68:71], v[98:101]\n"
"buffer_load_dwordx4 v1, s[20:23], m0 offen sc1 lds\n"
"v_mfma_f32_16x16x32_bf16 v[102:105], v[62:65], a[64:67], v[102:105]\n"
"s_waitcnt lgkmcnt(4)\n"
"ds_read_b128 v[54:57], v3 offset:53248\n"
"v_mfma_f32_16x16x32_bf16 v[106:109], v[62:65], a[68:71], v[106:109]\n"
"s_add_u32 m0, s59, 4096\n"
"v_mfma_f32_16x16x32_bf16 v[86:89], v[66:69], a[72:75], v[86:89]\n"
"s_waitcnt lgkmcnt(4)\n"
"ds_read_b128 v[58:61], v3 offset:54272\n"
"v_mfma_f32_16x16x32_bf16 v[90:93], v[66:69], a[76:79], v[90:93]\n"
"buffer_load_dwordx4 v1, s[20:23], m0 offen sc1 lds\n"
"v_mfma_f32_16x16x32_bf16 v[94:97], v[70:73], a[72:75], v[94:97]\n"
"s_waitcnt lgkmcnt(4)\n"
"ds_read_b128 v[62:65], v3 offset:55296\n"
"v_mfma_f32_16x16x32_bf16 v[98:101], v[70:73], a[76:79], v[98:101]\n"
"s_add_u32 m0, s59, 8192\n"
"v_mfma_f32_16x16x32_bf16 v[102:105], v[74:77], a[72:75], v[102:105]\n"
"s_waitcnt lgkmcnt(4)\n"
"ds_read_b128 v[66:69], v3 offset:56320\n"
"v_mfma_f32_16x16x32_bf16 v[106:109], v[74:77], a[76:79], v[106:109]\n"
"buffer_load_dwordx4 v1, s[20:23], m0 offen sc1 lds\n"
"v_mfma_f32_16x16x32_bf16 v[86:89], v[78:81], a[80:83], v[86:89]\n"
"s_waitcnt lgkmcnt(4)\n"
"ds_read_b128 v[70:73], v3 offset:57344\n"
"v_mfma_f32_16x16x32_bf16 v[90:93], v[78:81], a[84:87], v[90:93]\n"
"s_add_u32 m0, s59, 65536\n"
"v_add_u32 v1, 4294914048, v1\n"
"v_mfma_f32_16x16x32_bf16 v[94:97], v[82:85], a[80:83], v[94:97]\n"
"s_waitcnt lgkmcnt(4)\n"
"ds_read_b128 v[74:77], v3 offset:58368\n"
"v_mfma_f32_16x16x32_bf16 v[98:101], v[82:85], a[84:87], v[98:101]\n"
"buffer_load_dwordx4 v1, s[20:23], m0 offen sc1 lds\n"
"v_mfma_f32_16x16x32_bf16 v[102:105], v[54:57], a[80:83], v[102:105]\n"
"s_waitcnt lgkmcnt(4)\n"
"ds_read_b128 v[78:81], v3 offset:59392\n"
"v_mfma_f32_16x16x32_bf16 v[106:109], v[54:57], a[84:87], v[106:109]\n"
"s_add_u32 m0, s59, 69632\n"
"v_mfma_f32_16x16x32_bf16 v[86:89], v[58:61], a[88:91], v[86:89]\n"
"ds_read_b128 v[82:85], v3 offset:60416\n"
"v_mfma_f32_16x16x32_bf16 v[90:93], v[58:61], a[92:95], v[90:93]\n"
"s_barrier\n"
"s_waitcnt lgkmcnt(5)\n"
"v_mfma_f32_16x16x32_bf16 v[94:97], v[62:65], a[88:91], v[94:97]\n"
"s_waitcnt lgkmcnt(4)\n"
"ds_read_b128 v[54:57], v3 offset:61440\n"
"v_mfma_f32_16x16x32_bf16 v[98:101], v[62:65], a[92:95], v[98:101]\n"
"buffer_load_dwordx4 v1, s[20:23], m0 offen sc1 lds\n"
"v_mfma_f32_16x16x32_bf16 v[102:105], v[66:69], a[88:91], v[102:105]\n"
"s_waitcnt lgkmcnt(4)\n"
"ds_read_b128 v[58:61], v3 offset:62464\n"
"s_add_u32 m0, s59, 73728\n"
"v_mfma_f32_16x16x32_bf16 v[106:109], v[66:69], a[92:95], v[106:109]\n"
"buffer_load_dwordx4 v1, s[20:23], m0 offen sc1 lds\n"
"v_mfma_f32_16x16x32_bf16 v[86:89], v[70:73], a[96:99], v[86:89]\n"
"s_waitcnt lgkmcnt(4)\n"
"ds_read_b128 v[62:65], v3 offset:63488\n"
"v_mfma_f32_16x16x32_bf16 v[90:93], v[70:73], a[100:103], v[90:93]\n"
"s_add_u32 m0, s59, 77824\n"
"v_mfma_f32_16x16x32_bf16 v[94:97], v[74:77], a[96:99], v[94:97]\n"
"s_waitcnt lgkmcnt(4)\n"
"ds_read_b128 v[66:69], v3 offset:64512\n"
"v_mfma_f32_16x16x32_bf16 v[98:101], v[74:77], a[100:103], v[98:101]\n"
"buffer_load_dwordx4 v1, s[20:23], m0 offen sc1 lds\n"
"v_mfma_f32_16x16x32_bf16 v[102:105], v[78:81], a[96:99], v[102:105]\n"
"s_waitcnt lgkmcnt(4)\n"
"ds_read_b128 v[70:73], v4\n"
"v_mfma_f32_16x16x32_bf16 v[106:109], v[78:81], a[100:103], v[106:109]\n"
"s_add_u32 m0, s59, 81920\n"
"v_mfma_f32_16x16x32_bf16 v[86:89], v[82:85], a[104:107], v[86:89]\n"
"s_waitcnt lgkmcnt(4)\n"
"ds_read_b128 v[74:77], v4 offset:1024\n"
"v_mfma_f32_16x16x32_bf16 v[90:93], v[82:85], a[108:111], v[90:93]\n"
"buffer_load_dwordx4 v1, s[20:23], m0 offen sc1 lds\n"
"v_mfma_f32_16x16x32_bf16 v[94:97], v[54:57], a[104:107], v[94:97]\n"
"s_waitcnt lgkmcnt(4)\n"
"ds_read_b128 v[78:81], v4 offset:2048\n"
"v_mfma_f32_16x16x32_bf16 v[98:101], v[54:57], a[108:111], v[98:101]\n"
"s_add_u32 m0, s59, 86016\n"
"v_mfma_f32_16x16x32_bf16 v[102:105], v[58:61], a[104:107], v[102:105]\n"
"s_waitcnt lgkmcnt(4)\n"
"ds_read_b128 v[82:85], v4 offset:3072\n"
"v_mfma_f32_16x16x32_bf16 v[106:109], v[58:61], a[108:111], v[106:109]\n"
"buffer_load_dwordx4 v1, s[20:23], m0 offen sc1 lds\n"
"v_mfma_f32_16x16x32_bf16 v[86:89], v[62:65], a[112:115], v[86:89]\n"
"s_waitcnt lgkmcnt(4)\n"
"ds_read_b128 v[54:57], v2 offset:12288\n"
"v_mfma_f32_16x16x32_bf16 v[90:93], v[62:65], a[116:119], v[90:93]\n"
"s_add_u32 m0, s59, 90112\n"
"v_mfma_f32_16x16x32_bf16 v[94:97], v[66:69], a[112:115], v[94:97]\n"
"s_waitcnt lgkmcnt(4)\n"
"ds_read_b128 v[58:61], v2 offset:13312\n"
"v_mfma_f32_16x16x32_bf16 v[98:101], v[66:69], a[116:119], v[98:101]\n"
"buffer_load_dwordx4 v1, s[20:23], m0 offen sc1 lds\n"
"v_mfma_f32_16x16x32_bf16 v[102:105], v[70:73], a[112:115], v[102:105]\n"
"s_waitcnt lgkmcnt(4)\n"
"ds_read_b128 v[62:65], v2 offset:14336\n"
"v_mfma_f32_16x16x32_bf16 v[106:109], v[70:73], a[116:119], v[106:109]\n"
"s_add_u32 m0, s59, 94208\n"
"s_add_u32 s60, s66, 4294873088\n"
"v_add_u32 v1, s60, v2\n"
"v_mfma_f32_16x16x32_bf16 v[86:89], v[74:77], a[120:123], v[86:89]\n"
"s_waitcnt lgkmcnt(4)\n"
"ds_read_b128 v[66:69], v2 offset:15360\n"
"v_mfma_f32_16x16x32_bf16 v[90:93], v[74:77], a[124:127], v[90:93]\n"
"buffer_load_dwordx4 v1, s[20:23], m0 offen sc1 lds\n"
"v_mfma_f32_16x16x32_bf16 v[94:97], v[78:81], a[120:123], v[94:97]\n"
"s_waitcnt lgkmcnt(4)\n"
"ds_read_b128 v[70:73], v2 offset:16384\n"
"v_mfma_f32_16x16x32_bf16 v[98:101], v[78:81], a[124:127], v[98:101]\n"
"v_mfma_f32_16x16x32_bf16 v[102:105], v[82:85], a[120:123], v[102:105]\n"
"s_waitcnt lgkmcnt(4)\n"
"ds_read_b128 v[74:77], v2 offset:17408\n"
"v_mfma_f32_16x16x32_bf16 v[106:109], v[82:85], a[124:127], v[106:109]\n"
"v_mfma_f32_16x16x32_bf16 v[86:89], v[54:57], a[128:131], v[86:89]\n"
"s_waitcnt lgkmcnt(4)\n"
"ds_read_b128 v[78:81], v2 offset:18432\n"
"v_mfma_f32_16x16x32_bf16 v[90:93], v[54:57], a[132:135], v[90:93]\n"
"v_mfma_f32_16x16x32_bf16 v[94:97], v[58:61], a[128:131], v[94:97]\n"
"s_waitcnt lgkmcnt(4)\n"
"ds_read_b128 v[82:85], v2 offset:19456\n"
"v_mfma_f32_16x16x32_bf16 v[98:101], v[58:61], a[132:135], v[98:101]\n"
"v_mfma_f32_16x16x32_bf16 v[102:105], v[62:65], a[128:131], v[102:105]\n"
"s_waitcnt lgkmcnt(4)\n"
"ds_read_b128 v[54:57], v2 offset:20480\n"
"ds_read_b32 v174, v48\n"
"v_mfma_f32_16x16x32_bf16 v[106:109], v[62:65], a[132:135], v[106:109]\n"
"v_mfma_f32_16x16x32_bf16 v[86:89], v[66:69], a[136:139], v[86:89]\n"
"s_waitcnt lgkmcnt(5)\n"
"ds_read_b128 v[58:61], v2 offset:21504\n"
"ds_read_b32 v175, v48 offset:64\n"
"v_mfma_f32_16x16x32_bf16 v[90:93], v[66:69], a[140:143], v[90:93]\n"
"v_mfma_f32_16x16x32_bf16 v[94:97], v[70:73], a[136:139], v[94:97]\n"
"s_waitcnt lgkmcnt(6)\n"
"ds_read_b128 v[62:65], v2 offset:22528\n"
"v_mfma_f32_16x16x32_bf16 v[98:101], v[70:73], a[140:143], v[98:101]\n"
"ds_read_b32 v176, v48 offset:128\n"
"v_mfma_f32_16x16x32_bf16 v[102:105], v[74:77], a[136:139], v[102:105]\n"
"s_waitcnt lgkmcnt(7)\n"
"ds_read_b128 v[66:69], v2 offset:23552\n"
"v_mfma_f32_16x16x32_bf16 v[106:109], v[74:77], a[140:143], v[106:109]\n"
"v_mfma_f32_16x16x32_bf16 v[86:89], v[78:81], a[144:147], v[86:89]\n"
"ds_read_b32 v177, v48 offset:192\n"
"v_mfma_f32_16x16x32_bf16 v[90:93], v[78:81], a[148:151], v[90:93]\n"
"s_waitcnt lgkmcnt(8)\n"
"v_mfma_f32_16x16x32_bf16 v[94:97], v[82:85], a[144:147], v[94:97]\n"
"s_mov_b32 s63, 1179648\n"
"v_mfma_f32_16x16x32_bf16 v[98:101], v[82:85], a[148:151], v[98:101]\n"
"s_waitcnt lgkmcnt(7)\n"
"buffer_load_dwordx2 v[254:255], v10, s[20:23], s63 offen sc1\n"
"v_mfma_f32_16x16x32_bf16 v[102:105], v[54:57], a[144:147], v[102:105]\n"
"ds_read_b128 v[180:183], v50 offset:320\n"
"v_mfma_f32_16x16x32_bf16 v[106:109], v[54:57], a[148:151], v[106:109]\n"
"s_waitcnt lgkmcnt(6)\n"
"ds_read_b128 v[184:187], v50 offset:384\n"
"v_mfma_f32_16x16x32_bf16 v[86:89], v[58:61], a[152:155], v[86:89]\n"
"v_mfma_f32_16x16x32_bf16 v[90:93], v[58:61], a[156:159], v[90:93]\n"
"s_waitcnt lgkmcnt(5)\n"
"ds_read_b128 v[188:191], v50 offset:448\n"
"v_mfma_f32_16x16x32_bf16 v[94:97], v[62:65], a[152:155], v[94:97]\n"
"v_mfma_f32_16x16x32_bf16 v[98:101], v[62:65], a[156:159], v[98:101]\n"
"s_waitcnt lgkmcnt(4)\n"
"v_mfma_f32_16x16x32_bf16 v[102:105], v[66:69], a[152:155], v[102:105]\n"
"v_mfma_f32_16x16x32_bf16 v[106:109], v[66:69], a[156:159], v[106:109]\n"
"s_waitcnt lgkmcnt(3)\n"
"v_pk_mul_f32 v[178:179], v[176:177], s[70:71]\n"
"s_waitcnt lgkmcnt(2)\n"
"v_pk_fma_f32 v[86:87], v[174:175], v[180:181], v[86:87] op_sel_hi:[0,1,1] neg_lo:[1,0,0] neg_hi:[1,0,0]\n"
"v_pk_mul_f32 v[192:193], v[86:87], v[178:179] op_sel_hi:[1,0]\n"
"v_pk_fma_f32 v[86:87], v[86:87], v[176:177], s[68:69] op_sel_hi:[1,0,1]\n"
"v_exp_f32_e64 v192, v192 clamp\n"
"v_exp_f32_e64 v193, v193 clamp\n"
"v_max_f32 v86, s68, v86\n"
"v_max_f32 v87, s68, v87\n"
"v_pk_add_f32 v[86:87], v[86:87], v[192:193]\n"
"v_cvt_pk_bf16_f32 v218, v86, v87\n"
"v_pk_fma_f32 v[88:89], v[174:175], v[182:183], v[88:89] op_sel_hi:[0,1,1] neg_lo:[1,0,0] neg_hi:[1,0,0]\n"
"v_pk_mul_f32 v[194:195], v[88:89], v[178:179] op_sel_hi:[1,0]\n"
"v_pk_fma_f32 v[88:89], v[88:89], v[176:177], s[68:69] op_sel_hi:[1,0,1]\n"
"v_exp_f32_e64 v194, v194 clamp\n"
"v_exp_f32_e64 v195, v195 clamp\n"
"v_max_f32 v88, s68, v88\n"
"v_max_f32 v89, s68, v89\n"
"v_pk_add_f32 v[88:89], v[88:89], v[194:195]\n"
"v_cvt_pk_bf16_f32 v219, v88, v89\n"
"v_pk_fma_f32 v[90:91], v[174:175], v[180:181], v[90:91] op_sel:[1,0,0] neg_lo:[1,0,0] neg_hi:[1,0,0]\n"
"v_pk_mul_f32 v[192:193], v[90:91], v[178:179] op_sel:[0,1]\n"
"v_pk_fma_f32 v[90:91], v[90:91], v[176:177], s[68:69] op_sel:[0,1,0]\n"
"v_exp_f32_e64 v192, v192 clamp\n"
"v_exp_f32_e64 v193, v193 clamp\n"
"v_max_f32 v90, s68, v90\n"
"v_max_f32 v91, s68, v91\n"
"v_pk_add_f32 v[90:91], v[90:91], v[192:193]\n"
"v_cvt_pk_bf16_f32 v224, v90, v91\n"
"v_pk_fma_f32 v[92:93], v[174:175], v[182:183], v[92:93] op_sel:[1,0,0] neg_lo:[1,0,0] neg_hi:[1,0,0]\n"
"v_pk_mul_f32 v[194:195], v[92:93], v[178:179] op_sel:[0,1]\n"
"v_pk_fma_f32 v[92:93], v[92:93], v[176:177], s[68:69] op_sel:[0,1,0]\n"
"v_exp_f32_e64 v194, v194 clamp\n"
"v_exp_f32_e64 v195, v195 clamp\n"
"v_max_f32 v92, s68, v92\n"
"v_max_f32 v93, s68, v93\n"
"v_pk_add_f32 v[92:93], v[92:93], v[194:195]\n"
"v_cvt_pk_bf16_f32 v225, v92, v93\n"
"s_waitcnt lgkmcnt(1)\n"
"v_pk_fma_f32 v[94:95], v[174:175], v[184:185], v[94:95] op_sel_hi:[0,1,1] neg_lo:[1,0,0] neg_hi:[1,0,0]\n"
"v_pk_mul_f32 v[192:193], v[94:95], v[178:179] op_sel_hi:[1,0]\n"
"v_pk_fma_f32 v[94:95], v[94:95], v[176:177], s[68:69] op_sel_hi:[1,0,1]\n"
"v_exp_f32_e64 v192, v192 clamp\n"
"v_exp_f32_e64 v193, v193 clamp\n"
"v_max_f32 v94, s68, v94\n"
"v_max_f32 v95, s68, v95\n"
"v_pk_add_f32 v[94:95], v[94:95], v[192:193]\n"
"v_cvt_pk_bf16_f32 v222, v94, v95\n"
"v_pk_fma_f32 v[96:97], v[174:175], v[186:187], v[96:97] op_sel_hi:[0,1,1] neg_lo:[1,0,0] neg_hi:[1,0,0]\n"
"v_pk_mul_f32 v[194:195], v[96:97], v[178:179] op_sel_hi:[1,0]\n"
"v_pk_fma_f32 v[96:97], v[96:97], v[176:177], s[68:69] op_sel_hi:[1,0,1]\n"
"v_exp_f32_e64 v194, v194 clamp\n"
"v_exp_f32_e64 v195, v195 clamp\n"
"v_max_f32 v96, s68, v96\n"
"v_max_f32 v97, s68, v97\n"
"v_pk_add_f32 v[96:97], v[96:97], v[194:195]\n"
"v_cvt_pk_bf16_f32 v223, v96, v97\n"
"v_pk_fma_f32 v[98:99], v[174:175], v[184:185], v[98:99] op_sel:[1,0,0] neg_lo:[1,0,0] neg_hi:[1,0,0]\n"
"v_pk_mul_f32 v[192:193], v[98:99], v[178:179] op_sel:[0,1]\n"
"v_pk_fma_f32 v[98:99], v[98:99], v[176:177], s[68:69] op_sel:[0,1,0]\n"
"v_exp_f32_e64 v192, v192 clamp\n"
"v_exp_f32_e64 v193, v193 clamp\n"
"v_max_f32 v98, s68, v98\n"
"v_max_f32 v99, s68, v99\n"
"v_pk_add_f32 v[98:99], v[98:99], v[192:193]\n"
"v_cvt_pk_bf16_f32 v228, v98, v99\n"
"v_pk_fma_f32 v[100:101], v[174:175], v[186:187], v[100:101] op_sel:[1,0,0] neg_lo:[1,0,0] neg_hi:[1,0,0]\n"
"v_pk_mul_f32 v[194:195], v[100:101], v[178:179] op_sel:[0,1]\n"
"v_pk_fma_f32 v[100:101], v[100:101], v[176:177], s[68:69] op_sel:[0,1,0]\n"
"v_exp_f32_e64 v194, v194 clamp\n"
"v_exp_f32_e64 v195, v195 clamp\n"
"v_max_f32 v100, s68, v100\n"
"v_max_f32 v101, s68, v101\n"
"v_pk_add_f32 v[100:101], v[100:101], v[194:195]\n"
"v_cvt_pk_bf16_f32 v229, v100, v101\n"
"s_waitcnt lgkmcnt(0)\n"
"v_pk_fma_f32 v[102:103], v[174:175], v[188:189], v[102:103] op_sel_hi:[0,1,1] neg_lo:[1,0,0] neg_hi:[1,0,0]\n"
"v_pk_mul_f32 v[192:193], v[102:103], v[178:179] op_sel_hi:[1,0]\n"
"v_pk_fma_f32 v[102:103], v[102:103], v[176:177], s[68:69] op_sel_hi:[1,0,1]\n"
"v_exp_f32_e64 v192, v192 clamp\n"
"v_exp_f32_e64 v193, v193 clamp\n"
"v_max_f32 v102, s68, v102\n"
"v_max_f32 v103, s68, v103\n"
"v_pk_add_f32 v[102:103], v[102:103], v[192:193]\n"
"v_cvt_pk_bf16_f32 v220, v102, v103\n"
"s_waitcnt vmcnt(0)\n"
"v_cmp_ne_u64 s[18:19], v[254:255], v[8:9]\n"
"s_nop 0\n"
"s_and_b32 s18, s18, 65535\n"
"s_cbranch_scc0 L21_%=\n"
"s_mov_b32 s72, 0\n"
"L22_%=:\n"
"buffer_load_dwordx2 v[254:255], v10, s[20:23], s63 offen sc1\n"
"s_add_u32 s72, s72, 1\n"
"s_waitcnt vmcnt(0)\n"
"v_cmp_ne_u64 s[18:19], v[254:255], v[8:9]\n"
"s_nop 0\n"
"s_and_b32 s18, s18, 65535\n"
"s_cbranch_scc0 L21_%=\n"
"s_cmp_lt_u32 s72, 4194304\n"
"s_cbranch_scc1 L22_%=\n"
"L21_%=:\n"
"buffer_load_dwordx4 v[114:117], v53, s[96:99], 0 offen sc1\n"
"v_pk_fma_f32 v[104:105], v[174:175], v[190:191], v[104:105] op_sel_hi:[0,1,1] neg_lo:[1,0,0] neg_hi:[1,0,0]\n"
"v_pk_mul_f32 v[194:195], v[104:105], v[178:179] op_sel_hi:[1,0]\n"
"v_pk_fma_f32 v[104:105], v[104:105], v[176:177], s[68:69] op_sel_hi:[1,0,1]\n"
"buffer_load_dwordx4 v[118:121], v53, s[96:99], s81 offen sc1\n"
"v_exp_f32_e64 v194, v194 clamp\n"
"v_exp_f32_e64 v195, v195 clamp\n"
"v_max_f32 v104, s68, v104\n"
"buffer_load_dwordx4 v[122:125], v53, s[96:99], s83 offen sc1\n"
"v_max_f32 v105, s68, v105\n"
"v_pk_add_f32 v[104:105], v[104:105], v[194:195]\n"
"v_cvt_pk_bf16_f32 v221, v104, v105\n"
"v_pk_fma_f32 v[106:107], v[174:175], v[188:189], v[106:107] op_sel:[1,0,0] neg_lo:[1,0,0] neg_hi:[1,0,0]\n"
"buffer_load_dwordx4 v[126:129], v53, s[96:99], s85 offen sc1\n"
"v_pk_mul_f32 v[192:193], v[106:107], v[178:179] op_sel:[0,1]\n"
"v_pk_fma_f32 v[106:107], v[106:107], v[176:177], s[68:69] op_sel:[0,1,0]\n"
"v_exp_f32_e64 v192, v192 clamp\n"
"buffer_load_dwordx4 v[130:133], v53, s[96:99], s87 offen sc1\n"
"v_exp_f32_e64 v193, v193 clamp\n"
"v_max_f32 v106, s68, v106\n"
"v_max_f32 v107, s68, v107\n"
"buffer_load_dwordx4 v[134:137], v53, s[96:99], s89 offen sc1\n"
"v_pk_add_f32 v[106:107], v[106:107], v[192:193]\n"
"v_cvt_pk_bf16_f32 v226, v106, v107\n"
"v_pk_fma_f32 v[108:109], v[174:175], v[190:191], v[108:109] op_sel:[1,0,0] neg_lo:[1,0,0] neg_hi:[1,0,0]\n"
"v_pk_mul_f32 v[194:195], v[108:109], v[178:179] op_sel:[0,1]\n"
"buffer_load_dwordx4 v[138:141], v53, s[96:99], s91 offen sc1\n"
"v_pk_fma_f32 v[108:109], v[108:109], v[176:177], s[68:69] op_sel:[0,1,0]\n"
"v_exp_f32_e64 v194, v194 clamp\n"
"v_exp_f32_e64 v195, v195 clamp\n"
"buffer_load_dwordx4 v[142:145], v53, s[96:99], s93 offen sc1\n"
"v_max_f32 v108, s68, v108\n"
"v_max_f32 v109, s68, v109\n"
"v_pk_add_f32 v[108:109], v[108:109], v[194:195]\n"
"v_cvt_pk_bf16_f32 v227, v108, v109\n"
"s_barrier\n"
"s_waitcnt vmcnt(7)\n"
"v_mfma_f32_16x16x32_bf16 v[110:113], v[234:237], v[114:117], 0\n"
"s_waitcnt vmcnt(6)\n"
"v_mfma_f32_16x16x32_bf16 v[110:113], v[234:237], v[118:121], v[110:113]\n"
"s_waitcnt vmcnt(5)\n"
"v_mfma_f32_16x16x32_bf16 v[110:113], v[234:237], v[122:125], v[110:113]\n"
"s_waitcnt vmcnt(4)\n"
"v_mfma_f32_16x16x32_bf16 v[110:113], v[234:237], v[126:129], v[110:113]\n"
"s_waitcnt vmcnt(3)\n"
"v_mfma_f32_16x16x32_bf16 v[110:113], v[234:237], v[130:133], v[110:113]\n"
"s_waitcnt vmcnt(2)\n"
"v_mfma_f32_16x16x32_bf16 v[110:113], v[234:237], v[134:137], v[110:113]\n"
"s_waitcnt vmcnt(1)\n"
"v_mfma_f32_16x16x32_bf16 v[110:113], v[234:237], v[138:141], v[110:113]\n"
"s_waitcnt vmcnt(0)\n"
"v_mfma_f32_16x16x32_bf16 v[110:113], v[234:237], v[142:145], v[110:113]\n"
"s_nop 7\n"
"v_cvt_pk_bf16_f32 v110, v110, v111\n"
"v_cvt_pk_bf16_f32 v111, v112, v113\n"
"ds_write_b64 v22, v[110:111]\n"
"s_waitcnt lgkmcnt(0)\n"
"s_barrier\n"
"ds_read_b128 v[174:177], v26 offset:1280\n"
"ds_read_b128 v[178:181], v26 offset:2624\n"
"ds_read_b128 v[182:185], v23\n"
"ds_read_b128 v[186:189], v23 offset:1344\n"
"ds_read_b128 v[190:193], v24\n"
"ds_read_b128 v[194:197], v25\n"
"s_waitcnt lgkmcnt(5)\n"
"v_mfma_f32_16x16x32_bf16 v[122:125], v[174:177], v[218:221], 0\n"
"v_mfma_f32_16x16x32_bf16 v[142:145], v[174:177], v[224:227], 0\n"
"s_waitcnt lgkmcnt(4)\n"
"v_mfma_f32_16x16x32_bf16 v[126:129], v[178:181], v[220:223], 0\n"
"v_mfma_f32_16x16x32_bf16 v[146:149], v[178:181], v[226:229], 0\n"
"s_waitcnt lgkmcnt(3)\n"
"v_mfma_f32_16x16x32_bf16 v[110:113], v[182:185], v[218:221], 0\n"
"v_mfma_f32_16x16x32_bf16 v[130:133], v[182:185], v[224:227], 0\n"
"s_waitcnt lgkmcnt(2)\n"
"v_mfma_f32_16x16x32_bf16 v[114:117], v[186:189], v[220:223], 0\n"
"v_mfma_f32_16x16x32_bf16 v[134:137], v[186:189], v[226:229], 0\n"
"s_waitcnt lgkmcnt(1)\n"
"v_mfma_f32_16x16x32_bf16 v[118:121], v[190:193], v[218:221], 0\n"
"v_mfma_f32_16x16x32_bf16 v[138:141], v[190:193], v[224:227], 0\n"
"s_waitcnt lgkmcnt(0)\n"
"v_mfma_f32_16x16x32_bf16 v[118:121], v[194:197], v[220:223], v[118:121]\n"
"v_mfma_f32_16x16x32_bf16 v[138:141], v[194:197], v[226:229], v[138:141]\n"
"v_add_f32 v122, s68, v122\n"
"v_add_f32 v126, s68, v126\n"
"v_rcp_f32 v122, v122\n"
"v_rcp_f32 v126, v126\n"
"v_add_f32 v142, s68, v142\n"
"v_add_f32 v146, s68, v146\n"
"v_rcp_f32 v142, v142\n"
"v_rcp_f32 v146, v146\n"
"v_cndmask_b32 v123, v122, v126, s[76:77]\n"
"v_pk_mul_f32 v[110:111], v[110:111], v[122:123] op_sel_hi:[1,0]\n"
"v_pk_mul_f32 v[112:113], v[112:113], v[122:123] op_sel_hi:[1,0]\n"
"v_pk_mul_f32 v[114:115], v[114:115], v[126:127] op_sel_hi:[1,0]\n"
"v_pk_mul_f32 v[116:117], v[116:117], v[126:127] op_sel_hi:[1,0]\n"
"v_pk_mul_f32 v[118:119], v[118:119], v[122:123] op_sel:[0,1]\n"
"v_pk_mul_f32 v[120:121], v[120:121], v[122:123] op_sel:[0,1]\n"
"v_cvt_pk_bf16_f32 v218, v110, v111\n"
"v_cvt_pk_bf16_f32 v219, v112, v113\n"
"v_cvt_pk_bf16_f32 v220, v114, v115\n"
"v_cvt_pk_bf16_f32 v221, v116, v117\n"
"buffer_store_dwordx4 v[218:221], v28, s[96:99], 0 offen sc1\n"
"v_cvt_pk_bf16_f32 v230, v118, v119\n"
"v_cvt_pk_bf16_f32 v231, v120, v121\n"
"v_cndmask_b32 v143, v142, v146, s[76:77]\n"
"v_pk_mul_f32 v[130:131], v[130:131], v[142:143] op_sel_hi:[1,0]\n"
"v_pk_mul_f32 v[132:133], v[132:133], v[142:143] op_sel_hi:[1,0]\n"
"v_pk_mul_f32 v[134:135], v[134:135], v[146:147] op_sel_hi:[1,0]\n"
"v_pk_mul_f32 v[136:137], v[136:137], v[146:147] op_sel_hi:[1,0]\n"
"v_pk_mul_f32 v[138:139], v[138:139], v[142:143] op_sel:[0,1]\n"
"v_pk_mul_f32 v[140:141], v[140:141], v[142:143] op_sel:[0,1]\n"
"v_cvt_pk_bf16_f32 v222, v130, v131\n"
"v_cvt_pk_bf16_f32 v223, v132, v133\n"
"v_cvt_pk_bf16_f32 v224, v134, v135\n"
"v_cvt_pk_bf16_f32 v225, v136, v137\n"
"buffer_store_dwordx4 v[222:225], v28, s[96:99], s84 offen sc1\n"
"v_cvt_pk_bf16_f32 v232, v138, v139\n"
"v_cvt_pk_bf16_f32 v233, v140, v141\n"
"s_nop 0\n"
"v_permlane16_swap_b32 v230, v232\n"
"v_permlane16_swap_b32 v231, v233\n"
"buffer_store_dwordx4 v[230:233], v29, s[96:99], 0 offen sc1\n"
"s_mov_b32 s3, 1310720\n"
"s_waitcnt vmcnt(0)\n"
"buffer_store_dwordx2 v[8:9], v14, s[20:23], s3 offen sc1\n"
"s_add_u32 m0, s59, 98304\n"
"s_nop 0\n"
"buffer_load_dwordx4 v1, s[20:23], m0 offen sc1 lds\n"
"s_add_u32 m0, s59, 102400\n"
"s_nop 0\n"
"buffer_load_dwordx4 v1, s[20:23], m0 offen sc1 lds\n"
"s_add_u32 m0, s59, 106496\n"
"s_nop 0\n"
"buffer_load_dwordx4 v1, s[20:23], m0 offen sc1 lds\n"
"s_add_u32 m0, s59, 110592\n"
"s_nop 0\n"
"buffer_load_dwordx4 v1, s[20:23], m0 offen sc1 lds\n"
"s_add_u32 m0, s59, 114688\n"
"s_nop 0\n"
"buffer_load_dwordx4 v1, s[20:23], m0 offen sc1 lds\n"
"s_add_u32 m0, s59, 118784\n"
"s_nop 0\n"
"buffer_load_dwordx4 v1, s[20:23], m0 offen sc1 lds\n"
"s_add_u32 m0, s59, 122880\n"
"s_nop 0\n"
"buffer_load_dwordx4 v1, s[20:23], m0 offen sc1 lds\n"
"s_add_u32 m0, s59, 126976\n"
"s_nop 0\n"
"buffer_load_dwordx4 v1, s[20:23], m0 offen sc1 lds\n"
"s_add_u32 m0, s59, 131072\n"
"s_nop 0\n"
"buffer_load_dwordx4 v1, s[20:23], m0 offen sc1 lds\n"
"s_add_u32 m0, s59, 12288\n"
"v_add_u32 v1, 122880, v1\n"
"buffer_load_dwordx4 v1, s[20:23], m0 offen sc1 lds\n"
"s_add_u32 m0, s59, 16384\n"
"s_nop 0\n"
"buffer_load_dwordx4 v1, s[20:23], m0 offen sc1 lds\n"
"s_add_u32 m0, s59, 20480\n"
"s_nop 0\n"
"buffer_load_dwordx4 v1, s[20:23], m0 offen sc1 lds\n"
"s_barrier\n"
"s_mov_b32 s63, 1310720\n"
"s_mov_b32 s72, 0\n"
"L24_%=:\n"
"buffer_load_dwordx2 v[254:255], v11, s[20:23], s63 offen sc1\n"
"s_add_u32 s72, s72, 1\n"
"s_waitcnt vmcnt(0)\n"
"v_cmp_ne_u64 s[18:19], v[254:255], v[8:9]\n"
"s_nop 0\n"
"s_and_b32 s18, s18, 65535\n"
"s_cbranch_scc0 L23_%=\n"
"s_cmp_lt_u32 s72, 4194304\n"
"s_cbranch_scc1 L24_%=\n"
"L23_%=:\n"
"; P3 layer 1\n"
"buffer_load_dwordx4 a[0:3], v30, s[96:99], 0 offen sc1\n"
"buffer_load_dwordx4 a[4:7], v31, s[96:99], 0 offen sc1\n"
"buffer_load_dwordx4 a[8:11], v30, s[96:99], 0 offen offset:2048 sc1\n"
"buffer_load_dwordx4 a[12:15], v31, s[96:99], 0 offen offset:2048 sc1\n"
"buffer_load_dwordx4 a[16:19], v30, s[96:99], s80 offen sc1\n"
"buffer_load_dwordx4 a[20:23], v31, s[96:99], s80 offen sc1\n"
"buffer_load_dwordx4 a[24:27], v30, s[96:99], s80 offen offset:2048 sc1\n"
"buffer_load_dwordx4 a[28:31], v31, s[96:99], s80 offen offset:2048 sc1\n"
"buffer_load_dwordx4 a[32:35], v30, s[96:99], s81 offen sc1\n"
"buffer_load_dwordx4 a[36:39], v31, s[96:99], s81 offen sc1\n"
"buffer_load_dwordx4 a[40:43], v30, s[96:99], s81 offen offset:2048 sc1\n"
"buffer_load_dwordx4 a[44:47], v31, s[96:99], s81 offen offset:2048 sc1\n"
"ds_read_b128 v[54:57], v5 offset:24576\n"
"ds_read_b128 v[58:61], v5 offset:26624\n"
"ds_read_b128 v[62:65], v5 offset:28672\n"
"ds_read_b128 v[66:69], v5 offset:30720\n"
"ds_read_b128 v[70:73], v5 offset:32768\n"
"ds_read_b128 v[74:77], v5 offset:34816\n"
"s_waitcnt vmcnt(11) lgkmcnt(5)\n"
"v_mfma_f32_16x16x32_bf16 v[150:153], v[54:57], a[0:3], v[198:201]\n"
"s_waitcnt vmcnt(10) lgkmcnt(4)\n"
"ds_read_b128 v[78:81], v5 offset:36864\n"
"v_mfma_f32_16x16x32_bf16 v[154:157], v[54:57], a[4:7], 0\n"
"buffer_load_dwordx4 a[48:51], v30, s[96:99], s82 offen sc1\n"
"v_mfma_f32_16x16x32_bf16 v[158:161], v[58:61], a[0:3], v[202:205]\n"
"s_waitcnt lgkmcnt(4)\n"
"ds_read_b128 v[82:85], v5 offset:38912\n"
"v_mfma_f32_16x16x32_bf16 v[162:165], v[58:61], a[4:7], 0\n"
"v_mfma_f32_16x16x32_bf16 v[182:185], v[62:65], a[0:3], v[214:217]\n"
"s_waitcnt vmcnt(10) lgkmcnt(4)\n"
"ds_read_b128 v[54:57], v5 offset:40960\n"
"v_mfma_f32_16x16x32_bf16 v[186:189], v[62:65], a[4:7], 0\n"
"buffer_load_dwordx4 a[52:55], v31, s[96:99], s82 offen sc1\n"
"v_mfma_f32_16x16x32_bf16 v[150:153], v[66:69], a[8:11], v[150:153]\n"
"s_waitcnt vmcnt(10) lgkmcnt(4)\n"
"ds_read_b128 v[58:61], v5 offset:43008\n"
"v_mfma_f32_16x16x32_bf16 v[154:157], v[66:69], a[12:15], v[154:157]\n"
"v_mfma_f32_16x16x32_bf16 v[158:161], v[70:73], a[8:11], v[158:161]\n"
"s_waitcnt lgkmcnt(4)\n"
"ds_read_b128 v[62:65], v5 offset:45056\n"
"v_mfma_f32_16x16x32_bf16 v[162:165], v[70:73], a[12:15], v[162:165]\n"
"buffer_load_dwordx4 a[56:59], v30, s[96:99], s82 offen offset:2048 sc1\n"
"v_mfma_f32_16x16x32_bf16 v[182:185], v[74:77], a[8:11], v[182:185]\n"
"s_waitcnt vmcnt(10) lgkmcnt(4)\n"
"ds_read_b128 v[66:69], v5 offset:47104\n"
"v_mfma_f32_16x16x32_bf16 v[186:189], v[74:77], a[12:15], v[186:189]\n"
"v_mfma_f32_16x16x32_bf16 v[150:153], v[78:81], a[16:19], v[150:153]\n"
"s_waitcnt vmcnt(9) lgkmcnt(4)\n"
"ds_read_b128 v[70:73], v5 offset:49152\n"
"v_mfma_f32_16x16x32_bf16 v[154:157], v[78:81], a[20:23], v[154:157]\n"
"buffer_load_dwordx4 a[60:63], v31, s[96:99], s82 offen offset:2048 sc1\n"
"v_mfma_f32_16x16x32_bf16 v[158:161], v[82:85], a[16:19], v[158:161]\n"
"s_waitcnt lgkmcnt(4)\n"
"ds_read_b128 v[74:77], v5 offset:51200\n"
"v_mfma_f32_16x16x32_bf16 v[162:165], v[82:85], a[20:23], v[162:165]\n"
"v_mfma_f32_16x16x32_bf16 v[182:185], v[54:57], a[16:19], v[182:185]\n"
"s_waitcnt vmcnt(9) lgkmcnt(4)\n"
"ds_read_b128 v[78:81], v7 offset:4096\n"
"v_mfma_f32_16x16x32_bf16 v[186:189], v[54:57], a[20:23], v[186:189]\n"
"buffer_load_dwordx4 a[64:67], v30, s[96:99], s83 offen sc1\n"
"v_mfma_f32_16x16x32_bf16 v[150:153], v[58:61], a[24:27], v[150:153]\n"
"s_waitcnt vmcnt(9)\n"
"ds_read_b128 v[82:85], v7 offset:6144\n"
"v_mfma_f32_16x16x32_bf16 v[154:157], v[58:61], a[28:31], v[154:157]\n"
"s_barrier\n"
"s_waitcnt lgkmcnt(5)\n"
"v_mfma_f32_16x16x32_bf16 v[158:161], v[62:65], a[24:27], v[158:161]\n"
"s_waitcnt lgkmcnt(4)\n"
"ds_read_b128 v[54:57], v7 offset:8192\n"
"v_mfma_f32_16x16x32_bf16 v[162:165], v[62:65], a[28:31], v[162:165]\n"
"buffer_load_dwordx4 a[68:71], v31, s[96:99], s83 offen sc1\n"
"v_mfma_f32_16x16x32_bf16 v[182:185], v[66:69], a[24:27], v[182:185]\n"
"s_waitcnt vmcnt(9) lgkmcnt(4)\n"
"ds_read_b128 v[58:61], v7 offset:10240\n"
"v_mfma_f32_16x16x32_bf16 v[186:189], v[66:69], a[28:31], v[186:189]\n"
"v_mfma_f32_16x16x32_bf16 v[150:153], v[70:73], a[32:35], v[150:153]\n"
"s_waitcnt vmcnt(8) lgkmcnt(4)\n"
"ds_read_b128 v[62:65], v7 offset:12288\n"
"v_mfma_f32_16x16x32_bf16 v[154:157], v[70:73], a[36:39], v[154:157]\n"
"buffer_load_dwordx4 a[72:75], v30, s[96:99], s83 offen offset:2048 sc1\n"
"v_mfma_f32_16x16x32_bf16 v[158:161], v[74:77], a[32:35], v[158:161]\n"
"s_waitcnt lgkmcnt(4)\n"
"ds_read_b128 v[66:69], v7 offset:14336\n"
"v_mfma_f32_16x16x32_bf16 v[162:165], v[74:77], a[36:39], v[162:165]\n"
"v_mfma_f32_16x16x32_bf16 v[182:185], v[78:81], a[32:35], v[182:185]\n"
"s_waitcnt vmcnt(8) lgkmcnt(4)\n"
"ds_read_b128 v[70:73], v7 offset:16384\n"
"v_mfma_f32_16x16x32_bf16 v[186:189], v[78:81], a[36:39], v[186:189]\n"
"buffer_load_dwordx4 a[76:79], v31, s[96:99], s83 offen offset:2048 sc1\n"
"v_mfma_f32_16x16x32_bf16 v[150:153], v[82:85], a[40:43], v[150:153]\n"
"s_waitcnt vmcnt(8) lgkmcnt(4)\n"
"ds_read_b128 v[74:77], v7 offset:18432\n"
"v_mfma_f32_16x16x32_bf16 v[154:157], v[82:85], a[44:47], v[154:157]\n"
"v_mfma_f32_16x16x32_bf16 v[158:161], v[54:57], a[40:43], v[158:161]\n"
"s_waitcnt lgkmcnt(4)\n"
"ds_read_b128 v[78:81], v7 offset:20480\n"
"v_mfma_f32_16x16x32_bf16 v[162:165], v[54:57], a[44:47], v[162:165]\n"
"v_mfma_f32_16x16x32_bf16 v[182:185], v[58:61], a[40:43], v[182:185]\n"
"s_waitcnt vmcnt(7) lgkmcnt(4)\n"
"ds_read_b128 v[82:85], v7 offset:22528\n"
"v_mfma_f32_16x16x32_bf16 v[186:189], v[58:61], a[44:47], v[186:189]\n"
"v_mfma_f32_16x16x32_bf16 v[150:153], v[62:65], a[48:51], v[150:153]\n"
"s_waitcnt vmcnt(6) lgkmcnt(4)\n"
"ds_read_b128 v[54:57], v5 offset:53248\n"
"v_mfma_f32_16x16x32_bf16 v[154:157], v[62:65], a[52:55], v[154:157]\n"
"s_add_u32 m0, s59, 24576\n"
"v_mfma_f32_16x16x32_bf16 v[158:161], v[66:69], a[48:51], v[158:161]\n"
"s_waitcnt lgkmcnt(4)\n"
"ds_read_b128 v[58:61], v5 offset:55296\n"
"v_mfma_f32_16x16x32_bf16 v[162:165], v[66:69], a[52:55], v[162:165]\n"
"buffer_load_dwordx4 v1, s[20:23], m0 offen sc1 lds\n"
"v_mfma_f32_16x16x32_bf16 v[182:185], v[70:73], a[48:51], v[182:185]\n"
"s_waitcnt vmcnt(6) lgkmcnt(4)\n"
"ds_read_b128 v[62:65], v5 offset:57344\n"
"v_mfma_f32_16x16x32_bf16 v[186:189], v[70:73], a[52:55], v[186:189]\n"
"s_add_u32 m0, s59, 28672\n"
"v_mfma_f32_16x16x32_bf16 v[150:153], v[74:77], a[56:59], v[150:153]\n"
"s_waitcnt vmcnt(5) lgkmcnt(4)\n"
"ds_read_b128 v[66:69], v5 offset:59392\n"
"v_mfma_f32_16x16x32_bf16 v[154:157], v[74:77], a[60:63], v[154:157]\n"
"buffer_load_dwordx4 v1, s[20:23], m0 offen sc1 lds\n"
"v_mfma_f32_16x16x32_bf16 v[158:161], v[78:81], a[56:59], v[158:161]\n"
"s_waitcnt lgkmcnt(4)\n"
"ds_read_b128 v[70:73], v5 offset:61440\n"
"v_mfma_f32_16x16x32_bf16 v[162:165], v[78:81], a[60:63], v[162:165]\n"
"s_add_u32 m0, s59, 32768\n"
"v_mfma_f32_16x16x32_bf16 v[182:185], v[82:85], a[56:59], v[182:185]\n"
"ds_read_b128 v[74:77], v5 offset:63488\n"
"v_mfma_f32_16x16x32_bf16 v[186:189], v[82:85], a[60:63], v[186:189]\n"
"s_barrier\n"
"s_waitcnt vmcnt(5) lgkmcnt(5)\n"
"v_mfma_f32_16x16x32_bf16 v[150:153], v[54:57], a[64:67], v[150:153]\n"
"s_waitcnt vmcnt(4) lgkmcnt(4)\n"
"ds_read_b128 v[78:81], v5\n"
"v_mfma_f32_16x16x32_bf16 v[154:157], v[54:57], a[68:71], v[154:157]\n"
"buffer_load_dwordx4 v1, s[20:23], m0 offen sc1 lds\n"
"v_mfma_f32_16x16x32_bf16 v[158:161], v[58:61], a[64:67], v[158:161]\n"
"s_waitcnt lgkmcnt(4)\n"
"ds_read_b128 v[82:85], v5 offset:2048\n"
"s_add_u32 m0, s59, 36864\n"
"v_mfma_f32_16x16x32_bf16 v[162:165], v[58:61], a[68:71], v[162:165]\n"
"buffer_load_dwordx4 v1, s[20:23], m0 offen sc1 lds\n"
"v_mfma_f32_16x16x32_bf16 v[182:185], v[62:65], a[64:67], v[182:185]\n"
"s_waitcnt vmcnt(5) lgkmcnt(4)\n"
"ds_read_b128 v[54:57], v5 offset:4096\n"
"v_mfma_f32_16x16x32_bf16 v[186:189], v[62:65], a[68:71], v[186:189]\n"
"s_add_u32 m0, s59, 40960\n"
"v_mfma_f32_16x16x32_bf16 v[150:153], v[66:69], a[72:75], v[150:153]\n"
"s_waitcnt vmcnt(4) lgkmcnt(4)\n"
"ds_read_b128 v[58:61], v5 offset:6144\n"
"v_mfma_f32_16x16x32_bf16 v[154:157], v[66:69], a[76:79], v[154:157]\n"
"buffer_load_dwordx4 v1, s[20:23], m0 offen sc1 lds\n"
"v_mfma_f32_16x16x32_bf16 v[158:161], v[70:73], a[72:75], v[158:161]\n"
"s_waitcnt lgkmcnt(4)\n"
"ds_read_b128 v[62:65], v5 offset:8192\n"
"v_mfma_f32_16x16x32_bf16 v[162:165], v[70:73], a[76:79], v[162:165]\n"
"s_add_u32 m0, s59, 45056\n"
"v_mfma_f32_16x16x32_bf16 v[182:185], v[74:77], a[72:75], v[182:185]\n"
"s_waitcnt lgkmcnt(4)\n"
"ds_read_b128 v[66:69], v5 offset:10240\n"
"v_mfma_f32_16x16x32_bf16 v[186:189], v[74:77], a[76:79], v[186:189]\n"
"buffer_load_dwordx4 v1, s[20:23], m0 offen sc1 lds\n"
"v_mfma_f32_16x16x32_bf16 v[166:169], v[78:81], a[0:3], v[206:209]\n"
"s_waitcnt lgkmcnt(4)\n"
"ds_read_b128 v[70:73], v6\n"
"v_mfma_f32_16x16x32_bf16 v[170:173], v[78:81], a[4:7], 0\n"
"v_mfma_f32_16x16x32_bf16 v[174:177], v[82:85], a[0:3], v[210:213]\n"
"s_waitcnt lgkmcnt(4)\n"
"ds_read_b128 v[74:77], v6 offset:2048\n"
"v_mfma_f32_16x16x32_bf16 v[178:181], v[82:85], a[4:7], 0\n"
"ds_write_b128 v43, v[154:157]\n"
"v_mfma_f32_16x16x32_bf16 v[166:169], v[54:57], a[8:11], v[166:169]\n"
"s_waitcnt lgkmcnt(5)\n"
"ds_read_b128 v[78:81], v6 offset:4096\n"
"v_mfma_f32_16x16x32_bf16 v[170:173], v[54:57], a[12:15], v[170:173]\n"
"ds_write_b128 v43, v[162:165] offset:1024\n"
"v_mfma_f32_16x16x32_bf16 v[174:177], v[58:61], a[8:11], v[174:177]\n"
"ds_read_b128 v[82:85], v6 offset:6144\n"
"ds_write_b128 v43, v[186:189] offset:4096\n"
"v_mfma_f32_16x16x32_bf16 v[178:181], v[58:61], a[12:15], v[178:181]\n"
"s_waitcnt lgkmcnt(0)\n"
"s_barrier\n"
"v_mfma_f32_16x16x32_bf16 v[166:169], v[62:65], a[16:19], v[166:169]\n"
"ds_read_b128 v[54:57], v6 offset:8192\n"
"ds_read_b128 v[86:89], v44\n"
"v_mfma_f32_16x16x32_bf16 v[170:173], v[62:65], a[20:23], v[170:173]\n"
"s_add_u32 m0, s59, 49152\n"
"v_mfma_f32_16x16x32_bf16 v[174:177], v[66:69], a[16:19], v[174:177]\n"
"ds_read_b128 v[58:61], v6 offset:10240\n"
"ds_read_b128 v[90:93], v44 offset:1024\n"
"ds_read_b128 v[102:105], v44 offset:4096\n"
"v_mfma_f32_16x16x32_bf16 v[178:181], v[66:69], a[20:23], v[178:181]\n"
"buffer_load_dwordx4 v1, s[20:23], m0 offen sc1 lds\n"
"v_mfma_f32_16x16x32_bf16 v[166:169], v[70:73], a[24:27], v[166:169]\n"
"ds_read_b128 v[62:65], v6 offset:12288\n"
"v_mfma_f32_16x16x32_bf16 v[170:173], v[70:73], a[28:31], v[170:173]\n"
"s_add_u32 m0, s59, 53248\n"
"v_mfma_f32_16x16x32_bf16 v[174:177], v[74:77], a[24:27], v[174:177]\n"
"s_waitcnt lgkmcnt(4)\n"
"ds_read_b128 v[66:69], v6 offset:14336\n"
"v_add_f32 v198, v150, v86\n"
"v_add_f32 v199, v151, v87\n"
"v_add_f32 v200, v152, v88\n"
"v_add_f32 v201, v153, v89\n"
"v_mfma_f32_16x16x32_bf16 v[178:181], v[74:77], a[28:31], v[178:181]\n"
"buffer_load_dwordx4 v1, s[20:23], m0 offen sc1 lds\n"
"v_mfma_f32_16x16x32_bf16 v[166:169], v[78:81], a[32:35], v[166:169]\n"
"s_waitcnt lgkmcnt(3)\n"
"ds_read_b128 v[70:73], v6 offset:16384\n"
"v_cvt_pk_bf16_f32 v218, v198, v199\n"
"v_cvt_pk_bf16_f32 v219, v200, v201\n"
"v_mfma_f32_16x16x32_bf16 v[170:173], v[78:81], a[36:39], v[170:173]\n"
"s_add_u32 m0, s59, 57344\n"
"v_add_f32 v202, v158, v90\n"
"v_add_f32 v203, v159, v91\n"
"v_add_f32 v204, v160, v92\n"
"v_mfma_f32_16x16x32_bf16 v[174:177], v[82:85], a[32:35], v[174:177]\n"
"ds_read_b128 v[74:77], v6 offset:18432\n"
"v_add_f32 v205, v161, v93\n"
"v_cvt_pk_bf16_f32 v220, v202, v203\n"
"v_mfma_f32_16x16x32_bf16 v[178:181], v[82:85], a[36:39], v[178:181]\n"
"buffer_load_dwordx4 v1, s[20:23], m0 offen sc1 lds\n"
"v_cvt_pk_bf16_f32 v221, v204, v205\n"
"v_mfma_f32_16x16x32_bf16 v[166:169], v[54:57], a[40:43], v[166:169]\n"
"s_waitcnt lgkmcnt(4)\n"
"ds_read_b128 v[78:81], v6 offset:20480\n"
"v_add_f32 v214, v182, v102\n"
"v_add_f32 v215, v183, v103\n"
"v_mfma_f32_16x16x32_bf16 v[170:173], v[54:57], a[44:47], v[170:173]\n"
"s_add_u32 m0, s59, 61440\n"
"v_add_f32 v216, v184, v104\n"
"v_add_f32 v217, v185, v105\n"
"v_cvt_pk_bf16_f32 v226, v214, v215\n"
"v_mfma_f32_16x16x32_bf16 v[174:177], v[58:61], a[40:43], v[174:177]\n"
"s_waitcnt lgkmcnt(4)\n"
"ds_read_b128 v[82:85], v6 offset:22528\n"
"v_cvt_pk_bf16_f32 v227, v216, v217\n"
"v_mov_b32 v228, v226\n"
"v_mfma_f32_16x16x32_bf16 v[178:181], v[58:61], a[44:47], v[178:181]\n"
"buffer_load_dwordx4 v1, s[20:23], m0 offen sc1 lds\n"
"v_mov_b32 v229, v227\n"
"v_mfma_f32_16x16x32_bf16 v[166:169], v[62:65], a[48:51], v[166:169]\n"
"s_waitcnt lgkmcnt(4)\n"
"ds_read_b128 v[54:57], v6 offset:24576\n"
"v_add_f32 v230, v198, v200\n"
"v_add_f32 v231, v199, v201\n"
"v_mfma_f32_16x16x32_bf16 v[170:173], v[62:65], a[52:55], v[170:173]\n"
"s_add_u32 m0, s59, 0\n"
"v_add_u32 v1, 65536, v1\n"
"v_mul_f32 v232, v198, v198\n"
"v_mul_f32 v233, v199, v199\n"
"v_mfma_f32_16x16x32_bf16 v[174:177], v[66:69], a[48:51], v[174:177]\n"
"s_waitcnt lgkmcnt(4)\n"
"ds_read_b128 v[58:61], v6 offset:26624\n"
"v_fmac_f32 v232, v200, v200\n"
"v_fmac_f32 v233, v201, v201\n"
"v_mfma_f32_16x16x32_bf16 v[178:181], v[66:69], a[52:55], v[178:181]\n"
"buffer_load_dwordx4 v1, s[20:23], m0 offen sc1 lds\n"
"v_add_f32 v230, v230, v202\n"
"v_mfma_f32_16x16x32_bf16 v[166:169], v[70:73], a[56:59], v[166:169]\n"
"s_add_u32 m0, s59, 4096\n"
"v_fmac_f32 v232, v202, v202\n"
"v_add_f32 v231, v231, v203\n"
"v_fmac_f32 v233, v203, v203\n"
"v_mfma_f32_16x16x32_bf16 v[170:173], v[70:73], a[60:63], v[170:173]\n"
"s_waitcnt lgkmcnt(4)\n"
"buffer_load_dwordx4 v1, s[20:23], m0 offen sc1 lds\n"
"v_add_f32 v230, v230, v204\n"
"v_mfma_f32_16x16x32_bf16 v[174:177], v[74:77], a[56:59], v[174:177]\n"
"v_fmac_f32 v232, v204, v204\n"
"v_add_f32 v231, v231, v205\n"
"v_mfma_f32_16x16x32_bf16 v[178:181], v[74:77], a[60:63], v[178:181]\n"
"s_waitcnt lgkmcnt(3)\n"
"buffer_store_dwordx4 v[218:221], v34, s[96:99], 0 offen sc1\n"
"v_fmac_f32 v233, v205, v205\n"
"v_mfma_f32_16x16x32_bf16 v[166:169], v[78:81], a[64:67], v[166:169]\n"
"v_permlane16_swap_b32 v226, v228\n"
"v_mfma_f32_16x16x32_bf16 v[170:173], v[78:81], a[68:71], v[170:173]\n"
"s_waitcnt lgkmcnt(2)\n"
"v_add_f32 v230, v230, v214\n"
"v_mfma_f32_16x16x32_bf16 v[174:177], v[82:85], a[64:67], v[174:177]\n"
"v_fmac_f32 v232, v214, v214\n"
"v_add_f32 v231, v231, v215\n"
"v_mfma_f32_16x16x32_bf16 v[178:181], v[82:85], a[68:71], v[178:181]\n"
"s_waitcnt lgkmcnt(1)\n"
"v_permlane16_swap_b32 v227, v229\n"
"v_fmac_f32 v233, v215, v215\n"
"v_mfma_f32_16x16x32_bf16 v[166:169], v[54:57], a[72:75], v[166:169]\n"
"buffer_store_dwordx4 v[226:229], v35, s[96:99], 0 offen sc1\n"
"v_add_f32 v230, v230, v216\n"
"v_mfma_f32_16x16x32_bf16 v[170:173], v[54:57], a[76:79], v[170:173]\n"
"s_waitcnt lgkmcnt(0)\n"
"v_fmac_f32 v232, v216, v216\n"
"v_mfma_f32_16x16x32_bf16 v[174:177], v[58:61], a[72:75], v[174:177]\n"
"v_add_f32 v231, v231, v217\n"
"v_fmac_f32 v233, v217, v217\n"
"v_mfma_f32_16x16x32_bf16 v[178:181], v[58:61], a[76:79], v[178:181]\n"
"s_nop 1\n"
"ds_write_b128 v43, v[170:173] offset:2048\n"
"s_nop 4\n"
"ds_write_b128 v43, v[178:181] offset:3072\n"
"s_waitcnt lgkmcnt(0)\n"
"s_barrier\n"
"ds_read_b128 v[94:97], v44 offset:2048\n"
"ds_read_b128 v[98:101], v44 offset:3072\n"
"s_waitcnt lgkmcnt(1)\n"
"v_pk_add_f32 v[206:207], v[166:167], v[94:95]\n"
"v_pk_add_f32 v[208:209], v[168:169], v[96:97]\n"
"v_cvt_pk_bf16_f32 v222, v206, v207\n"
"v_cvt_pk_bf16_f32 v223, v208, v209\n"
"s_waitcnt lgkmcnt(0)\n"
"v_pk_add_f32 v[210:211], v[174:175], v[98:99]\n"
"v_pk_add_f32 v[212:213], v[176:177], v[100:101]\n"
"v_cvt_pk_bf16_f32 v224, v210, v211\n"
"v_cvt_pk_bf16_f32 v225, v212, v213\n"
"buffer_store_dwordx4 v[222:225], v34, s[96:99], 0 offen offset:1024 sc1\n"
"v_pk_add_f32 v[230:231], v[230:231], v[206:207]\n"
"v_pk_fma_f32 v[232:233], v[206:207], v[206:207], v[232:233]\n"
"v_pk_add_f32 v[230:231], v[230:231], v[208:209]\n"
"v_pk_fma_f32 v[232:233], v[208:209], v[208:209], v[232:233]\n"
"v_pk_add_f32 v[230:231], v[230:231], v[210:211]\n"
"v_pk_fma_f32 v[232:233], v[210:211], v[210:211], v[232:233]\n"
"v_pk_add_f32 v[230:231], v[230:231], v[212:213]\n"
"v_pk_fma_f32 v[232:233], v[212:213], v[212:213], v[232:233]\n"
"v_add_f32 v230, v230, v231\n"
"v_add_f32 v232, v232, v233\n"
"s_nop 1\n"
"v_permlane32_swap_b32 v230, v232\n"
"v_add_f32 v230, v230, v232\n"
"v_mov_b32 v232, v230\n"
"s_nop 1\n"
"v_permlane16_swap_b32 v230, v232\n"
"v_add_f32 v230, v230, v232\n"
"buffer_store_dword v230, v36, s[96:99], 0 offen sc1\n"
"s_add_u32 m0, s59, 8192\n"
"s_nop 0\n"
"buffer_load_dwordx4 v1, s[20:23], m0 offen sc1 lds\n"
"s_add_u32 m0, s59, 65536\n"
"v_add_u32 v1, 4294914048, v1\n"
"buffer_load_dwordx4 v1, s[20:23], m0 offen sc1 lds\n"
"s_add_u32 m0, s59, 69632\n"
"s_nop 0\n"
"buffer_load_dwordx4 v1, s[20:23], m0 offen sc1 lds\n"
"s_mov_b32 s3, 1441792\n"
"s_waitcnt vmcnt(3)\n"
"buffer_store_dwordx2 v[8:9], v14, s[20:23], s3 offen sc1\n"
"s_barrier\n"
"s_add_u32 m0, s59, 135168\n"
"v_add_u32 v1, 4294905856, v1\n"
"buffer_load_dwordx4 v1, s[20:23], m0 offen sc1 lds\n"
"s_add_u32 m0, s59, 139264\n"
"s_nop 0\n"
"buffer_load_dwordx4 v1, s[20:23], m0 offen sc1 lds\n"
"s_add_u32 m0, s59, 143360\n"
"s_nop 0\n"
"buffer_load_dwordx4 v1, s[20:23], m0 offen sc1 lds\n"
"s_add_u32 m0, s59, 147456\n"
"s_nop 0\n"
"buffer_load_dwordx4 v1, s[20:23], m0 offen sc1 lds\n"
"s_add_u32 m0, s59, 151552\n"
"s_nop 0\n"
"buffer_load_dwordx4 v1, s[20:23], m0 offen sc1 lds\n"
"s_add_u32 m0, s59, 73728\n"
"v_add_u32 v1, 81920, v1\n"
"buffer_load_dwordx4 v1, s[20:23], m0 offen sc1 lds\n"
"s_add_u32 m0, s59, 77824\n"
"s_nop 0\n"
"buffer_load_dwordx4 v1, s[20:23], m0 offen sc1 lds\n"
"s_add_u32 m0, s59, 81920\n"
"s_nop 0\n"
"buffer_load_dwordx4 v1, s[20:23], m0 offen sc1 lds\n"
"s_barrier\n"
"s_mov_b32 s63, 1441792\n"
"s_mov_b32 s72, 0\n"
"L26_%=:\n"
"buffer_load_dwordx2 v[254:255], v12, s[20:23], s63 offen sc1\n"
"s_add_u32 s72, s72, 1\n"
"s_waitcnt vmcnt(0)\n"
"v_cmp_ne_u64 s[18:19], v[254:255], v[8:9]\n"
"s_nop 0\n"
"s_and_b32 s18, s18, 65535\n"
"s_cbranch_scc0 L25_%=\n"
"s_cmp_lt_u32 s72, 4194304\n"
"s_cbranch_scc1 L26_%=\n"
"L25_%=:\n"
"; P4 layer 1\n"
"s_mov_b32 s73, 6422528\n"
"s_mov_b32 s62, 6426624\n"
"buffer_load_dwordx4 a[0:3], v37, s[96:99], 0 offen sc1\n"
"buffer_load_dwordx4 a[4:7], v37, s[96:99], s84 offen sc1\n"
"buffer_load_dwordx4 a[8:11], v37, s[96:99], 0 offen offset:1024 sc1\n"
"buffer_load_dwordx4 a[12:15], v37, s[96:99], s84 offen offset:1024 sc1\n"
"buffer_load_dwordx4 a[16:19], v37, s[96:99], 0 offen offset:2048 sc1\n"
"buffer_load_dwordx4 a[20:23], v37, s[96:99], s84 offen offset:2048 sc1\n"
"buffer_load_dwordx4 a[24:27], v37, s[96:99], 0 offen offset:3072 sc1\n"
"buffer_load_dwordx4 a[28:31], v37, s[96:99], s84 offen offset:3072 sc1\n"
"buffer_load_dwordx4 a[32:35], v37, s[96:99], s80 offen sc1\n"
"buffer_load_dwordx4 a[36:39], v37, s[96:99], s85 offen sc1\n"
"buffer_load_dwordx4 a[40:43], v37, s[96:99], s80 offen offset:1024 sc1\n"
"buffer_load_dwordx4 a[44:47], v37, s[96:99], s85 offen offset:1024 sc1\n"
"ds_read_b128 v[54:57], v3 offset:28672\n"
"ds_read_b128 v[58:61], v3 offset:29696\n"
"ds_read_b128 v[62:65], v3 offset:30720\n"
"ds_read_b128 v[66:69], v3 offset:31744\n"
"ds_read_b128 v[70:73], v3 offset:32768\n"
"ds_read_b128 v[74:77], v3 offset:33792\n"
"s_waitcnt vmcnt(11) lgkmcnt(5)\n"
"v_mfma_f32_16x16x32_bf16 v[86:89], v[54:57], a[0:3], 0\n"
"s_waitcnt vmcnt(10) lgkmcnt(4)\n"
"ds_read_b128 v[78:81], v3 offset:34816\n"
"v_mfma_f32_16x16x32_bf16 v[90:93], v[54:57], a[4:7], 0\n"
"buffer_load_dwordx4 v[218:221], v38, s[96:99], s73 offen sc1\n"
"v_mfma_f32_16x16x32_bf16 v[94:97], v[58:61], a[0:3], 0\n"
"s_waitcnt lgkmcnt(4)\n"
"ds_read_b128 v[82:85], v3 offset:35840\n"
"v_mfma_f32_16x16x32_bf16 v[98:101], v[58:61], a[4:7], 0\n"
"v_mfma_f32_16x16x32_bf16 v[102:105], v[62:65], a[0:3], 0\n"
"s_waitcnt lgkmcnt(4)\n"
"ds_read_b128 v[54:57], v3 offset:36864\n"
"v_mfma_f32_16x16x32_bf16 v[106:109], v[62:65], a[4:7], 0\n"
"buffer_load_dwordx4 v[222:225], v38, s[96:99], s62 offen sc1\n"
"v_mfma_f32_16x16x32_bf16 v[110:113], v[66:69], a[0:3], 0\n"
"s_waitcnt vmcnt(11) lgkmcnt(4)\n"
"ds_read_b128 v[58:61], v3 offset:37888\n"
"v_mfma_f32_16x16x32_bf16 v[114:117], v[66:69], a[4:7], 0\n"
"v_mfma_f32_16x16x32_bf16 v[86:89], v[70:73], a[8:11], v[86:89]\n"
"s_waitcnt vmcnt(10) lgkmcnt(4)\n"
"ds_read_b128 v[62:65], v3 offset:38912\n"
"v_mfma_f32_16x16x32_bf16 v[90:93], v[70:73], a[12:15], v[90:93]\n"
"buffer_load_dwordx4 a[48:51], v37, s[96:99], s80 offen offset:2048 sc1\n"
"v_mfma_f32_16x16x32_bf16 v[94:97], v[74:77], a[8:11], v[94:97]\n"
"s_waitcnt lgkmcnt(4)\n"
"ds_read_b128 v[66:69], v3 offset:39936\n"
"v_mfma_f32_16x16x32_bf16 v[98:101], v[74:77], a[12:15], v[98:101]\n"
"v_mfma_f32_16x16x32_bf16 v[102:105], v[78:81], a[8:11], v[102:105]\n"
"s_waitcnt lgkmcnt(4)\n"
"ds_read_b128 v[70:73], v3 offset:40960\n"
"v_mfma_f32_16x16x32_bf16 v[106:109], v[78:81], a[12:15], v[106:109]\n"
"buffer_load_dwordx4 a[52:55], v37, s[96:99], s85 offen offset:2048 sc1\n"
"v_mfma_f32_16x16x32_bf16 v[110:113], v[82:85], a[8:11], v[110:113]\n"
"s_waitcnt vmcnt(11) lgkmcnt(4)\n"
"ds_read_b128 v[74:77], v3 offset:41984\n"
"v_mfma_f32_16x16x32_bf16 v[114:117], v[82:85], a[12:15], v[114:117]\n"
"v_mfma_f32_16x16x32_bf16 v[86:89], v[54:57], a[16:19], v[86:89]\n"
"s_waitcnt vmcnt(10) lgkmcnt(4)\n"
"ds_read_b128 v[78:81], v3 offset:43008\n"
"v_mfma_f32_16x16x32_bf16 v[90:93], v[54:57], a[20:23], v[90:93]\n"
"buffer_load_dwordx4 a[56:59], v37, s[96:99], s80 offen offset:3072 sc1\n"
"v_mfma_f32_16x16x32_bf16 v[94:97], v[58:61], a[16:19], v[94:97]\n"
"s_waitcnt lgkmcnt(4)\n"
"ds_read_b128 v[82:85], v3 offset:44032\n"
"v_mfma_f32_16x16x32_bf16 v[98:101], v[58:61], a[20:23], v[98:101]\n"
"v_mfma_f32_16x16x32_bf16 v[102:105], v[62:65], a[16:19], v[102:105]\n"
"s_waitcnt lgkmcnt(4)\n"
"ds_read_b128 v[54:57], v3 offset:45056\n"
"v_mfma_f32_16x16x32_bf16 v[106:109], v[62:65], a[20:23], v[106:109]\n"
"buffer_load_dwordx4 a[60:63], v37, s[96:99], s85 offen offset:3072 sc1\n"
"v_mfma_f32_16x16x32_bf16 v[110:113], v[66:69], a[16:19], v[110:113]\n"
"s_waitcnt vmcnt(11) lgkmcnt(4)\n"
"ds_read_b128 v[58:61], v3 offset:46080\n"
"v_mfma_f32_16x16x32_bf16 v[114:117], v[66:69], a[20:23], v[114:117]\n"
"v_mfma_f32_16x16x32_bf16 v[86:89], v[70:73], a[24:27], v[86:89]\n"
"s_waitcnt vmcnt(10) lgkmcnt(4)\n"
"ds_read_b128 v[62:65], v3 offset:47104\n"
"v_mfma_f32_16x16x32_bf16 v[90:93], v[70:73], a[28:31], v[90:93]\n"
"buffer_load_dwordx4 a[64:67], v37, s[96:99], s81 offen sc1\n"
"v_mfma_f32_16x16x32_bf16 v[94:97], v[74:77], a[24:27], v[94:97]\n"
"s_waitcnt lgkmcnt(4)\n"
"ds_read_b128 v[66:69], v3 offset:48128\n"
"v_mfma_f32_16x16x32_bf16 v[98:101], v[74:77], a[28:31], v[98:101]\n"
"v_mfma_f32_16x16x32_bf16 v[102:105], v[78:81], a[24:27], v[102:105]\n"
"s_waitcnt lgkmcnt(4)\n"
"ds_read_b128 v[70:73], v3 offset:49152\n"
"v_mfma_f32_16x16x32_bf16 v[106:109], v[78:81], a[28:31], v[106:109]\n"
"buffer_load_dwordx4 a[68:71], v37, s[96:99], s86 offen sc1\n"
"v_mfma_f32_16x16x32_bf16 v[110:113], v[82:85], a[24:27], v[110:113]\n"
"s_waitcnt vmcnt(11) lgkmcnt(4)\n"
"ds_read_b128 v[74:77], v3 offset:50176\n"
"v_mfma_f32_16x16x32_bf16 v[114:117], v[82:85], a[28:31], v[114:117]\n"
"v_mfma_f32_16x16x32_bf16 v[86:89], v[54:57], a[32:35], v[86:89]\n"
"s_waitcnt vmcnt(10) lgkmcnt(4)\n"
"ds_read_b128 v[78:81], v3 offset:51200\n"
"v_mfma_f32_16x16x32_bf16 v[90:93], v[54:57], a[36:39], v[90:93]\n"
"buffer_load_dwordx4 a[72:75], v37, s[96:99], s81 offen offset:1024 sc1\n"
"v_mfma_f32_16x16x32_bf16 v[94:97], v[58:61], a[32:35], v[94:97]\n"
"s_waitcnt lgkmcnt(4)\n"
"ds_read_b128 v[82:85], v3 offset:52224\n"
"v_mfma_f32_16x16x32_bf16 v[98:101], v[58:61], a[36:39], v[98:101]\n"
"v_mfma_f32_16x16x32_bf16 v[102:105], v[62:65], a[32:35], v[102:105]\n"
"s_waitcnt vmcnt(7) lgkmcnt(4)\n"
"ds_read_b128 v[54:57], v3 offset:53248\n"
"v_add_f32 v218, v218, v222\n"
"v_mfma_f32_16x16x32_bf16 v[106:109], v[62:65], a[36:39], v[106:109]\n"
"buffer_load_dwordx4 a[76:79], v37, s[96:99], s86 offen offset:1024 sc1\n"
"v_add_f32 v219, v219, v223\n"
"v_mfma_f32_16x16x32_bf16 v[110:113], v[66:69], a[32:35], v[110:113]\n"
"s_waitcnt lgkmcnt(4)\n"
"ds_read_b128 v[58:61], v3 offset:54272\n"
"v_add_f32 v220, v220, v224\n"
"v_mfma_f32_16x16x32_bf16 v[114:117], v[66:69], a[36:39], v[114:117]\n"
"v_add_f32 v221, v221, v225\n"
"v_mfma_f32_16x16x32_bf16 v[86:89], v[70:73], a[40:43], v[86:89]\n"
"s_waitcnt lgkmcnt(4)\n"
"ds_read_b128 v[62:65], v3 offset:55296\n"
"ds_read_b128 v[122:125], v50 offset:640\n"
"ds_read_b128 v[130:133], v50 offset:1152\n"
"v_mfma_f32_16x16x32_bf16 v[90:93], v[70:73], a[44:47], v[90:93]\n"
"buffer_load_dwordx4 a[80:83], v37, s[96:99], s81 offen offset:2048 sc1\n"
"v_mfma_f32_16x16x32_bf16 v[94:97], v[74:77], a[40:43], v[94:97]\n"
"s_waitcnt lgkmcnt(6)\n"
"ds_read_b128 v[66:69], v3 offset:56320\n"
"v_permlane32_swap_b32 v218, v220\n"
"v_mfma_f32_16x16x32_bf16 v[98:101], v[74:77], a[44:47], v[98:101]\n"
"v_permlane32_swap_b32 v219, v221\n"
"v_add_f32 v218, v218, v220\n"
"v_mfma_f32_16x16x32_bf16 v[102:105], v[78:81], a[40:43], v[102:105]\n"
"s_waitcnt lgkmcnt(6)\n"
"ds_read_b128 v[70:73], v3 offset:57344\n"
"v_add_f32 v219, v219, v221\n"
"v_mfma_f32_16x16x32_bf16 v[106:109], v[78:81], a[44:47], v[106:109]\n"
"buffer_load_dwordx4 a[84:87], v37, s[96:99], s86 offen offset:2048 sc1\n"
"v_mov_b32 v220, v218\n"
"v_mfma_f32_16x16x32_bf16 v[110:113], v[82:85], a[40:43], v[110:113]\n"
"s_waitcnt vmcnt(9) lgkmcnt(6)\n"
"ds_read_b128 v[74:77], v3 offset:58368\n"
"v_mov_b32 v221, v219\n"
"v_mfma_f32_16x16x32_bf16 v[114:117], v[82:85], a[44:47], v[114:117]\n"
"v_permlane16_swap_b32 v218, v220\n"
"v_mfma_f32_16x16x32_bf16 v[86:89], v[54:57], a[48:51], v[86:89]\n"
"s_waitcnt vmcnt(8) lgkmcnt(6)\n"
"ds_read_b128 v[78:81], v3 offset:59392\n"
"v_add_f32 v218, v218, v220\n"
"v_mul_f32 v218, 986500301, v218\n"
"v_mfma_f32_16x16x32_bf16 v[90:93], v[54:57], a[52:55], v[90:93]\n"
"buffer_load_dwordx4 a[88:91], v37, s[96:99], s81 offen offset:3072 sc1\n"
"v_mul_f32 v226, -1.0, v218\n"
"v_mfma_f32_16x16x32_bf16 v[94:97], v[58:61], a[48:51], v[94:97]\n"
"s_waitcnt lgkmcnt(6)\n"
"ds_read_b128 v[82:85], v3 offset:60416\n"
"v_mul_f32 v227, -1.0, v218\n"
"v_mfma_f32_16x16x32_bf16 v[98:101], v[58:61], a[52:55], v[98:101]\n"
"v_permlane16_swap_b32 v219, v221\n"
"v_mfma_f32_16x16x32_bf16 v[102:105], v[62:65], a[48:51], v[102:105]\n"
"s_waitcnt lgkmcnt(4)\n"
"ds_read_b128 v[54:57], v3 offset:61440\n"
"v_add_f32 v219, v219, v221\n"
"v_mul_f32 v219, 986500301, v219\n"
"v_mfma_f32_16x16x32_bf16 v[106:109], v[62:65], a[52:55], v[106:109]\n"
"buffer_load_dwordx4 a[92:95], v37, s[96:99], s86 offen offset:3072 sc1\n"
"v_fma_f32 v219, -v218, v218, v219\n"
"v_mfma_f32_16x16x32_bf16 v[110:113], v[66:69], a[48:51], v[110:113]\n"
"s_waitcnt vmcnt(9) lgkmcnt(4)\n"
"ds_read_b128 v[58:61], v3 offset:62464\n"
"v_max_f32 v219, 0, v219\n"
"v_mfma_f32_16x16x32_bf16 v[114:117], v[66:69], a[52:55], v[114:117]\n"
"v_permlane32_swap_b32 v226, v227\n"
"v_add_f32 v219, 925353388, v219\n"
"v_mfma_f32_16x16x32_bf16 v[86:89], v[70:73], a[56:59], v[86:89]\n"
"s_waitcnt vmcnt(8) lgkmcnt(4)\n"
"ds_read_b128 v[62:65], v3 offset:63488\n"
"v_mul_f32 v228, 1.0, v219\n"
"v_mfma_f32_16x16x32_bf16 v[90:93], v[70:73], a[60:63], v[90:93]\n"
"buffer_load_dwordx4 a[96:99], v37, s[96:99], s82 offen sc1\n"
"v_mul_f32 v229, 1.0, v219\n"
"v_mfma_f32_16x16x32_bf16 v[94:97], v[74:77], a[56:59], v[94:97]\n"
"s_waitcnt lgkmcnt(4)\n"
"ds_read_b128 v[66:69], v3 offset:64512\n"
"v_mul_f32 v118, v226, v122\n"
"v_mul_f32 v119, v226, v123\n"
"v_mfma_f32_16x16x32_bf16 v[98:101], v[74:77], a[60:63], v[98:101]\n"
"v_rsq_f32 v220, v219\n"
"v_mfma_f32_16x16x32_bf16 v[102:105], v[78:81], a[56:59], v[102:105]\n"
"s_waitcnt lgkmcnt(4)\n"
"ds_read_b128 v[70:73], v4\n"
"v_mul_f32 v230, 3216550459, v220\n"
"v_mul_f32 v231, 3216550459, v220\n"
"v_mfma_f32_16x16x32_bf16 v[106:109], v[78:81], a[60:63], v[106:109]\n"
"v_permlane32_swap_b32 v228, v229\n"
"v_mfma_f32_16x16x32_bf16 v[110:113], v[82:85], a[56:59], v[110:113]\n"
"s_waitcnt vmcnt(8) lgkmcnt(4)\n"
"ds_read_b128 v[74:77], v4 offset:1024\n"
"v_mul_f32 v120, v226, v124\n"
"v_mul_f32 v121, v226, v125\n"
"v_mfma_f32_16x16x32_bf16 v[114:117], v[82:85], a[60:63], v[114:117]\n"
"buffer_load_dwordx4 a[100:103], v37, s[96:99], s87 offen sc1\n"
"v_mfma_f32_16x16x32_bf16 v[86:89], v[54:57], a[64:67], v[86:89]\n"
"s_waitcnt vmcnt(8) lgkmcnt(4)\n"
"ds_read_b128 v[78:81], v4 offset:2048\n"
"v_mul_f32 v122, v227, v122\n"
"v_mul_f32 v123, v227, v123\n"
"v_mfma_f32_16x16x32_bf16 v[90:93], v[54:57], a[68:71], v[90:93]\n"
"v_permlane32_swap_b32 v230, v231\n"
"v_mfma_f32_16x16x32_bf16 v[94:97], v[58:61], a[64:67], v[94:97]\n"
"ds_read_b128 v[82:85], v4 offset:3072\n"
"v_mul_f32 v124, v227, v124\n"
"v_mul_f32 v125, v227, v125\n"
"v_mfma_f32_16x16x32_bf16 v[98:101], v[58:61], a[68:71], v[98:101]\n"
"s_barrier\n"
"s_waitcnt lgkmcnt(5)\n"
"v_mfma_f32_16x16x32_bf16 v[102:105], v[62:65], a[64:67], v[102:105]\n"
"s_waitcnt lgkmcnt(4)\n"
"ds_read_b128 v[54:57], v2 offset:12288\n"
"v_mul_f32 v126, v226, v130\n"
"v_mul_f32 v127, v226, v131\n"
"v_mfma_f32_16x16x32_bf16 v[106:109], v[62:65], a[68:71], v[106:109]\n"
"buffer_load_dwordx4 a[104:107], v37, s[96:99], s82 offen offset:1024 sc1\n"
"v_mfma_f32_16x16x32_bf16 v[110:113], v[66:69], a[64:67], v[110:113]\n"
"s_waitcnt vmcnt(8) lgkmcnt(4)\n"
"ds_read_b128 v[58:61], v2 offset:13312\n"
"v_mul_f32 v128, v226, v132\n"
"v_mul_f32 v129, v226, v133\n"
"v_mfma_f32_16x16x32_bf16 v[114:117], v[66:69], a[68:71], v[114:117]\n"
"buffer_load_dwordx4 a[108:111], v37, s[96:99], s87 offen offset:1024 sc1\n"
"v_mfma_f32_16x16x32_bf16 v[86:89], v[70:73], a[72:75], v[86:89]\n"
"s_waitcnt vmcnt(8) lgkmcnt(4)\n"
"ds_read_b128 v[62:65], v2 offset:14336\n"
"v_mul_f32 v130, v227, v130\n"
"v_mul_f32 v131, v227, v131\n"
"v_mfma_f32_16x16x32_bf16 v[90:93], v[70:73], a[76:79], v[90:93]\n"
"v_mul_f32 v132, v227, v132\n"
"v_mul_f32 v133, v227, v133\n"
"v_mfma_f32_16x16x32_bf16 v[94:97], v[74:77], a[72:75], v[94:97]\n"
"s_waitcnt lgkmcnt(4)\n"
"ds_read_b128 v[66:69], v2 offset:15360\n"
"ds_read_b128 v[138:141], v50 offset:704\n"
"ds_read_b128 v[146:149], v50 offset:1216\n"
"v_mfma_f32_16x16x32_bf16 v[98:101], v[74:77], a[76:79], v[98:101]\n"
"buffer_load_dwordx4 a[112:115], v37, s[96:99], s82 offen offset:2048 sc1\n"
"v_mfma_f32_16x16x32_bf16 v[102:105], v[78:81], a[72:75], v[102:105]\n"
"s_waitcnt lgkmcnt(6)\n"
"ds_read_b128 v[70:73], v2 offset:16384\n"
"ds_read_b128 v[154:157], v50 offset:768\n"
"ds_read_b128 v[162:165], v50 offset:1280\n"
"v_mfma_f32_16x16x32_bf16 v[106:109], v[78:81], a[76:79], v[106:109]\n"
"v_mfma_f32_16x16x32_bf16 v[110:113], v[82:85], a[72:75], v[110:113]\n"
"s_waitcnt vmcnt(8) lgkmcnt(8)\n"
"ds_read_b128 v[74:77], v2 offset:17408\n"
"v_mfma_f32_16x16x32_bf16 v[114:117], v[82:85], a[76:79], v[114:117]\n"
"buffer_load_dwordx4 a[116:119], v37, s[96:99], s87 offen offset:2048 sc1\n"
"v_mfma_f32_16x16x32_bf16 v[86:89], v[54:57], a[80:83], v[86:89]\n"
"s_waitcnt vmcnt(8) lgkmcnt(5)\n"
"ds_read_b128 v[78:81], v2 offset:18432\n"
"v_mul_f32 v134, v226, v138\n"
"v_mul_f32 v135, v226, v139\n"
"v_mul_f32 v136, v226, v140\n"
"v_mfma_f32_16x16x32_bf16 v[90:93], v[54:57], a[84:87], v[90:93]\n"
"v_mul_f32 v137, v226, v141\n"
"v_mul_f32 v138, v227, v138\n"
"v_mfma_f32_16x16x32_bf16 v[94:97], v[58:61], a[80:83], v[94:97]\n"
"ds_read_b128 v[82:85], v2 offset:19456\n"
"v_mul_f32 v139, v227, v139\n"
"v_mul_f32 v140, v227, v140\n"
"v_mfma_f32_16x16x32_bf16 v[98:101], v[58:61], a[84:87], v[98:101]\n"
"buffer_load_dwordx4 a[120:123], v37, s[96:99], s82 offen offset:3072 sc1\n"
"v_mfma_f32_16x16x32_bf16 v[102:105], v[62:65], a[80:83], v[102:105]\n"
"s_waitcnt lgkmcnt(6)\n"
"ds_read_b128 v[54:57], v2 offset:20480\n"
"v_mul_f32 v141, v227, v141\n"
"v_mul_f32 v142, v226, v146\n"
"v_mfma_f32_16x16x32_bf16 v[106:109], v[62:65], a[84:87], v[106:109]\n"
"v_mul_f32 v143, v226, v147\n"
"v_mul_f32 v144, v226, v148\n"
"v_mul_f32 v145, v226, v149\n"
"v_mfma_f32_16x16x32_bf16 v[110:113], v[66:69], a[80:83], v[110:113]\n"
"s_waitcnt vmcnt(8) lgkmcnt(6)\n"
"ds_read_b128 v[58:61], v2 offset:21504\n"
"v_mul_f32 v146, v227, v146\n"
"v_mfma_f32_16x16x32_bf16 v[114:117], v[66:69], a[84:87], v[114:117]\n"
"buffer_load_dwordx4 a[124:127], v37, s[96:99], s87 offen offset:3072 sc1\n"
"v_mul_f32 v147, v227, v147\n"
"v_mfma_f32_16x16x32_bf16 v[86:89], v[70:73], a[88:91], v[86:89]\n"
"s_waitcnt vmcnt(8) lgkmcnt(4)\n"
"ds_read_b128 v[62:65], v2 offset:22528\n"
"v_mul_f32 v148, v227, v148\n"
"v_mfma_f32_16x16x32_bf16 v[90:93], v[70:73], a[92:95], v[90:93]\n"
"v_mul_f32 v149, v227, v149\n"
"v_mul_f32 v150, v226, v154\n"
"v_mul_f32 v151, v226, v155\n"
"v_mfma_f32_16x16x32_bf16 v[94:97], v[74:77], a[88:91], v[94:97]\n"
"s_waitcnt lgkmcnt(4)\n"
"ds_read_b128 v[66:69], v2 offset:23552\n"
"v_mul_f32 v152, v226, v156\n"
"v_mfma_f32_16x16x32_bf16 v[98:101], v[74:77], a[92:95], v[98:101]\n"
"buffer_load_dwordx4 a[128:131], v37, s[96:99], s83 offen sc1\n"
"v_mul_f32 v153, v226, v157\n"
"v_mfma_f32_16x16x32_bf16 v[102:105], v[78:81], a[88:91], v[102:105]\n"
"s_waitcnt lgkmcnt(4)\n"
"ds_read_b128 v[70:73], v2 offset:24576\n"
"v_mul_f32 v154, v227, v154\n"
"v_mfma_f32_16x16x32_bf16 v[106:109], v[78:81], a[92:95], v[106:109]\n"
"v_mul_f32 v155, v227, v155\n"
"v_mul_f32 v156, v227, v156\n"
"v_mul_f32 v157, v227, v157\n"
"v_mfma_f32_16x16x32_bf16 v[110:113], v[82:85], a[88:91], v[110:113]\n"
"s_waitcnt vmcnt(8) lgkmcnt(4)\n"
"ds_read_b128 v[74:77], v2 offset:25600\n"
"v_mul_f32 v158, v226, v162\n"
"v_mfma_f32_16x16x32_bf16 v[114:117], v[82:85], a[92:95], v[114:117]\n"
"buffer_load_dwordx4 a[132:135], v37, s[96:99], s88 offen sc1\n"
"v_mul_f32 v159, v226, v163\n"
"v_mfma_f32_16x16x32_bf16 v[86:89], v[54:57], a[96:99], v[86:89]\n"
"s_waitcnt vmcnt(8) lgkmcnt(4)\n"
"ds_read_b128 v[78:81], v2 offset:26624\n"
"v_mul_f32 v160, v226, v164\n"
"v_mfma_f32_16x16x32_bf16 v[90:93], v[54:57], a[100:103], v[90:93]\n"
"v_mul_f32 v161, v226, v165\n"
"v_mul_f32 v162, v227, v162\n"
"v_mul_f32 v163, v227, v163\n"
"v_mfma_f32_16x16x32_bf16 v[94:97], v[58:61], a[96:99], v[94:97]\n"
"s_waitcnt lgkmcnt(4)\n"
"ds_read_b128 v[82:85], v2 offset:27648\n"
"v_mul_f32 v164, v227, v164\n"
"v_mfma_f32_16x16x32_bf16 v[98:101], v[58:61], a[100:103], v[98:101]\n"
"buffer_load_dwordx4 a[136:139], v37, s[96:99], s83 offen offset:1024 sc1\n"
"v_mul_f32 v165, v227, v165\n"
"v_mfma_f32_16x16x32_bf16 v[102:105], v[62:65], a[96:99], v[102:105]\n"
"s_waitcnt lgkmcnt(4)\n"
"ds_read_b128 v[54:57], v2 offset:28672\n"
"ds_read_b128 v[170:173], v50 offset:832\n"
"v_mfma_f32_16x16x32_bf16 v[106:109], v[62:65], a[100:103], v[106:109]\n"
"ds_read_b128 v[178:181], v50 offset:1344\n"
"ds_read_b128 v[186:189], v50 offset:896\n"
"ds_read_b128 v[194:197], v50 offset:1408\n"
"v_mfma_f32_16x16x32_bf16 v[110:113], v[66:69], a[96:99], v[110:113]\n"
"s_waitcnt vmcnt(8) lgkmcnt(8)\n"
"ds_read_b128 v[58:61], v2 offset:29696\n"
"v_mfma_f32_16x16x32_bf16 v[114:117], v[66:69], a[100:103], v[114:117]\n"
"buffer_load_dwordx4 a[140:143], v37, s[96:99], s88 offen offset:1024 sc1\n"
"v_mfma_f32_16x16x32_bf16 v[86:89], v[70:73], a[104:107], v[86:89]\n"
"s_waitcnt vmcnt(8) lgkmcnt(8)\n"
"ds_read_b128 v[62:65], v2 offset:30720\n"
"v_mfma_f32_16x16x32_bf16 v[90:93], v[70:73], a[108:111], v[90:93]\n"
"v_mfma_f32_16x16x32_bf16 v[94:97], v[74:77], a[104:107], v[94:97]\n"
"s_waitcnt lgkmcnt(5)\n"
"ds_read_b128 v[66:69], v2 offset:31744\n"
"v_mul_f32 v166, v226, v170\n"
"v_mul_f32 v167, v226, v171\n"
"v_mul_f32 v168, v226, v172\n"
"v_mul_f32 v169, v226, v173\n"
"v_mfma_f32_16x16x32_bf16 v[98:101], v[74:77], a[108:111], v[98:101]\n"
"buffer_load_dwordx4 a[144:147], v37, s[96:99], s83 offen offset:2048 sc1\n"
"v_mfma_f32_16x16x32_bf16 v[102:105], v[78:81], a[104:107], v[102:105]\n"
"ds_read_b128 v[70:73], v2 offset:32768\n"
"v_mul_f32 v170, v227, v170\n"
"v_mul_f32 v171, v227, v171\n"
"v_mfma_f32_16x16x32_bf16 v[106:109], v[78:81], a[108:111], v[106:109]\n"
"v_mul_f32 v172, v227, v172\n"
"v_mul_f32 v173, v227, v173\n"
"v_mfma_f32_16x16x32_bf16 v[110:113], v[82:85], a[104:107], v[110:113]\n"
"s_waitcnt vmcnt(8) lgkmcnt(6)\n"
"ds_read_b128 v[74:77], v2 offset:33792\n"
"v_mul_f32 v174, v226, v178\n"
"v_mul_f32 v175, v226, v179\n"
"v_mfma_f32_16x16x32_bf16 v[114:117], v[82:85], a[108:111], v[114:117]\n"
"buffer_load_dwordx4 a[148:151], v37, s[96:99], s88 offen offset:2048 sc1\n"
"v_mfma_f32_16x16x32_bf16 v[86:89], v[54:57], a[112:115], v[86:89]\n"
"s_waitcnt vmcnt(8) lgkmcnt(4)\n"
"ds_read_b128 v[78:81], v2 offset:34816\n"
"v_mul_f32 v176, v226, v180\n"
"v_mul_f32 v177, v226, v181\n"
"v_mfma_f32_16x16x32_bf16 v[90:93], v[54:57], a[116:119], v[90:93]\n"
"v_mul_f32 v178, v227, v178\n"
"v_mul_f32 v179, v227, v179\n"
"v_mfma_f32_16x16x32_bf16 v[94:97], v[58:61], a[112:115], v[94:97]\n"
"s_waitcnt lgkmcnt(4)\n"
"ds_read_b128 v[82:85], v2 offset:35840\n"
"v_mul_f32 v180, v227, v180\n"
"v_mul_f32 v181, v227, v181\n"
"v_mfma_f32_16x16x32_bf16 v[98:101], v[58:61], a[116:119], v[98:101]\n"
"buffer_load_dwordx4 a[152:155], v37, s[96:99], s83 offen offset:3072 sc1\n"
"v_mfma_f32_16x16x32_bf16 v[102:105], v[62:65], a[112:115], v[102:105]\n"
"s_waitcnt lgkmcnt(4)\n"
"ds_read_b128 v[54:57], v2 offset:36864\n"
"v_mul_f32 v182, v226, v186\n"
"v_mul_f32 v183, v226, v187\n"
"v_mfma_f32_16x16x32_bf16 v[106:109], v[62:65], a[116:119], v[106:109]\n"
"v_mul_f32 v184, v226, v188\n"
"v_mul_f32 v185, v226, v189\n"
"v_mul_f32 v186, v227, v186\n"
"v_mfma_f32_16x16x32_bf16 v[110:113], v[66:69], a[112:115], v[110:113]\n"
"s_waitcnt vmcnt(8) lgkmcnt(4)\n"
"ds_read_b128 v[58:61], v2 offset:37888\n"
"v_mul_f32 v187, v227, v187\n"
"v_mfma_f32_16x16x32_bf16 v[114:117], v[66:69], a[116:119], v[114:117]\n"
"buffer_load_dwordx4 a[156:159], v37, s[96:99], s88 offen offset:3072 sc1\n"
"v_mul_f32 v188, v227, v188\n"
"v_mfma_f32_16x16x32_bf16 v[86:89], v[70:73], a[120:123], v[86:89]\n"
"s_waitcnt vmcnt(8) lgkmcnt(4)\n"
"ds_read_b128 v[62:65], v2 offset:38912\n"
"v_mul_f32 v189, v227, v189\n"
"v_mfma_f32_16x16x32_bf16 v[90:93], v[70:73], a[124:127], v[90:93]\n"
"v_mul_f32 v190, v226, v194\n"
"v_mul_f32 v191, v226, v195\n"
"v_mul_f32 v192, v226, v196\n"
"v_mfma_f32_16x16x32_bf16 v[94:97], v[74:77], a[120:123], v[94:97]\n"
"s_waitcnt lgkmcnt(4)\n"
"ds_read_b128 v[66:69], v2 offset:39936\n"
"v_mul_f32 v193, v226, v197\n"
"v_mfma_f32_16x16x32_bf16 v[98:101], v[74:77], a[124:127], v[98:101]\n"
"s_add_u32 m0, s59, 86016\n"
"v_mul_f32 v194, v227, v194\n"
"v_mul_f32 v195, v227, v195\n"
"v_mul_f32 v196, v227, v196\n"
"v_mfma_f32_16x16x32_bf16 v[102:105], v[78:81], a[120:123], v[102:105]\n"
"s_waitcnt lgkmcnt(4)\n"
"ds_read_b128 v[70:73], v2 offset:40960\n"
"v_mul_f32 v197, v227, v197\n"
"v_mfma_f32_16x16x32_bf16 v[106:109], v[78:81], a[124:127], v[106:109]\n"
"buffer_load_dwordx4 v1, s[20:23], m0 offen sc1 lds\n"
"ds_read_b128 v[232:235], v50 offset:512\n"
"v_mfma_f32_16x16x32_bf16 v[110:113], v[82:85], a[120:123], v[110:113]\n"
"s_waitcnt vmcnt(8) lgkmcnt(5)\n"
"ds_read_b128 v[74:77], v2 offset:41984\n"
"ds_read_b128 v[236:239], v50 offset:1024\n"
"v_mfma_f32_16x16x32_bf16 v[114:117], v[82:85], a[124:127], v[114:117]\n"
"s_add_u32 m0, s59, 90112\n"
"v_mfma_f32_16x16x32_bf16 v[86:89], v[54:57], a[128:131], v[86:89]\n"
"s_waitcnt vmcnt(7) lgkmcnt(6)\n"
"ds_read_b128 v[78:81], v2 offset:43008\n"
"v_mfma_f32_16x16x32_bf16 v[90:93], v[54:57], a[132:135], v[90:93]\n"
"buffer_load_dwordx4 v1, s[20:23], m0 offen sc1 lds\n"
"v_mfma_f32_16x16x32_bf16 v[94:97], v[58:61], a[128:131], v[94:97]\n"
"s_waitcnt lgkmcnt(6)\n"
"ds_read_b128 v[82:85], v2 offset:44032\n"
"v_mfma_f32_16x16x32_bf16 v[98:101], v[58:61], a[132:135], v[98:101]\n"
"s_add_u32 m0, s59, 94208\n"
"v_mfma_f32_16x16x32_bf16 v[102:105], v[62:65], a[128:131], v[102:105]\n"
"s_waitcnt lgkmcnt(6)\n"
"ds_read_b128 v[54:57], v2 offset:45056\n"
"v_mfma_f32_16x16x32_bf16 v[106:109], v[62:65], a[132:135], v[106:109]\n"
"buffer_load_dwordx4 v1, s[20:23], m0 offen sc1 lds\n"
"v_mfma_f32_16x16x32_bf16 v[110:113], v[66:69], a[128:131], v[110:113]\n"
"s_waitcnt vmcnt(8) lgkmcnt(6)\n"
"ds_read_b128 v[58:61], v2 offset:46080\n"
"v_mfma_f32_16x16x32_bf16 v[114:117], v[66:69], a[132:135], v[114:117]\n"
"s_add_u32 m0, s59, 98304\n"
"v_mfma_f32_16x16x32_bf16 v[86:89], v[70:73], a[136:139], v[86:89]\n"
"s_waitcnt vmcnt(7) lgkmcnt(5)\n"
"ds_read_b128 v[62:65], v2 offset:47104\n"
"v_mfma_f32_16x16x32_bf16 v[90:93], v[70:73], a[140:143], v[90:93]\n"
"buffer_load_dwordx4 v1, s[20:23], m0 offen sc1 lds\n"
"v_mfma_f32_16x16x32_bf16 v[94:97], v[74:77], a[136:139], v[94:97]\n"
"s_waitcnt lgkmcnt(4)\n"
"ds_read_b128 v[66:69], v2 offset:48128\n"
"v_mfma_f32_16x16x32_bf16 v[98:101], v[74:77], a[140:143], v[98:101]\n"
"s_add_u32 m0, s59, 102400\n"
"v_mfma_f32_16x16x32_bf16 v[102:105], v[78:81], a[136:139], v[102:105]\n"
"s_waitcnt lgkmcnt(4)\n"
"ds_read_b128 v[70:73], v2 offset:49152\n"
"v_mfma_f32_16x16x32_bf16 v[106:109], v[78:81], a[140:143], v[106:109]\n"
"buffer_load_dwordx4 v1, s[20:23], m0 offen sc1 lds\n"
"v_mfma_f32_16x16x32_bf16 v[110:113], v[82:85], a[136:139], v[110:113]\n"
"s_waitcnt vmcnt(8) lgkmcnt(4)\n"
"ds_read_b128 v[74:77], v2 offset:50176\n"
"v_mfma_f32_16x16x32_bf16 v[114:117], v[82:85], a[140:143], v[114:117]\n"
"s_add_u32 m0, s59, 106496\n"
"v_mfma_f32_16x16x32_bf16 v[86:89], v[54:57], a[144:147], v[86:89]\n"
"s_waitcnt vmcnt(7) lgkmcnt(4)\n"
"ds_read_b128 v[78:81], v2 offset:51200\n"
"v_mfma_f32_16x16x32_bf16 v[90:93], v[54:57], a[148:151], v[90:93]\n"
"buffer_load_dwordx4 v1, s[20:23], m0 offen sc1 lds\n"
"v_mfma_f32_16x16x32_bf16 v[94:97], v[58:61], a[144:147], v[94:97]\n"
"ds_read_b128 v[82:85], v2 offset:52224\n"
"v_mfma_f32_16x16x32_bf16 v[98:101], v[58:61], a[148:151], v[98:101]\n"
"s_barrier\n"
"s_waitcnt lgkmcnt(5)\n"
"v_mfma_f32_16x16x32_bf16 v[102:105], v[62:65], a[144:147], v[102:105]\n"
"s_waitcnt lgkmcnt(4)\n"
"ds_read_b128 v[54:57], v2 offset:53248\n"
"v_mfma_f32_16x16x32_bf16 v[106:109], v[62:65], a[148:151], v[106:109]\n"
"s_add_u32 m0, s59, 110592\n"
"v_mfma_f32_16x16x32_bf16 v[110:113], v[66:69], a[144:147], v[110:113]\n"
"s_waitcnt vmcnt(7) lgkmcnt(4)\n"
"ds_read_b128 v[58:61], v2 offset:54272\n"
"v_mfma_f32_16x16x32_bf16 v[114:117], v[66:69], a[148:151], v[114:117]\n"
"buffer_load_dwordx4 v1, s[20:23], m0 offen sc1 lds\n"
"v_mfma_f32_16x16x32_bf16 v[86:89], v[70:73], a[152:155], v[86:89]\n"
"s_waitcnt vmcnt(7) lgkmcnt(4)\n"
"ds_read_b128 v[62:65], v2 offset:55296\n"
"v_mfma_f32_16x16x32_bf16 v[90:93], v[70:73], a[156:159], v[90:93]\n"
"s_add_u32 m0, s59, 114688\n"
"v_mfma_f32_16x16x32_bf16 v[94:97], v[74:77], a[152:155], v[94:97]\n"
"s_waitcnt lgkmcnt(4)\n"
"ds_read_b128 v[66:69], v2 offset:56320\n"
"v_mfma_f32_16x16x32_bf16 v[98:101], v[74:77], a[156:159], v[98:101]\n"
"buffer_load_dwordx4 v1, s[20:23], m0 offen sc1 lds\n"
"v_mfma_f32_16x16x32_bf16 v[102:105], v[78:81], a[152:155], v[102:105]\n"
"s_waitcnt lgkmcnt(4)\n"
"ds_read_b128 v[70:73], v2 offset:57344\n"
"v_mfma_f32_16x16x32_bf16 v[106:109], v[78:81], a[156:159], v[106:109]\n"
"s_add_u32 m0, s59, 118784\n"
"v_mfma_f32_16x16x32_bf16 v[110:113], v[82:85], a[152:155], v[110:113]\n"
"s_waitcnt lgkmcnt(4)\n"
"ds_read_b128 v[74:77], v2 offset:58368\n"
"v_mfma_f32_16x16x32_bf16 v[114:117], v[82:85], a[156:159], v[114:117]\n"
"buffer_load_dwordx4 v1, s[20:23], m0 offen sc1 lds\n"
"v_mfma_f32_16x16x32_bf16 v[118:121], v[54:57], a[0:3], v[118:121]\n"
"s_waitcnt lgkmcnt(4)\n"
"ds_read_b128 v[78:81], v2 offset:59392\n"
"v_mfma_f32_16x16x32_bf16 v[122:125], v[54:57], a[4:7], v[122:125]\n"
"s_add_u32 m0, s59, 122880\n"
"v_mfma_f32_16x16x32_bf16 v[126:129], v[58:61], a[0:3], v[126:129]\n"
"s_waitcnt lgkmcnt(4)\n"
"ds_read_b128 v[82:85], v2 offset:60416\n"
"v_mfma_f32_16x16x32_bf16 v[130:133], v[58:61], a[4:7], v[130:133]\n"
"buffer_load_dwordx4 v1, s[20:23], m0 offen sc1 lds\n"
"v_fmac_f32 v86, v226, v232\n"
"v_mul_f32 v218, v230, v86\n"
"v_fmac_f32 v87, v226, v233\n"
"v_mfma_f32_16x16x32_bf16 v[118:121], v[62:65], a[8:11], v[118:121]\n"
"s_waitcnt lgkmcnt(4)\n"
"ds_read_b128 v[54:57], v2 offset:61440\n"
"v_mul_f32 v219, v230, v87\n"
"v_fmac_f32 v94, v226, v236\n"
"v_mfma_f32_16x16x32_bf16 v[122:125], v[62:65], a[12:15], v[122:125]\n"
"s_add_u32 m0, s59, 126976\n"
"v_exp_f32 v218, v218\n"
"v_mul_f32 v86, v86, v94\n"
"v_mfma_f32_16x16x32_bf16 v[126:129], v[66:69], a[8:11], v[126:129]\n"
"s_waitcnt lgkmcnt(4)\n"
"ds_read_b128 v[58:61], v2 offset:62464\n"
"v_fma_f32 v218, v218, v228, v228\n"
"v_fmac_f32 v88, v226, v234\n"
"v_mfma_f32_16x16x32_bf16 v[130:133], v[66:69], a[12:15], v[130:133]\n"
"buffer_load_dwordx4 v1, s[20:23], m0 offen sc1 lds\n"
"v_mul_f32 v220, v230, v88\n"
"v_mfma_f32_16x16x32_bf16 v[118:121], v[70:73], a[16:19], v[118:121]\n"
"s_waitcnt lgkmcnt(4)\n"
"ds_read_b128 v[62:65], v2 offset:63488\n"
"v_fmac_f32 v95, v226, v237\n"
"v_mul_f32 v87, v87, v95\n"
"v_mfma_f32_16x16x32_bf16 v[122:125], v[70:73], a[20:23], v[122:125]\n"
"s_add_u32 m0, s59, 131072\n"
"v_exp_f32 v219, v219\n"
"v_fmac_f32 v89, v226, v235\n"
"v_mfma_f32_16x16x32_bf16 v[126:129], v[74:77], a[16:19], v[126:129]\n"
"s_waitcnt lgkmcnt(4)\n"
"ds_read_b128 v[66:69], v2 offset:64512\n"
"v_fma_f32 v219, v219, v228, v228\n"
"v_rcp_f32 v218, v218\n"
"v_mfma_f32_16x16x32_bf16 v[130:133], v[74:77], a[20:23], v[130:133]\n"
"buffer_load_dwordx4 v1, s[20:23], m0 offen sc1 lds\n"
"v_mul_f32 v86, v86, v218\n"
"v_mfma_f32_16x16x32_bf16 v[118:121], v[78:81], a[24:27], v[118:121]\n"
"s_waitcnt lgkmcnt(4)\n"
"ds_read_b128 v[70:73], v2\n"
"v_mul_f32 v221, v230, v89\n"
"v_fmac_f32 v96, v226, v238\n"
"v_mfma_f32_16x16x32_bf16 v[122:125], v[78:81], a[28:31], v[122:125]\n"
"s_add_u32 m0, s59, 12288\n"
"v_add_u32 v1, 122880, v1\n"
"v_mul_f32 v88, v88, v96\n"
"v_fmac_f32 v90, v227, v232\n"
"v_mfma_f32_16x16x32_bf16 v[126:129], v[82:85], a[24:27], v[126:129]\n"
"s_waitcnt lgkmcnt(4)\n"
"ds_read_b128 v[74:77], v2 offset:1024\n"
"v_mul_f32 v222, v231, v90\n"
"v_fmac_f32 v97, v226, v239\n"
"v_mfma_f32_16x16x32_bf16 v[130:133], v[82:85], a[28:31], v[130:133]\n"
"buffer_load_dwordx4 v1, s[20:23], m0 offen sc1 lds\n"
"v_mul_f32 v89, v89, v97\n"
"v_mfma_f32_16x16x32_bf16 v[118:121], v[54:57], a[32:35], v[118:121]\n"
"s_waitcnt lgkmcnt(4)\n"
"ds_read_b128 v[78:81], v2 offset:2048\n"
"v_fmac_f32 v91, v227, v233\n"
"v_mul_f32 v223, v231, v91\n"
"v_mfma_f32_16x16x32_bf16 v[122:125], v[54:57], a[36:39], v[122:125]\n"
"s_add_u32 m0, s59, 16384\n"
"v_exp_f32 v220, v220\n"
"v_rcp_f32 v219, v219\n"
"v_mfma_f32_16x16x32_bf16 v[126:129], v[58:61], a[32:35], v[126:129]\n"
"s_waitcnt lgkmcnt(4)\n"
"ds_read_b128 v[82:85], v2 offset:3072\n"
"v_fma_f32 v220, v220, v228, v228\n"
"v_mul_f32 v87, v87, v219\n"
"v_mfma_f32_16x16x32_bf16 v[130:133], v[58:61], a[36:39], v[130:133]\n"
"v_exp_f32 v221, v221\n"
"v_cvt_pk_bf16_f32 v94, v86, v87\n"
"v_mfma_f32_16x16x32_bf16 v[118:121], v[62:65], a[40:43], v[118:121]\n"
"s_waitcnt lgkmcnt(4)\n"
"ds_read_b128 v[54:57], v2 offset:4096\n"
"v_fma_f32 v221, v221, v228, v228\n"
"v_fmac_f32 v98, v227, v236\n"
"v_mfma_f32_16x16x32_bf16 v[122:125], v[62:65], a[44:47], v[122:125]\n"
"buffer_load_dwordx4 v1, s[20:23], m0 offen sc1 lds\n"
"v_mul_f32 v90, v90, v98\n"
"v_mfma_f32_16x16x32_bf16 v[126:129], v[66:69], a[40:43], v[126:129]\n"
"s_waitcnt lgkmcnt(4)\n"
"ds_read_b128 v[58:61], v2 offset:5120\n"
"v_fmac_f32 v92, v227, v234\n"
"v_mul_f32 v224, v231, v92\n"
"v_mfma_f32_16x16x32_bf16 v[130:133], v[66:69], a[44:47], v[130:133]\n"
"s_add_u32 m0, s59, 20480\n"
"v_rcp_f32 v220, v220\n"
"v_fmac_f32 v99, v227, v237\n"
"v_mfma_f32_16x16x32_bf16 v[118:121], v[70:73], a[48:51], v[118:121]\n"
"s_waitcnt lgkmcnt(4)\n"
"ds_read_b128 v[62:65], v2 offset:6144\n"
"v_mul_f32 v88, v88, v220\n"
"v_exp_f32 v222, v222\n"
"v_mfma_f32_16x16x32_bf16 v[122:125], v[70:73], a[52:55], v[122:125]\n"
"buffer_load_dwordx4 v1, s[20:23], m0 offen sc1 lds\n"
"v_fma_f32 v222, v222, v229, v229\n"
"v_mfma_f32_16x16x32_bf16 v[126:129], v[74:77], a[48:51], v[126:129]\n"
"s_waitcnt lgkmcnt(4)\n"
"ds_read_b128 v[66:69], v2 offset:7168\n"
"v_mul_f32 v91, v91, v99\n"
"v_fmac_f32 v93, v227, v235\n"
"v_mfma_f32_16x16x32_bf16 v[130:133], v[74:77], a[52:55], v[130:133]\n"
"s_add_u32 m0, s59, 24576\n"
"v_rcp_f32 v221, v221\n"
"v_mul_f32 v225, v231, v93\n"
"v_mfma_f32_16x16x32_bf16 v[118:121], v[78:81], a[56:59], v[118:121]\n"
"s_waitcnt lgkmcnt(4)\n"
"ds_read_b128 v[70:73], v2 offset:8192\n"
"v_mul_f32 v89, v89, v221\n"
"v_cvt_pk_bf16_f32 v95, v88, v89\n"
"v_mfma_f32_16x16x32_bf16 v[122:125], v[78:81], a[60:63], v[122:125]\n"
"buffer_load_dwordx4 v1, s[20:23], m0 offen sc1 lds\n"
"v_fmac_f32 v100, v227, v238\n"
"v_mfma_f32_16x16x32_bf16 v[126:129], v[82:85], a[56:59], v[126:129]\n"
"s_waitcnt lgkmcnt(4)\n"
"ds_read_b128 v[74:77], v2 offset:9216\n"
"v_mul_f32 v92, v92, v100\n"
"v_fmac_f32 v101, v227, v239\n"
"v_mfma_f32_16x16x32_bf16 v[130:133], v[82:85], a[60:63], v[130:133]\n"
"s_add_u32 m0, s59, 28672\n"
"v_exp_f32 v223, v223\n"
"v_mul_f32 v93, v93, v101\n"
"v_mfma_f32_16x16x32_bf16 v[118:121], v[54:57], a[64:67], v[118:121]\n"
"s_waitcnt lgkmcnt(4)\n"
"ds_read_b128 v[78:81], v2 offset:10240\n"
"v_fma_f32 v223, v223, v229, v229\n"
"v_rcp_f32 v222, v222\n"
"v_mfma_f32_16x16x32_bf16 v[122:125], v[54:57], a[68:71], v[122:125]\n"
"buffer_load_dwordx4 v1, s[20:23], m0 offen sc1 lds\n"
"v_mul_f32 v90, v90, v222\n"
"v_mfma_f32_16x16x32_bf16 v[126:129], v[58:61], a[64:67], v[126:129]\n"
"s_waitcnt lgkmcnt(4)\n"
"ds_read_b128 v[82:85], v2 offset:11264\n"
"v_exp_f32 v224, v224\n"
"v_mfma_f32_16x16x32_bf16 v[130:133], v[58:61], a[68:71], v[130:133]\n"
"s_add_u32 m0, s59, 32768\n"
"v_rcp_f32 v223, v223\n"
"v_fma_f32 v224, v224, v229, v229\n"
"v_mfma_f32_16x16x32_bf16 v[118:121], v[62:65], a[72:75], v[118:121]\n"
"s_waitcnt lgkmcnt(4)\n"
"ds_read_b128 v[54:57], v3\n"
"v_mul_f32 v91, v91, v223\n"
"v_cvt_pk_bf16_f32 v96, v90, v91\n"
"v_mfma_f32_16x16x32_bf16 v[122:125], v[62:65], a[76:79], v[122:125]\n"
"v_exp_f32 v225, v225\n"
"v_mfma_f32_16x16x32_bf16 v[126:129], v[66:69], a[72:75], v[126:129]\n"
"s_waitcnt lgkmcnt(4)\n"
"ds_read_b128 v[58:61], v3 offset:1024\n"
"v_fma_f32 v225, v225, v229, v229\n"
"v_rcp_f32 v224, v224\n"
"v_mfma_f32_16x16x32_bf16 v[130:133], v[66:69], a[76:79], v[130:133]\n"
"buffer_load_dwordx4 v1, s[20:23], m0 offen sc1 lds\n"
"v_mul_f32 v92, v92, v224\n"
"v_mfma_f32_16x16x32_bf16 v[118:121], v[70:73], a[80:83], v[118:121]\n"
"s_waitcnt lgkmcnt(4)\n"
"ds_read_b128 v[62:65], v3 offset:2048\n"
"v_rcp_f32 v225, v225\n"
"v_mfma_f32_16x16x32_bf16 v[122:125], v[70:73], a[84:87], v[122:125]\n"
"s_add_u32 m0, s59, 36864\n"
"v_mul_f32 v93, v93, v225\n"
"v_cvt_pk_bf16_f32 v97, v92, v93\n"
"v_permlane16_swap_b32 v94, v96\n"
"v_mfma_f32_16x16x32_bf16 v[126:129], v[74:77], a[80:83], v[126:129]\n"
"s_waitcnt lgkmcnt(4)\n"
"ds_read_b128 v[66:69], v3 offset:3072\n"
"v_mfma_f32_16x16x32_bf16 v[130:133], v[74:77], a[84:87], v[130:133]\n"
"buffer_load_dwordx4 v1, s[20:23], m0 offen sc1 lds\n"
"v_permlane16_swap_b32 v95, v97\n"
"v_mfma_f32_16x16x32_bf16 v[118:121], v[78:81], a[88:91], v[118:121]\n"
"s_waitcnt lgkmcnt(4)\n"
"ds_read_b128 v[70:73], v3 offset:4096\n"
"buffer_store_dwordx4 v[94:97], v39, s[96:99], 0 offen sc1\n"
"v_mfma_f32_16x16x32_bf16 v[122:125], v[78:81], a[92:95], v[122:125]\n"
"s_add_u32 m0, s59, 40960\n"
"v_mfma_f32_16x16x32_bf16 v[126:129], v[82:85], a[88:91], v[126:129]\n"
"s_waitcnt lgkmcnt(4)\n"
"ds_read_b128 v[74:77], v3 offset:5120\n"
"ds_read_b128 v[240:243], v50 offset:576\n"
"ds_read_b128 v[244:247], v50 offset:1088\n"
"v_mfma_f32_16x16x32_bf16 v[130:133], v[82:85], a[92:95], v[130:133]\n"
"buffer_load_dwordx4 v1, s[20:23], m0 offen sc1 lds\n"
"v_mfma_f32_16x16x32_bf16 v[118:121], v[54:57], a[96:99], v[118:121]\n"
"s_waitcnt lgkmcnt(6)\n"
"ds_read_b128 v[78:81], v3 offset:6144\n"
"v_mfma_f32_16x16x32_bf16 v[122:125], v[54:57], a[100:103], v[122:125]\n"
"s_add_u32 s62, s67, 118784\n"
"v_mfma_f32_16x16x32_bf16 v[126:129], v[58:61], a[96:99], v[126:129]\n"
"s_waitcnt lgkmcnt(6)\n"
"ds_read_b128 v[82:85], v3 offset:7168\n"
"v_mfma_f32_16x16x32_bf16 v[130:133], v[58:61], a[100:103], v[130:133]\n"
"buffer_load_dwordx4 a[216:219], v5, s[20:23], s62 offen sc1\n"
"v_mfma_f32_16x16x32_bf16 v[118:121], v[62:65], a[104:107], v[118:121]\n"
"s_waitcnt lgkmcnt(2)\n"
"ds_read_b128 v[54:57], v4 offset:4096\n"
"v_fmac_f32 v102, v226, v240\n"
"v_mul_f32 v218, v230, v102\n"
"v_fmac_f32 v103, v226, v241\n"
"v_mul_f32 v219, v230, v103\n"
"v_mfma_f32_16x16x32_bf16 v[122:125], v[62:65], a[108:111], v[122:125]\n"
"v_exp_f32 v218, v218\n"
"v_fmac_f32 v110, v226, v244\n"
"v_mfma_f32_16x16x32_bf16 v[126:129], v[66:69], a[104:107], v[126:129]\n"
"ds_read_b128 v[58:61], v4 offset:5120\n"
"v_fma_f32 v218, v218, v228, v228\n"
"v_mul_f32 v102, v102, v110\n"
"v_mfma_f32_16x16x32_bf16 v[130:133], v[66:69], a[108:111], v[130:133]\n"
"buffer_load_dwordx4 a[220:223], v5, s[20:23], s62 offen offset:2048 sc1\n"
"v_fmac_f32 v104, v226, v242\n"
"v_mul_f32 v220, v230, v104\n"
"v_mfma_f32_16x16x32_bf16 v[118:121], v[70:73], a[112:115], v[118:121]\n"
"ds_read_b128 v[62:65], v4 offset:6144\n"
"v_fmac_f32 v111, v226, v245\n"
"v_mul_f32 v103, v103, v111\n"
"v_mfma_f32_16x16x32_bf16 v[122:125], v[70:73], a[116:119], v[122:125]\n"
"s_add_u32 s62, s67, 122880\n"
"v_exp_f32 v219, v219\n"
"v_rcp_f32 v218, v218\n"
"v_mfma_f32_16x16x32_bf16 v[126:129], v[74:77], a[112:115], v[126:129]\n"
"s_waitcnt lgkmcnt(4)\n"
"ds_read_b128 v[66:69], v4 offset:7168\n"
"v_fma_f32 v219, v219, v228, v228\n"
"v_mul_f32 v102, v102, v218\n"
"v_mfma_f32_16x16x32_bf16 v[130:133], v[74:77], a[116:119], v[130:133]\n"
"v_exp_f32 v220, v220\n"
"v_fmac_f32 v105, v226, v243\n"
"v_mfma_f32_16x16x32_bf16 v[118:121], v[78:81], a[120:123], v[118:121]\n"
"s_waitcnt lgkmcnt(4)\n"
"ds_read_b128 v[70:73], v4 offset:8192\n"
"v_mul_f32 v221, v230, v105\n"
"v_rcp_f32 v219, v219\n"
"v_mfma_f32_16x16x32_bf16 v[122:125], v[78:81], a[124:127], v[122:125]\n"
"buffer_load_dwordx4 a[224:227], v5, s[20:23], s62 offen sc1\n"
"v_fmac_f32 v112, v226, v246\n"
"v_mfma_f32_16x16x32_bf16 v[126:129], v[82:85], a[120:123], v[126:129]\n"
"s_waitcnt lgkmcnt(4)\n"
"ds_read_b128 v[74:77], v4 offset:9216\n"
"v_fma_f32 v220, v220, v228, v228\n"
"v_mul_f32 v104, v104, v112\n"
"v_mul_f32 v103, v103, v219\n"
"v_mfma_f32_16x16x32_bf16 v[130:133], v[82:85], a[124:127], v[130:133]\n"
"v_exp_f32 v221, v221\n"
"v_cvt_pk_bf16_f32 v110, v102, v103\n"
"v_mfma_f32_16x16x32_bf16 v[118:121], v[54:57], a[128:131], v[118:121]\n"
"s_waitcnt lgkmcnt(4)\n"
"ds_read_b128 v[78:81], v4 offset:10240\n"
"v_fmac_f32 v106, v227, v240\n"
"v_mul_f32 v222, v231, v106\n"
"v_mfma_f32_16x16x32_bf16 v[122:125], v[54:57], a[132:135], v[122:125]\n"
"buffer_load_dwordx4 a[228:231], v5, s[20:23], s62 offen offset:2048 sc1\n"
"v_rcp_f32 v220, v220\n"
"v_mfma_f32_16x16x32_bf16 v[126:129], v[58:61], a[128:131], v[126:129]\n"
"ds_read_b128 v[82:85], v4 offset:11264\n"
"v_fmac_f32 v113, v226, v247\n"
"v_fma_f32 v221, v221, v228, v228\n"
"v_mfma_f32_16x16x32_bf16 v[130:133], v[58:61], a[132:135], v[130:133]\n"
"s_waitcnt vmcnt(21)\n"
"s_barrier\n"
"v_mul_f32 v105, v105, v113\n"
"s_waitcnt lgkmcnt(5)\n"
"v_mfma_f32_16x16x32_bf16 v[118:121], v[62:65], a[136:139], v[118:121]\n"
"s_waitcnt lgkmcnt(4)\n"
"ds_read_b128 v[54:57], v4 offset:12288\n"
"v_mul_f32 v104, v104, v220\n"
"v_fmac_f32 v107, v227, v241\n"
"v_mul_f32 v223, v231, v107\n"
"v_mfma_f32_16x16x32_bf16 v[122:125], v[62:65], a[140:143], v[122:125]\n"
"s_add_u32 m0, s59, 45056\n"
"v_exp_f32 v222, v222\n"
"v_fmac_f32 v114, v227, v244\n"
"v_mfma_f32_16x16x32_bf16 v[126:129], v[66:69], a[136:139], v[126:129]\n"
"s_waitcnt lgkmcnt(4)\n"
"ds_read_b128 v[58:61], v4 offset:13312\n"
"v_fma_f32 v222, v222, v229, v229\n"
"v_rcp_f32 v221, v221\n"
"v_mfma_f32_16x16x32_bf16 v[130:133], v[66:69], a[140:143], v[130:133]\n"
"buffer_load_dwordx4 v1, s[20:23], m0 offen sc1 lds\n"
"v_mul_f32 v106, v106, v114\n"
"v_mfma_f32_16x16x32_bf16 v[118:121], v[70:73], a[144:147], v[118:121]\n"
"s_waitcnt lgkmcnt(4)\n"
"ds_read_b128 v[62:65], v4 offset:14336\n"
"v_mul_f32 v105, v105, v221\n"
"v_cvt_pk_bf16_f32 v111, v104, v105\n"
"v_mfma_f32_16x16x32_bf16 v[122:125], v[70:73], a[148:151], v[122:125]\n"
"s_add_u32 m0, s59, 49152\n"
"v_exp_f32 v223, v223\n"
"v_fmac_f32 v108, v227, v242\n"
"v_mul_f32 v224, v231, v108\n"
"v_mfma_f32_16x16x32_bf16 v[126:129], v[74:77], a[144:147], v[126:129]\n"
"s_waitcnt lgkmcnt(4)\n"
"ds_read_b128 v[66:69], v4 offset:15360\n"
"v_fmac_f32 v115, v227, v245\n"
"v_fma_f32 v223, v223, v229, v229\n"
"v_mfma_f32_16x16x32_bf16 v[130:133], v[74:77], a[148:151], v[130:133]\n"
"buffer_load_dwordx4 v1, s[20:23], m0 offen sc1 lds\n"
"v_mul_f32 v107, v107, v115\n"
"v_mfma_f32_16x16x32_bf16 v[118:121], v[78:81], a[152:155], v[118:121]\n"
"s_waitcnt lgkmcnt(4)\n"
"ds_read_b128 v[70:73], v4 offset:16384\n"
"v_fmac_f32 v109, v227, v243\n"
"v_rcp_f32 v222, v222\n"
"v_mfma_f32_16x16x32_bf16 v[122:125], v[78:81], a[156:159], v[122:125]\n"
"s_add_u32 m0, s59, 53248\n"
"v_exp_f32 v224, v224\n"
"v_mul_f32 v106, v106, v222\n"
"v_mfma_f32_16x16x32_bf16 v[126:129], v[82:85], a[152:155], v[126:129]\n"
"s_waitcnt lgkmcnt(4)\n"
"ds_read_b128 v[74:77], v4 offset:17408\n"
"v_mul_f32 v225, v231, v109\n"
"v_fmac_f32 v116, v227, v246\n"
"v_mfma_f32_16x16x32_bf16 v[130:133], v[82:85], a[156:159], v[130:133]\n"
"buffer_load_dwordx4 v1, s[20:23], m0 offen sc1 lds\n"
"v_rcp_f32 v223, v223\n"
"v_mfma_f32_16x16x32_bf16 v[134:137], v[54:57], a[0:3], v[134:137]\n"
"s_waitcnt lgkmcnt(4)\n"
"ds_read_b128 v[78:81], v4 offset:18432\n"
"v_fma_f32 v224, v224, v229, v229\n"
"v_mul_f32 v108, v108, v116\n"
"v_mfma_f32_16x16x32_bf16 v[138:141], v[54:57], a[4:7], v[138:141]\n"
"s_add_u32 m0, s59, 57344\n"
"v_exp_f32 v225, v225\n"
"v_mul_f32 v107, v107, v223\n"
"v_mfma_f32_16x16x32_bf16 v[142:145], v[58:61], a[0:3], v[142:145]\n"
"s_waitcnt lgkmcnt(4)\n"
"ds_read_b128 v[82:85], v4 offset:19456\n"
"v_cvt_pk_bf16_f32 v112, v106, v107\n"
"v_fmac_f32 v117, v227, v247\n"
"v_mfma_f32_16x16x32_bf16 v[146:149], v[58:61], a[4:7], v[146:149]\n"
"v_rcp_f32 v224, v224\n"
"v_fma_f32 v225, v225, v229, v229\n"
"v_mul_f32 v109, v109, v117\n"
"v_mfma_f32_16x16x32_bf16 v[134:137], v[62:65], a[8:11], v[134:137]\n"
"s_waitcnt lgkmcnt(4)\n"
"ds_read_b128 v[54:57], v4 offset:20480\n"
"v_mul_f32 v108, v108, v224\n"
"v_mul_f32 v218, v230, v118\n"
"v_mfma_f32_16x16x32_bf16 v[138:141], v[62:65], a[12:15], v[138:141]\n"
"buffer_load_dwordx4 v1, s[20:23], m0 offen sc1 lds\n"
"v_mul_f32 v219, v230, v119\n"
"v_mfma_f32_16x16x32_bf16 v[142:145], v[66:69], a[8:11], v[142:145]\n"
"s_waitcnt lgkmcnt(4)\n"
"ds_read_b128 v[58:61], v4 offset:21504\n"
"v_mul_f32 v118, v118, v126\n"
"v_rcp_f32 v225, v225\n"
"v_mfma_f32_16x16x32_bf16 v[146:149], v[66:69], a[12:15], v[146:149]\n"
"s_add_u32 m0, s59, 61440\n"
"v_exp_f32 v218, v218\n"
"v_mul_f32 v109, v109, v225\n"
"v_cvt_pk_bf16_f32 v113, v108, v109\n"
"v_mfma_f32_16x16x32_bf16 v[134:137], v[70:73], a[16:19], v[134:137]\n"
"s_waitcnt lgkmcnt(4)\n"
"ds_read_b128 v[62:65], v4 offset:22528\n"
"v_fma_f32 v218, v218, v228, v228\n"
"v_permlane16_swap_b32 v110, v112\n"
"v_mfma_f32_16x16x32_bf16 v[138:141], v[70:73], a[20:23], v[138:141]\n"
"buffer_load_dwordx4 v1, s[20:23], m0 offen sc1 lds\n"
"v_permlane16_swap_b32 v111, v113\n"
"v_mul_f32 v220, v230, v120\n"
"v_mfma_f32_16x16x32_bf16 v[142:145], v[74:77], a[16:19], v[142:145]\n"
"s_waitcnt lgkmcnt(4)\n"
"ds_read_b128 v[66:69], v4 offset:23552\n"
"v_mul_f32 v119, v119, v127\n"
"v_exp_f32 v219, v219\n"
"v_mul_f32 v221, v230, v121\n"
"buffer_store_dwordx4 v[110:113], v39, s[96:99], 0 offen offset:512 sc1\n"
"v_mfma_f32_16x16x32_bf16 v[146:149], v[74:77], a[20:23], v[146:149]\n"
"s_add_u32 m0, s59, 65536\n"
"v_rcp_f32 v218, v218\n"
"v_mfma_f32_16x16x32_bf16 v[134:137], v[78:81], a[24:27], v[134:137]\n"
"s_waitcnt lgkmcnt(4)\n"
"ds_read_b128 v[70:73], v3 offset:8192\n"
"v_fma_f32 v219, v219, v228, v228\n"
"v_mfma_f32_16x16x32_bf16 v[138:141], v[78:81], a[28:31], v[138:141]\n"
"v_exp_f32 v220, v220\n"
"v_mfma_f32_16x16x32_bf16 v[142:145], v[82:85], a[24:27], v[142:145]\n"
"s_waitcnt lgkmcnt(4)\n"
"ds_read_b128 v[74:77], v3 offset:9216\n"
"v_mul_f32 v118, v118, v218\n"
"v_mfma_f32_16x16x32_bf16 v[146:149], v[82:85], a[28:31], v[146:149]\n"
"buffer_load_dwordx4 v1, s[20:23], m0 offen sc1 lds\n"
"v_mfma_f32_16x16x32_bf16 v[134:137], v[54:57], a[32:35], v[134:137]\n"
"s_waitcnt lgkmcnt(4)\n"
"ds_read_b128 v[78:81], v3 offset:10240\n"
"v_fma_f32 v220, v220, v228, v228\n"
"v_mfma_f32_16x16x32_bf16 v[138:141], v[54:57], a[36:39], v[138:141]\n"
"s_add_u32 m0, s59, 69632\n"
"v_rcp_f32 v219, v219\n"
"v_mfma_f32_16x16x32_bf16 v[142:145], v[58:61], a[32:35], v[142:145]\n"
"s_waitcnt lgkmcnt(4)\n"
"ds_read_b128 v[82:85], v3 offset:11264\n"
"v_mul_f32 v120, v120, v128\n"
"v_mfma_f32_16x16x32_bf16 v[146:149], v[58:61], a[36:39], v[146:149]\n"
"buffer_load_dwordx4 v1, s[20:23], m0 offen sc1 lds\n"
"v_mfma_f32_16x16x32_bf16 v[134:137], v[62:65], a[40:43], v[134:137]\n"
"s_waitcnt lgkmcnt(4)\n"
"ds_read_b128 v[54:57], v3 offset:12288\n"
"v_mul_f32 v119, v119, v219\n"
"v_mfma_f32_16x16x32_bf16 v[138:141], v[62:65], a[44:47], v[138:141]\n"
"s_add_u32 m0, s59, 0\n"
"v_add_u32 v1, 73728, v1\n"
"v_cvt_pk_bf16_f32 v126, v118, v119\n"
"v_mfma_f32_16x16x32_bf16 v[142:145], v[66:69], a[40:43], v[142:145]\n"
"s_waitcnt lgkmcnt(4)\n"
"ds_read_b128 v[58:61], v3 offset:13312\n"
"v_mul_f32 v222, v231, v122\n"
"v_mul_f32 v121, v121, v129\n"
"v_mfma_f32_16x16x32_bf16 v[146:149], v[66:69], a[44:47], v[146:149]\n"
"v_exp_f32 v221, v221\n"
"v_mfma_f32_16x16x32_bf16 v[134:137], v[70:73], a[48:51], v[134:137]\n"
"s_waitcnt lgkmcnt(4)\n"
"ds_read_b128 v[62:65], v3 offset:14336\n"
"v_fma_f32 v221, v221, v228, v228\n"
"v_mfma_f32_16x16x32_bf16 v[138:141], v[70:73], a[52:55], v[138:141]\n"
"buffer_load_dwordx4 v1, s[20:23], m0 offen sc1 lds\n"
"v_mfma_f32_16x16x32_bf16 v[142:145], v[74:77], a[48:51], v[142:145]\n"
"s_waitcnt lgkmcnt(4)\n"
"ds_read_b128 v[66:69], v3 offset:15360\n"
"v_mul_f32 v223, v231, v123\n"
"v_mfma_f32_16x16x32_bf16 v[146:149], v[74:77], a[52:55], v[146:149]\n"
"s_add_u32 m0, s59, 4096\n"
"v_rcp_f32 v220, v220\n"
"v_mfma_f32_16x16x32_bf16 v[134:137], v[78:81], a[56:59], v[134:137]\n"
"s_waitcnt lgkmcnt(4)\n"
"ds_read_b128 v[70:73], v3 offset:16384\n"
"v_mul_f32 v120, v120, v220\n"
"v_mfma_f32_16x16x32_bf16 v[138:141], v[78:81], a[60:63], v[138:141]\n"
"buffer_load_dwordx4 v1, s[20:23], m0 offen sc1 lds\n"
"v_mfma_f32_16x16x32_bf16 v[142:145], v[82:85], a[56:59], v[142:145]\n"
"s_waitcnt lgkmcnt(4)\n"
"ds_read_b128 v[74:77], v3 offset:17408\n"
"v_mul_f32 v122, v122, v130\n"
"v_mfma_f32_16x16x32_bf16 v[146:149], v[82:85], a[60:63], v[146:149]\n"
"s_add_u32 m0, s59, 8192\n"
"v_exp_f32 v222, v222\n"
"v_mfma_f32_16x16x32_bf16 v[134:137], v[54:57], a[64:67], v[134:137]\n"
"s_waitcnt lgkmcnt(4)\n"
"ds_read_b128 v[78:81], v3 offset:18432\n"
"v_fma_f32 v222, v222, v229, v229\n"
"v_mfma_f32_16x16x32_bf16 v[138:141], v[54:57], a[68:71], v[138:141]\n"
"v_rcp_f32 v221, v221\n"
"v_mul_f32 v224, v231, v124\n"
"v_mfma_f32_16x16x32_bf16 v[142:145], v[58:61], a[64:67], v[142:145]\n"
"s_waitcnt lgkmcnt(4)\n"
"ds_read_b128 v[82:85], v3 offset:19456\n"
"v_mul_f32 v121, v121, v221\n"
"v_mfma_f32_16x16x32_bf16 v[146:149], v[58:61], a[68:71], v[146:149]\n"
"buffer_load_dwordx4 v1, s[20:23], m0 offen sc1 lds\n"
"v_mfma_f32_16x16x32_bf16 v[134:137], v[62:65], a[72:75], v[134:137]\n"
"s_waitcnt lgkmcnt(4)\n"
"ds_read_b128 v[54:57], v3 offset:20480\n"
"v_cvt_pk_bf16_f32 v127, v120, v121\n"
"v_mfma_f32_16x16x32_bf16 v[138:141], v[62:65], a[76:79], v[138:141]\n"
"s_add_u32 s62, s67, 126976\n"
"v_exp_f32 v223, v223\n"
"v_mfma_f32_16x16x32_bf16 v[142:145], v[66:69], a[72:75], v[142:145]\n"
"s_waitcnt lgkmcnt(4)\n"
"ds_read_b128 v[58:61], v3 offset:21504\n"
"v_fma_f32 v223, v223, v229, v229\n"
"v_mfma_f32_16x16x32_bf16 v[146:149], v[66:69], a[76:79], v[146:149]\n"
"buffer_load_dwordx4 a[232:235], v5, s[20:23], s62 offen sc1\n"
"v_mfma_f32_16x16x32_bf16 v[134:137], v[70:73], a[80:83], v[134:137]\n"
"s_waitcnt lgkmcnt(4)\n"
"ds_read_b128 v[62:65], v3 offset:22528\n"
"v_mul_f32 v123, v123, v131\n"
"v_mfma_f32_16x16x32_bf16 v[138:141], v[70:73], a[84:87], v[138:141]\n"
"v_rcp_f32 v222, v222\n"
"v_mfma_f32_16x16x32_bf16 v[142:145], v[74:77], a[80:83], v[142:145]\n"
"s_waitcnt lgkmcnt(4)\n"
"ds_read_b128 v[66:69], v3 offset:23552\n"
"v_mul_f32 v122, v122, v222\n"
"v_mfma_f32_16x16x32_bf16 v[146:149], v[74:77], a[84:87], v[146:149]\n"
"v_exp_f32 v224, v224\n"
"v_mfma_f32_16x16x32_bf16 v[134:137], v[78:81], a[88:91], v[134:137]\n"
"s_waitcnt lgkmcnt(4)\n"
"ds_read_b128 v[70:73], v3 offset:24576\n"
"v_mul_f32 v225, v231, v125\n"
"v_mfma_f32_16x16x32_bf16 v[138:141], v[78:81], a[92:95], v[138:141]\n"
"buffer_load_dwordx4 a[236:239], v5, s[20:23], s62 offen offset:2048 sc1\n"
"v_fma_f32 v224, v224, v229, v229\n"
"v_mfma_f32_16x16x32_bf16 v[142:145], v[82:85], a[88:91], v[142:145]\n"
"s_waitcnt lgkmcnt(4)\n"
"ds_read_b128 v[74:77], v3 offset:25600\n"
"v_mul_f32 v124, v124, v132\n"
"v_mfma_f32_16x16x32_bf16 v[146:149], v[82:85], a[92:95], v[146:149]\n"
"s_add_u32 s62, s67, 131072\n"
"v_rcp_f32 v223, v223\n"
"v_mfma_f32_16x16x32_bf16 v[134:137], v[54:57], a[96:99], v[134:137]\n"
"ds_read_b128 v[78:81], v3 offset:26624\n"
"s_mov_b32 s3, 1572864\n"
"s_waitcnt vmcnt(7)\n"
"v_mfma_f32_16x16x32_bf16 v[138:141], v[54:57], a[100:103], v[138:141]\n"
"s_waitcnt lgkmcnt(5)\n"
"buffer_load_dwordx4 a[240:243], v5, s[20:23], s62 offen sc1\n"
"v_mfma_f32_16x16x32_bf16 v[142:145], v[58:61], a[96:99], v[142:145]\n"
"s_waitcnt lgkmcnt(4)\n"
"ds_read_b128 v[82:85], v3 offset:27648\n"
"v_mul_f32 v123, v123, v223\n"
"v_mfma_f32_16x16x32_bf16 v[146:149], v[58:61], a[100:103], v[146:149]\n"
"buffer_store_dwordx2 v[8:9], v14, s[20:23], s3 offen sc1\n"
"v_mfma_f32_16x16x32_bf16 v[134:137], v[62:65], a[104:107], v[134:137]\n"
"s_waitcnt lgkmcnt(4)\n"
"ds_read_b128 v[54:57], v3 offset:28672\n"
"v_cvt_pk_bf16_f32 v128, v122, v123\n"
"v_mfma_f32_16x16x32_bf16 v[138:141], v[62:65], a[108:111], v[138:141]\n"
"v_exp_f32 v225, v225\n"
"v_mfma_f32_16x16x32_bf16 v[142:145], v[66:69], a[104:107], v[142:145]\n"
"s_waitcnt lgkmcnt(4)\n"
"ds_read_b128 v[58:61], v3 offset:29696\n"
"v_fma_f32 v225, v225, v229, v229\n"
"v_mfma_f32_16x16x32_bf16 v[146:149], v[66:69], a[108:111], v[146:149]\n"
"buffer_load_dwordx4 a[244:247], v5, s[20:23], s62 offen offset:2048 sc1\n"
"v_mfma_f32_16x16x32_bf16 v[134:137], v[70:73], a[112:115], v[134:137]\n"
"s_waitcnt lgkmcnt(4)\n"
"ds_read_b128 v[62:65], v3 offset:30720\n"
"v_mul_f32 v125, v125, v133\n"
"v_mfma_f32_16x16x32_bf16 v[138:141], v[70:73], a[116:119], v[138:141]\n"
"s_add_u32 s62, s67, 135168\n"
"v_rcp_f32 v224, v224\n"
"v_mfma_f32_16x16x32_bf16 v[142:145], v[74:77], a[112:115], v[142:145]\n"
"s_waitcnt lgkmcnt(4)\n"
"ds_read_b128 v[66:69], v3 offset:31744\n"
"v_mul_f32 v124, v124, v224\n"
"v_mfma_f32_16x16x32_bf16 v[146:149], v[74:77], a[116:119], v[146:149]\n"
"buffer_load_dwordx4 a[248:251], v5, s[20:23], s62 offen sc1\n"
"v_mfma_f32_16x16x32_bf16 v[134:137], v[78:81], a[120:123], v[134:137]\n"
"s_waitcnt lgkmcnt(4)\n"
"ds_read_b128 v[70:73], v3 offset:32768\n"
"v_rcp_f32 v225, v225\n"
"v_mfma_f32_16x16x32_bf16 v[138:141], v[78:81], a[124:127], v[138:141]\n"
"v_permlane16_swap_b32 v126, v128\n"
"v_mfma_f32_16x16x32_bf16 v[142:145], v[82:85], a[120:123], v[142:145]\n"
"s_waitcnt lgkmcnt(4)\n"
"ds_read_b128 v[74:77], v3 offset:33792\n"
"v_mul_f32 v125, v125, v225\n"
"v_mfma_f32_16x16x32_bf16 v[146:149], v[82:85], a[124:127], v[146:149]\n"
"v_cvt_pk_bf16_f32 v129, v124, v125\n"
"v_mfma_f32_16x16x32_bf16 v[134:137], v[54:57], a[128:131], v[134:137]\n"
"s_waitcnt lgkmcnt(4)\n"
"ds_read_b128 v[78:81], v3 offset:34816\n"
"v_mfma_f32_16x16x32_bf16 v[138:141], v[54:57], a[132:135], v[138:141]\n"
"buffer_load_dwordx4 a[252:255], v5, s[20:23], s62 offen offset:2048 sc1\n"
"v_mfma_f32_16x16x32_bf16 v[142:145], v[58:61], a[128:131], v[142:145]\n"
"ds_read_b128 v[82:85], v3 offset:35840\n"
"v_mfma_f32_16x16x32_bf16 v[146:149], v[58:61], a[132:135], v[146:149]\n"
"s_barrier\n"
"s_waitcnt lgkmcnt(5)\n"
"v_mfma_f32_16x16x32_bf16 v[134:137], v[62:65], a[136:139], v[134:137]\n"
"s_waitcnt lgkmcnt(4)\n"
"ds_read_b128 v[54:57], v3 offset:36864\n"
"v_mfma_f32_16x16x32_bf16 v[138:141], v[62:65], a[140:143], v[138:141]\n"
"s_add_u32 m0, s59, 135168\n"
"v_add_u32 v1, 4294844416, v1\n"
"v_permlane16_swap_b32 v127, v129\n"
"v_mfma_f32_16x16x32_bf16 v[142:145], v[66:69], a[136:139], v[142:145]\n"
"s_waitcnt lgkmcnt(4)\n"
"ds_read_b128 v[58:61], v3 offset:37888\n"
"v_mfma_f32_16x16x32_bf16 v[146:149], v[66:69], a[140:143], v[146:149]\n"
"buffer_load_dwordx4 v1, s[20:23], m0 offen sc1 lds\n"
"v_mfma_f32_16x16x32_bf16 v[134:137], v[70:73], a[144:147], v[134:137]\n"
"s_waitcnt lgkmcnt(4)\n"
"ds_read_b128 v[62:65], v3 offset:38912\n"
"v_mfma_f32_16x16x32_bf16 v[138:141], v[70:73], a[148:151], v[138:141]\n"
"s_add_u32 m0, s59, 139264\n"
"v_mfma_f32_16x16x32_bf16 v[142:145], v[74:77], a[144:147], v[142:145]\n"
"s_waitcnt lgkmcnt(4)\n"
"ds_read_b128 v[66:69], v3 offset:39936\n"
"v_mfma_f32_16x16x32_bf16 v[146:149], v[74:77], a[148:151], v[146:149]\n"
"buffer_load_dwordx4 v1, s[20:23], m0 offen sc1 lds\n"
"v_mfma_f32_16x16x32_bf16 v[134:137], v[78:81], a[152:155], v[134:137]\n"
"s_waitcnt lgkmcnt(4)\n"
"ds_read_b128 v[70:73], v3 offset:40960\n"
"v_mfma_f32_16x16x32_bf16 v[138:141], v[78:81], a[156:159], v[138:141]\n"
"s_add_u32 m0, s59, 143360\n"
"v_mfma_f32_16x16x32_bf16 v[142:145], v[82:85], a[152:155], v[142:145]\n"
"s_waitcnt lgkmcnt(4)\n"
"ds_read_b128 v[74:77], v3 offset:41984\n"
"buffer_store_dwordx4 v[126:129], v39, s[96:99], s83 offen sc1\n"
"v_mfma_f32_16x16x32_bf16 v[146:149], v[82:85], a[156:159], v[146:149]\n"
"buffer_load_dwordx4 v1, s[20:23], m0 offen sc1 lds\n"
"v_mfma_f32_16x16x32_bf16 v[150:153], v[54:57], a[0:3], v[150:153]\n"
"s_waitcnt lgkmcnt(4)\n"
"ds_read_b128 v[78:81], v3 offset:43008\n"
"v_mfma_f32_16x16x32_bf16 v[154:157], v[54:57], a[4:7], v[154:157]\n"
"s_add_u32 m0, s59, 147456\n"
"v_mfma_f32_16x16x32_bf16 v[158:161], v[58:61], a[0:3], v[158:161]\n"
"s_waitcnt lgkmcnt(4)\n"
"ds_read_b128 v[82:85], v3 offset:44032\n"
"v_mfma_f32_16x16x32_bf16 v[162:165], v[58:61], a[4:7], v[162:165]\n"
"buffer_load_dwordx4 v1, s[20:23], m0 offen sc1 lds\n"
"v_mfma_f32_16x16x32_bf16 v[150:153], v[62:65], a[8:11], v[150:153]\n"
"s_waitcnt lgkmcnt(4)\n"
"ds_read_b128 v[54:57], v3 offset:45056\n"
"v_mul_f32 v218, v230, v134\n"
"v_mul_f32 v219, v230, v135\n"
"v_mfma_f32_16x16x32_bf16 v[154:157], v[62:65], a[12:15], v[154:157]\n"
"s_add_u32 m0, s59, 151552\n"
"s_add_u32 s60, s67, 4294815744\n"
"v_add_u32 v1, s60, v2\n"
"v_mul_f32 v134, v134, v142\n"
"v_mfma_f32_16x16x32_bf16 v[158:161], v[66:69], a[8:11], v[158:161]\n"
"s_waitcnt lgkmcnt(4)\n"
"ds_read_b128 v[58:61], v3 offset:46080\n"
"v_mul_f32 v220, v230, v136\n"
"v_mfma_f32_16x16x32_bf16 v[162:165], v[66:69], a[12:15], v[162:165]\n"
"v_exp_f32 v218, v218\n"
"v_mfma_f32_16x16x32_bf16 v[150:153], v[70:73], a[16:19], v[150:153]\n"
"s_waitcnt lgkmcnt(4)\n"
"ds_read_b128 v[62:65], v3 offset:47104\n"
"v_fma_f32 v218, v218, v228, v228\n"
"v_mfma_f32_16x16x32_bf16 v[154:157], v[70:73], a[20:23], v[154:157]\n"
"buffer_load_dwordx4 v1, s[20:23], m0 offen sc1 lds\n"
"v_mfma_f32_16x16x32_bf16 v[158:161], v[74:77], a[16:19], v[158:161]\n"
"s_waitcnt lgkmcnt(4)\n"
"ds_read_b128 v[66:69], v3 offset:48128\n"
"v_mul_f32 v135, v135, v143\n"
"v_mul_f32 v221, v230, v137\n"
"v_mfma_f32_16x16x32_bf16 v[162:165], v[74:77], a[20:23], v[162:165]\n"
"s_add_u32 m0, s59, 73728\n"
"v_add_u32 v1, 81920, v1\n"
"v_mul_f32 v136, v136, v144\n"
"v_mfma_f32_16x16x32_bf16 v[150:153], v[78:81], a[24:27], v[150:153]\n"
"s_waitcnt lgkmcnt(4)\n"
"ds_read_b128 v[70:73], v3 offset:49152\n"
"v_mul_f32 v222, v231, v138\n"
"v_mfma_f32_16x16x32_bf16 v[154:157], v[78:81], a[28:31], v[154:157]\n"
"buffer_load_dwordx4 v1, s[20:23], m0 offen sc1 lds\n"
"v_mfma_f32_16x16x32_bf16 v[158:161], v[82:85], a[24:27], v[158:161]\n"
"s_waitcnt lgkmcnt(4)\n"
"ds_read_b128 v[74:77], v3 offset:50176\n"
"v_mul_f32 v137, v137, v145\n"
"v_mfma_f32_16x16x32_bf16 v[162:165], v[82:85], a[28:31], v[162:165]\n"
"s_add_u32 m0, s59, 77824\n"
"v_exp_f32 v219, v219\n"
"v_mul_f32 v223, v231, v139\n"
"v_mfma_f32_16x16x32_bf16 v[150:153], v[54:57], a[32:35], v[150:153]\n"
"s_waitcnt lgkmcnt(4)\n"
"ds_read_b128 v[78:81], v3 offset:51200\n"
"v_fma_f32 v219, v219, v228, v228\n"
"v_mfma_f32_16x16x32_bf16 v[154:157], v[54:57], a[36:39], v[154:157]\n"
"v_rcp_f32 v218, v218\n"
"v_mfma_f32_16x16x32_bf16 v[158:161], v[58:61], a[32:35], v[158:161]\n"
"s_waitcnt lgkmcnt(4)\n"
"ds_read_b128 v[82:85], v3 offset:52224\n"
"buffer_load_dwordx4 v1, s[20:23], m0 offen sc1 lds\n"
"v_mfma_f32_16x16x32_bf16 v[162:165], v[58:61], a[36:39], v[162:165]\n"
"s_mov_b32 s63, 1572864\n"
"v_exp_f32 v220, v220\n"
"v_mfma_f32_16x16x32_bf16 v[150:153], v[62:65], a[40:43], v[150:153]\n"
"s_waitcnt lgkmcnt(4)\n"
"ds_read_b128 v[54:57], v3 offset:53248\n"
"v_mul_f32 v134, v134, v218\n"
"v_mfma_f32_16x16x32_bf16 v[154:157], v[62:65], a[44:47], v[154:157]\n"
"buffer_load_dwordx2 v[254:255], v11, s[20:23], s63 offen sc1\n"
"v_fma_f32 v220, v220, v228, v228\n"
"v_mfma_f32_16x16x32_bf16 v[158:161], v[66:69], a[40:43], v[158:161]\n"
"s_waitcnt lgkmcnt(4)\n"
"ds_read_b128 v[58:61], v3 offset:54272\n"
"v_mul_f32 v138, v138, v146\n"
"v_mfma_f32_16x16x32_bf16 v[162:165], v[66:69], a[44:47], v[162:165]\n"
"s_add_u32 m0, s59, 81920\n"
"v_rcp_f32 v219, v219\n"
"v_mfma_f32_16x16x32_bf16 v[150:153], v[70:73], a[48:51], v[150:153]\n"
"s_waitcnt lgkmcnt(4)\n"
"ds_read_b128 v[62:65], v3 offset:55296\n"
"v_mul_f32 v135, v135, v219\n"
"v_mfma_f32_16x16x32_bf16 v[154:157], v[70:73], a[52:55], v[154:157]\n"
"buffer_load_dwordx4 v1, s[20:23], m0 offen sc1 lds\n"
"v_mfma_f32_16x16x32_bf16 v[158:161], v[74:77], a[48:51], v[158:161]\n"
"s_waitcnt lgkmcnt(4)\n"
"ds_read_b128 v[66:69], v3 offset:56320\n"
"v_cvt_pk_bf16_f32 v142, v134, v135\n"
"v_mfma_f32_16x16x32_bf16 v[162:165], v[74:77], a[52:55], v[162:165]\n"
"s_add_u32 m0, s59, 86016\n"
"v_exp_f32 v221, v221\n"
"v_mul_f32 v224, v231, v140\n"
"v_mfma_f32_16x16x32_bf16 v[150:153], v[78:81], a[56:59], v[150:153]\n"
"s_waitcnt lgkmcnt(4)\n"
"ds_read_b128 v[70:73], v3 offset:57344\n"
"v_fma_f32 v221, v221, v228, v228\n"
"v_mfma_f32_16x16x32_bf16 v[154:157], v[78:81], a[60:63], v[154:157]\n"
"buffer_load_dwordx4 v1, s[20:23], m0 offen sc1 lds\n"
"v_mfma_f32_16x16x32_bf16 v[158:161], v[82:85], a[56:59], v[158:161]\n"
"s_waitcnt lgkmcnt(4)\n"
"ds_read_b128 v[74:77], v3 offset:58368\n"
"v_mul_f32 v139, v139, v147\n"
"v_mfma_f32_16x16x32_bf16 v[162:165], v[82:85], a[60:63], v[162:165]\n"
"s_add_u32 m0, s59, 90112\n"
"v_rcp_f32 v220, v220\n"
"v_mfma_f32_16x16x32_bf16 v[150:153], v[54:57], a[64:67], v[150:153]\n"
"s_waitcnt lgkmcnt(4)\n"
"ds_read_b128 v[78:81], v3 offset:59392\n"
"v_mul_f32 v136, v136, v220\n"
"v_mfma_f32_16x16x32_bf16 v[154:157], v[54:57], a[68:71], v[154:157]\n"
"buffer_load_dwordx4 v1, s[20:23], m0 offen sc1 lds\n"
"v_mul_f32 v225, v231, v141\n"
"v_mfma_f32_16x16x32_bf16 v[158:161], v[58:61], a[64:67], v[158:161]\n"
"ds_read_b128 v[82:85], v3 offset:60416\n"
"v_mul_f32 v140, v140, v148\n"
"v_mfma_f32_16x16x32_bf16 v[162:165], v[58:61], a[68:71], v[162:165]\n"
"s_barrier\n"
"s_waitcnt lgkmcnt(5)\n"
"v_mfma_f32_16x16x32_bf16 v[150:153], v[62:65], a[72:75], v[150:153]\n"
"s_waitcnt lgkmcnt(4)\n"
"ds_read_b128 v[54:57], v3 offset:61440\n"
"v_mul_f32 v141, v141, v149\n"
"v_mfma_f32_16x16x32_bf16 v[154:157], v[62:65], a[76:79], v[154:157]\n"
"s_add_u32 m0, s59, 94208\n"
"v_exp_f32 v222, v222\n"
"v_mfma_f32_16x16x32_bf16 v[158:161], v[66:69], a[72:75], v[158:161]\n"
"s_waitcnt lgkmcnt(4)\n"
"ds_read_b128 v[58:61], v3 offset:62464\n"
"v_fma_f32 v222, v222, v229, v229\n"
"v_mfma_f32_16x16x32_bf16 v[162:165], v[66:69], a[76:79], v[162:165]\n"
"buffer_load_dwordx4 v1, s[20:23], m0 offen sc1 lds\n"
"v_mfma_f32_16x16x32_bf16 v[150:153], v[70:73], a[80:83], v[150:153]\n"
"s_waitcnt lgkmcnt(4)\n"
"ds_read_b128 v[62:65], v3 offset:63488\n"
"v_rcp_f32 v221, v221\n"
"v_mfma_f32_16x16x32_bf16 v[154:157], v[70:73], a[84:87], v[154:157]\n"
"s_add_u32 m0, s59, 98304\n"
"v_exp_f32 v223, v223\n"
"v_mfma_f32_16x16x32_bf16 v[158:161], v[74:77], a[80:83], v[158:161]\n"
"s_waitcnt lgkmcnt(4)\n"
"ds_read_b128 v[66:69], v3 offset:64512\n"
"v_mul_f32 v137, v137, v221\n"
"v_mfma_f32_16x16x32_bf16 v[162:165], v[74:77], a[84:87], v[162:165]\n"
"buffer_load_dwordx4 v1, s[20:23], m0 offen sc1 lds\n"
"v_mfma_f32_16x16x32_bf16 v[150:153], v[78:81], a[88:91], v[150:153]\n"
"s_waitcnt lgkmcnt(4)\n"
"ds_read_b128 v[70:73], v4\n"
"v_cvt_pk_bf16_f32 v143, v136, v137\n"
"v_mfma_f32_16x16x32_bf16 v[154:157], v[78:81], a[92:95], v[154:157]\n"
"s_add_u32 m0, s59, 102400\n"
"v_rcp_f32 v222, v222\n"
"v_fma_f32 v223, v223, v229, v229\n"
"v_mfma_f32_16x16x32_bf16 v[158:161], v[82:85], a[88:91], v[158:161]\n"
"s_waitcnt lgkmcnt(4)\n"
"ds_read_b128 v[74:77], v4 offset:1024\n"
"v_mul_f32 v138, v138, v222\n"
"v_mfma_f32_16x16x32_bf16 v[162:165], v[82:85], a[92:95], v[162:165]\n"
"buffer_load_dwordx4 v1, s[20:23], m0 offen sc1 lds\n"
"v_mfma_f32_16x16x32_bf16 v[150:153], v[54:57], a[96:99], v[150:153]\n"
"s_waitcnt lgkmcnt(4)\n"
"ds_read_b128 v[78:81], v4 offset:2048\n"
"v_mfma_f32_16x16x32_bf16 v[154:157], v[54:57], a[100:103], v[154:157]\n"
"s_add_u32 m0, s59, 106496\n"
"v_exp_f32 v224, v224\n"
"v_mfma_f32_16x16x32_bf16 v[158:161], v[58:61], a[96:99], v[158:161]\n"
"s_waitcnt lgkmcnt(4)\n"
"ds_read_b128 v[82:85], v4 offset:3072\n"
"v_fma_f32 v224, v224, v229, v229\n"
"v_mfma_f32_16x16x32_bf16 v[162:165], v[58:61], a[100:103], v[162:165]\n"
"buffer_load_dwordx4 v1, s[20:23], m0 offen sc1 lds\n"
"v_mfma_f32_16x16x32_bf16 v[150:153], v[62:65], a[104:107], v[150:153]\n"
"s_waitcnt lgkmcnt(4)\n"
"ds_read_b128 v[54:57], v2 offset:12288\n"
"v_rcp_f32 v223, v223\n"
"v_mfma_f32_16x16x32_bf16 v[154:157], v[62:65], a[108:111], v[154:157]\n"
"s_add_u32 m0, s59, 110592\n"
"v_exp_f32 v225, v225\n"
"v_mfma_f32_16x16x32_bf16 v[158:161], v[66:69], a[104:107], v[158:161]\n"
"s_waitcnt lgkmcnt(4)\n"
"ds_read_b128 v[58:61], v2 offset:13312\n"
"v_mul_f32 v139, v139, v223\n"
"v_mfma_f32_16x16x32_bf16 v[162:165], v[66:69], a[108:111], v[162:165]\n"
"v_rcp_f32 v224, v224\n"
"v_mfma_f32_16x16x32_bf16 v[150:153], v[70:73], a[112:115], v[150:153]\n"
"s_waitcnt lgkmcnt(4)\n"
"ds_read_b128 v[62:65], v2 offset:14336\n"
"v_cvt_pk_bf16_f32 v144, v138, v139\n"
"v_mfma_f32_16x16x32_bf16 v[154:157], v[70:73], a[116:119], v[154:157]\n"
"buffer_load_dwordx4 v1, s[20:23], m0 offen sc1 lds\n"
"v_mfma_f32_16x16x32_bf16 v[158:161], v[74:77], a[112:115], v[158:161]\n"
"ds_read_b128 v[66:69], v2 offset:15360\n"
"v_fma_f32 v225, v225, v229, v229\n"
"v_mul_f32 v140, v140, v224\n"
"v_mfma_f32_16x16x32_bf16 v[162:165], v[74:77], a[116:119], v[162:165]\n"
"s_waitcnt vmcnt(8)\n"
"v_cmp_ne_u64 s[18:19], v[254:255], v[8:9]\n"
"s_nop 0\n"
"s_and_b32 s18, s18, 65535\n"
"s_cbranch_scc0 L27_%=\n"
"s_mov_b32 s72, 0\n"
"L28_%=:\n"
"buffer_load_dwordx2 v[254:255], v11, s[20:23], s63 offen sc1\n"
"s_add_u32 s72, s72, 1\n"
"s_waitcnt vmcnt(0)\n"
"v_cmp_ne_u64 s[18:19], v[254:255], v[8:9]\n"
"s_nop 0\n"
"s_and_b32 s18, s18, 65535\n"
"s_cbranch_scc0 L27_%=\n"
"s_cmp_lt_u32 s72, 4194304\n"
"s_cbranch_scc1 L28_%=\n"
"L27_%=:\n"
"s_waitcnt lgkmcnt(5)\n"
"v_mfma_f32_16x16x32_bf16 v[150:153], v[78:81], a[120:123], v[150:153]\n"
"s_waitcnt lgkmcnt(4)\n"
"ds_read_b128 v[70:73], v2 offset:16384\n"
"v_mfma_f32_16x16x32_bf16 v[154:157], v[78:81], a[124:127], v[154:157]\n"
"v_rcp_f32 v225, v225\n"
"v_mfma_f32_16x16x32_bf16 v[158:161], v[82:85], a[120:123], v[158:161]\n"
"s_waitcnt lgkmcnt(4)\n"
"ds_read_b128 v[74:77], v2 offset:17408\n"
"v_mul_f32 v141, v141, v225\n"
"v_mfma_f32_16x16x32_bf16 v[162:165], v[82:85], a[124:127], v[162:165]\n"
"buffer_load_dwordx4 v[86:89], v32, s[96:99], 0 offen sc1\n"
"v_cvt_pk_bf16_f32 v145, v140, v141\n"
"v_mfma_f32_16x16x32_bf16 v[150:153], v[54:57], a[128:131], v[150:153]\n"
"s_waitcnt lgkmcnt(4)\n"
"ds_read_b128 v[78:81], v2 offset:18432\n"
"v_mfma_f32_16x16x32_bf16 v[154:157], v[54:57], a[132:135], v[154:157]\n"
"v_permlane16_swap_b32 v142, v144\n"
"v_mfma_f32_16x16x32_bf16 v[158:161], v[58:61], a[128:131], v[158:161]\n"
"s_waitcnt lgkmcnt(4)\n"
"ds_read_b128 v[82:85], v2 offset:19456\n"
"v_permlane16_swap_b32 v143, v145\n"
"v_mfma_f32_16x16x32_bf16 v[162:165], v[58:61], a[132:135], v[162:165]\n"
"buffer_load_dwordx4 v[90:93], v33, s[96:99], 0 offen sc1\n"
"v_mfma_f32_16x16x32_bf16 v[150:153], v[62:65], a[136:139], v[150:153]\n"
"s_waitcnt lgkmcnt(4)\n"
"ds_read_b128 v[54:57], v2 offset:20480\n"
"v_mfma_f32_16x16x32_bf16 v[154:157], v[62:65], a[140:143], v[154:157]\n"
"s_add_u32 m0, s59, 114688\n"
"v_mfma_f32_16x16x32_bf16 v[158:161], v[66:69], a[136:139], v[158:161]\n"
"s_waitcnt lgkmcnt(4)\n"
"ds_read_b128 v[58:61], v2 offset:21504\n"
"buffer_store_dwordx4 v[142:145], v39, s[96:99], s83 offen offset:512 sc1\n"
"v_mfma_f32_16x16x32_bf16 v[162:165], v[66:69], a[140:143], v[162:165]\n"
"buffer_load_dwordx4 v1, s[20:23], m0 offen sc1 lds\n"
"v_mfma_f32_16x16x32_bf16 v[150:153], v[70:73], a[144:147], v[150:153]\n"
"s_waitcnt lgkmcnt(4)\n"
"ds_read_b128 v[62:65], v2 offset:22528\n"
"v_mfma_f32_16x16x32_bf16 v[154:157], v[70:73], a[148:151], v[154:157]\n"
"v_mfma_f32_16x16x32_bf16 v[158:161], v[74:77], a[144:147], v[158:161]\n"
"s_waitcnt lgkmcnt(4)\n"
"ds_read_b128 v[66:69], v2 offset:23552\n"
"v_mfma_f32_16x16x32_bf16 v[162:165], v[74:77], a[148:151], v[162:165]\n"
"buffer_load_dwordx4 v[94:97], v32, s[96:99], 0 offen offset:2048 sc1\n"
"v_mfma_f32_16x16x32_bf16 v[150:153], v[78:81], a[152:155], v[150:153]\n"
"s_waitcnt lgkmcnt(4)\n"
"ds_read_b128 v[70:73], v2 offset:24576\n"
"v_mfma_f32_16x16x32_bf16 v[154:157], v[78:81], a[156:159], v[154:157]\n"
"v_mfma_f32_16x16x32_bf16 v[158:161], v[82:85], a[152:155], v[158:161]\n"
"s_waitcnt lgkmcnt(4)\n"
"ds_read_b128 v[74:77], v2 offset:25600\n"
"v_mfma_f32_16x16x32_bf16 v[162:165], v[82:85], a[156:159], v[162:165]\n"
"v_mfma_f32_16x16x32_bf16 v[166:169], v[54:57], a[0:3], v[166:169]\n"
"s_waitcnt lgkmcnt(4)\n"
"ds_read_b128 v[78:81], v2 offset:26624\n"
"v_mfma_f32_16x16x32_bf16 v[170:173], v[54:57], a[4:7], v[170:173]\n"
"v_mfma_f32_16x16x32_bf16 v[174:177], v[58:61], a[0:3], v[174:177]\n"
"ds_read_b128 v[82:85], v2 offset:27648\n"
"v_mfma_f32_16x16x32_bf16 v[178:181], v[58:61], a[4:7], v[178:181]\n"
"s_barrier\n"
"v_mul_f32 v218, v230, v150\n"
"v_exp_f32 v218, v218\n"
"s_waitcnt lgkmcnt(5)\n"
"v_mfma_f32_16x16x32_bf16 v[166:169], v[62:65], a[8:11], v[166:169]\n"
"s_waitcnt lgkmcnt(4)\n"
"ds_read_b128 v[54:57], v2 offset:28672\n"
"v_mul_f32 v219, v230, v151\n"
"v_mfma_f32_16x16x32_bf16 v[170:173], v[62:65], a[12:15], v[170:173]\n"
"buffer_load_dwordx4 v[98:101], v33, s[96:99], 0 offen offset:2048 sc1\n"
"v_mfma_f32_16x16x32_bf16 v[174:177], v[66:69], a[8:11], v[174:177]\n"
"s_waitcnt lgkmcnt(4)\n"
"ds_read_b128 v[58:61], v2 offset:29696\n"
"v_fma_f32 v218, v218, v228, v228\n"
"v_mfma_f32_16x16x32_bf16 v[178:181], v[66:69], a[12:15], v[178:181]\n"
"s_add_u32 m0, s59, 118784\n"
"v_exp_f32 v219, v219\n"
"v_mfma_f32_16x16x32_bf16 v[166:169], v[70:73], a[16:19], v[166:169]\n"
"s_waitcnt lgkmcnt(4)\n"
"ds_read_b128 v[62:65], v2 offset:30720\n"
"v_mul_f32 v150, v150, v158\n"
"v_mul_f32 v220, v230, v152\n"
"v_mfma_f32_16x16x32_bf16 v[170:173], v[70:73], a[20:23], v[170:173]\n"
"buffer_load_dwordx4 v1, s[20:23], m0 offen sc1 lds\n"
"v_mfma_f32_16x16x32_bf16 v[174:177], v[74:77], a[16:19], v[174:177]\n"
"s_waitcnt lgkmcnt(4)\n"
"ds_read_b128 v[66:69], v2 offset:31744\n"
"v_fma_f32 v219, v219, v228, v228\n"
"v_mfma_f32_16x16x32_bf16 v[178:181], v[74:77], a[20:23], v[178:181]\n"
"buffer_load_dwordx4 v[102:105], v32, s[96:99], s80 offen sc1\n"
"v_mfma_f32_16x16x32_bf16 v[166:169], v[78:81], a[24:27], v[166:169]\n"
"s_waitcnt lgkmcnt(4)\n"
"ds_read_b128 v[70:73], v2 offset:32768\n"
"v_mul_f32 v151, v151, v159\n"
"v_mul_f32 v221, v230, v153\n"
"v_mfma_f32_16x16x32_bf16 v[170:173], v[78:81], a[28:31], v[170:173]\n"
"s_add_u32 m0, s59, 122880\n"
"v_rcp_f32 v218, v218\n"
"v_mfma_f32_16x16x32_bf16 v[174:177], v[82:85], a[24:27], v[174:177]\n"
"s_waitcnt lgkmcnt(4)\n"
"ds_read_b128 v[74:77], v2 offset:33792\n"
"v_mul_f32 v150, v150, v218\n"
"v_mfma_f32_16x16x32_bf16 v[178:181], v[82:85], a[28:31], v[178:181]\n"
"v_exp_f32 v220, v220\n"
"v_mfma_f32_16x16x32_bf16 v[166:169], v[54:57], a[32:35], v[166:169]\n"
"s_waitcnt lgkmcnt(4)\n"
"ds_read_b128 v[78:81], v2 offset:34816\n"
"v_fma_f32 v220, v220, v228, v228\n"
"v_mfma_f32_16x16x32_bf16 v[170:173], v[54:57], a[36:39], v[170:173]\n"
"buffer_load_dwordx4 v1, s[20:23], m0 offen sc1 lds\n"
"v_mul_f32 v152, v152, v160\n"
"v_mfma_f32_16x16x32_bf16 v[174:177], v[58:61], a[32:35], v[174:177]\n"
"s_waitcnt lgkmcnt(4)\n"
"ds_read_b128 v[82:85], v2 offset:35840\n"
"v_mul_f32 v222, v231, v154\n"
"v_mfma_f32_16x16x32_bf16 v[178:181], v[58:61], a[36:39], v[178:181]\n"
"v_rcp_f32 v219, v219\n"
"v_mfma_f32_16x16x32_bf16 v[166:169], v[62:65], a[40:43], v[166:169]\n"
"s_waitcnt lgkmcnt(4)\n"
"ds_read_b128 v[54:57], v2 offset:36864\n"
"v_mul_f32 v151, v151, v219\n"
"v_mfma_f32_16x16x32_bf16 v[170:173], v[62:65], a[44:47], v[170:173]\n"
"buffer_load_dwordx4 v[106:109], v33, s[96:99], s80 offen sc1\n"
"v_cvt_pk_bf16_f32 v158, v150, v151\n"
"v_mfma_f32_16x16x32_bf16 v[174:177], v[66:69], a[40:43], v[174:177]\n"
"s_waitcnt lgkmcnt(4)\n"
"ds_read_b128 v[58:61], v2 offset:37888\n"
"v_mul_f32 v153, v153, v161\n"
"v_mfma_f32_16x16x32_bf16 v[178:181], v[66:69], a[44:47], v[178:181]\n"
"s_add_u32 m0, s59, 126976\n"
"v_exp_f32 v221, v221\n"
"v_mfma_f32_16x16x32_bf16 v[166:169], v[70:73], a[48:51], v[166:169]\n"
"s_waitcnt lgkmcnt(4)\n"
"ds_read_b128 v[62:65], v2 offset:38912\n"
"v_fma_f32 v221, v221, v228, v228\n"
"v_mfma_f32_16x16x32_bf16 v[170:173], v[70:73], a[52:55], v[170:173]\n"
"buffer_load_dwordx4 v1, s[20:23], m0 offen sc1 lds\n"
"v_mfma_f32_16x16x32_bf16 v[174:177], v[74:77], a[48:51], v[174:177]\n"
"s_waitcnt lgkmcnt(4)\n"
"ds_read_b128 v[66:69], v2 offset:39936\n"
"v_mul_f32 v223, v231, v155\n"
"v_mul_f32 v154, v154, v162\n"
"v_mfma_f32_16x16x32_bf16 v[178:181], v[74:77], a[52:55], v[178:181]\n"
"v_rcp_f32 v220, v220\n"
"v_mfma_f32_16x16x32_bf16 v[166:169], v[78:81], a[56:59], v[166:169]\n"
"ds_read_b128 v[70:73], v2 offset:40960\n"
"s_mov_b32 s3, 1703936\n"
"s_waitcnt vmcnt(8)\n"
"v_mfma_f32_16x16x32_bf16 v[170:173], v[78:81], a[60:63], v[170:173]\n"
"s_waitcnt lgkmcnt(5)\n"
"buffer_load_dwordx4 v[110:113], v32, s[96:99], s80 offen offset:2048 sc1\n"
"v_mfma_f32_16x16x32_bf16 v[174:177], v[82:85], a[56:59], v[174:177]\n"
"s_waitcnt lgkmcnt(4)\n"
"ds_read_b128 v[74:77], v2 offset:41984\n"
"s_add_u32 m0, s59, 131072\n"
"v_mul_f32 v152, v152, v220\n"
"v_mfma_f32_16x16x32_bf16 v[178:181], v[82:85], a[60:63], v[178:181]\n"
"buffer_store_dwordx2 v[8:9], v14, s[20:23], s3 offen sc1\n"
"v_mfma_f32_16x16x32_bf16 v[166:169], v[54:57], a[64:67], v[166:169]\n"
"s_waitcnt lgkmcnt(4)\n"
"ds_read_b128 v[78:81], v2 offset:43008\n"
"v_mul_f32 v224, v231, v156\n"
"v_mfma_f32_16x16x32_bf16 v[170:173], v[54:57], a[68:71], v[170:173]\n"
"buffer_load_dwordx4 v1, s[20:23], m0 offen sc1 lds\n"
"v_mfma_f32_16x16x32_bf16 v[174:177], v[58:61], a[64:67], v[174:177]\n"
"s_waitcnt lgkmcnt(4)\n"
"ds_read_b128 v[82:85], v2 offset:44032\n"
"v_mul_f32 v155, v155, v163\n"
"v_mfma_f32_16x16x32_bf16 v[178:181], v[58:61], a[68:71], v[178:181]\n"
"v_exp_f32 v222, v222\n"
"v_mul_f32 v225, v231, v157\n"
"v_mfma_f32_16x16x32_bf16 v[166:169], v[62:65], a[72:75], v[166:169]\n"
"s_waitcnt lgkmcnt(4)\n"
"ds_read_b128 v[54:57], v2 offset:45056\n"
"v_fma_f32 v222, v222, v229, v229\n"
"v_mfma_f32_16x16x32_bf16 v[170:173], v[62:65], a[76:79], v[170:173]\n"
"buffer_load_dwordx4 v[114:117], v33, s[96:99], s80 offen offset:2048 sc1\n"
"v_mfma_f32_16x16x32_bf16 v[174:177], v[66:69], a[72:75], v[174:177]\n"
"s_waitcnt lgkmcnt(4)\n"
"ds_read_b128 v[58:61], v2 offset:46080\n"
"v_mul_f32 v156, v156, v164\n"
"v_mfma_f32_16x16x32_bf16 v[178:181], v[66:69], a[76:79], v[178:181]\n"
"s_add_u32 m0, s59, 12288\n"
"v_add_u32 v1, 122880, v1\n"
"v_mul_f32 v157, v157, v165\n"
"v_mfma_f32_16x16x32_bf16 v[166:169], v[70:73], a[80:83], v[166:169]\n"
"s_waitcnt lgkmcnt(4)\n"
"ds_read_b128 v[62:65], v2 offset:47104\n"
"v_rcp_f32 v221, v221\n"
"v_mfma_f32_16x16x32_bf16 v[170:173], v[70:73], a[84:87], v[170:173]\n"
"buffer_load_dwordx4 v1, s[20:23], m0 offen sc1 lds\n"
"v_mfma_f32_16x16x32_bf16 v[174:177], v[74:77], a[80:83], v[174:177]\n"
"s_waitcnt lgkmcnt(4)\n"
"ds_read_b128 v[66:69], v2 offset:48128\n"
"v_mul_f32 v153, v153, v221\n"
"v_mfma_f32_16x16x32_bf16 v[178:181], v[74:77], a[84:87], v[178:181]\n"
"v_exp_f32 v223, v223\n"
"v_mfma_f32_16x16x32_bf16 v[166:169], v[78:81], a[88:91], v[166:169]\n"
"s_waitcnt lgkmcnt(4)\n"
"ds_read_b128 v[70:73], v2 offset:49152\n"
"v_cvt_pk_bf16_f32 v159, v152, v153\n"
"v_fma_f32 v223, v223, v229, v229\n"
"v_mfma_f32_16x16x32_bf16 v[170:173], v[78:81], a[92:95], v[170:173]\n"
"buffer_load_dwordx4 v[118:121], v32, s[96:99], s81 offen sc1\n"
"v_mfma_f32_16x16x32_bf16 v[174:177], v[82:85], a[88:91], v[174:177]\n"
"s_waitcnt lgkmcnt(4)\n"
"ds_read_b128 v[74:77], v2 offset:50176\n"
"v_mfma_f32_16x16x32_bf16 v[178:181], v[82:85], a[92:95], v[178:181]\n"
"s_add_u32 m0, s59, 16384\n"
"v_rcp_f32 v222, v222\n"
"v_mfma_f32_16x16x32_bf16 v[166:169], v[54:57], a[96:99], v[166:169]\n"
"s_waitcnt lgkmcnt(4)\n"
"ds_read_b128 v[78:81], v2 offset:51200\n"
"v_mul_f32 v154, v154, v222\n"
"v_mfma_f32_16x16x32_bf16 v[170:173], v[54:57], a[100:103], v[170:173]\n"
"buffer_load_dwordx4 v1, s[20:23], m0 offen sc1 lds\n"
"v_mfma_f32_16x16x32_bf16 v[174:177], v[58:61], a[96:99], v[174:177]\n"
"ds_read_b128 v[82:85], v2 offset:52224\n"
"v_exp_f32 v224, v224\n"
"v_mfma_f32_16x16x32_bf16 v[178:181], v[58:61], a[100:103], v[178:181]\n"
"s_barrier\n"
"s_waitcnt lgkmcnt(5)\n"
"v_mfma_f32_16x16x32_bf16 v[166:169], v[62:65], a[104:107], v[166:169]\n"
"s_waitcnt lgkmcnt(4)\n"
"ds_read_b128 v[54:57], v2 offset:53248\n"
"v_fma_f32 v224, v224, v229, v229\n"
"v_mfma_f32_16x16x32_bf16 v[170:173], v[62:65], a[108:111], v[170:173]\n"
"buffer_load_dwordx4 v[122:125], v33, s[96:99], s81 offen sc1\n"
"v_mfma_f32_16x16x32_bf16 v[174:177], v[66:69], a[104:107], v[174:177]\n"
"s_waitcnt lgkmcnt(4)\n"
"ds_read_b128 v[58:61], v2 offset:54272\n"
"v_rcp_f32 v223, v223\n"
"v_mfma_f32_16x16x32_bf16 v[178:181], v[66:69], a[108:111], v[178:181]\n"
"s_add_u32 m0, s59, 20480\n"
"v_exp_f32 v225, v225\n"
"v_mfma_f32_16x16x32_bf16 v[166:169], v[70:73], a[112:115], v[166:169]\n"
"s_waitcnt lgkmcnt(4)\n"
"ds_read_b128 v[62:65], v2 offset:55296\n"
"v_mul_f32 v155, v155, v223\n"
"v_mfma_f32_16x16x32_bf16 v[170:173], v[70:73], a[116:119], v[170:173]\n"
"buffer_load_dwordx4 v1, s[20:23], m0 offen sc1 lds\n"
"v_cvt_pk_bf16_f32 v160, v154, v155\n"
"v_mfma_f32_16x16x32_bf16 v[174:177], v[74:77], a[112:115], v[174:177]\n"
"s_waitcnt lgkmcnt(4)\n"
"ds_read_b128 v[66:69], v2 offset:56320\n"
"v_fma_f32 v225, v225, v229, v229\n"
"v_mfma_f32_16x16x32_bf16 v[178:181], v[74:77], a[116:119], v[178:181]\n"
"v_rcp_f32 v224, v224\n"
"v_mfma_f32_16x16x32_bf16 v[166:169], v[78:81], a[120:123], v[166:169]\n"
"s_waitcnt lgkmcnt(4)\n"
"ds_read_b128 v[70:73], v2 offset:57344\n"
"v_mul_f32 v156, v156, v224\n"
"v_mfma_f32_16x16x32_bf16 v[170:173], v[78:81], a[124:127], v[170:173]\n"
"buffer_load_dwordx4 v[126:129], v32, s[96:99], s81 offen offset:2048 sc1\n"
"v_mfma_f32_16x16x32_bf16 v[174:177], v[82:85], a[120:123], v[174:177]\n"
"s_waitcnt lgkmcnt(4)\n"
"ds_read_b128 v[74:77], v2 offset:58368\n"
"v_rcp_f32 v225, v225\n"
"v_mfma_f32_16x16x32_bf16 v[178:181], v[82:85], a[124:127], v[178:181]\n"
"s_add_u32 m0, s59, 24576\n"
"v_mul_f32 v157, v157, v225\n"
"v_cvt_pk_bf16_f32 v161, v156, v157\n"
"v_mfma_f32_16x16x32_bf16 v[166:169], v[54:57], a[128:131], v[166:169]\n"
"s_waitcnt lgkmcnt(4)\n"
"ds_read_b128 v[78:81], v2 offset:59392\n"
"v_mfma_f32_16x16x32_bf16 v[170:173], v[54:57], a[132:135], v[170:173]\n"
"buffer_load_dwordx4 v1, s[20:23], m0 offen sc1 lds\n"
"v_mfma_f32_16x16x32_bf16 v[174:177], v[58:61], a[128:131], v[174:177]\n"
"s_waitcnt lgkmcnt(4)\n"
"ds_read_b128 v[82:85], v2 offset:60416\n"
"v_permlane16_swap_b32 v158, v160\n"
"v_mfma_f32_16x16x32_bf16 v[178:181], v[58:61], a[132:135], v[178:181]\n"
"v_permlane16_swap_b32 v159, v161\n"
"v_mfma_f32_16x16x32_bf16 v[166:169], v[62:65], a[136:139], v[166:169]\n"
"s_waitcnt lgkmcnt(4)\n"
"ds_read_b128 v[54:57], v2 offset:61440\n"
"v_mfma_f32_16x16x32_bf16 v[170:173], v[62:65], a[140:143], v[170:173]\n"
"s_mov_b32 s63, 1703936\n"
"v_mfma_f32_16x16x32_bf16 v[174:177], v[66:69], a[136:139], v[174:177]\n"
"s_waitcnt lgkmcnt(4)\n"
"ds_read_b128 v[58:61], v2 offset:62464\n"
"v_mfma_f32_16x16x32_bf16 v[178:181], v[66:69], a[140:143], v[178:181]\n"
"buffer_load_dwordx2 v[254:255], v11, s[20:23], s63 offen sc1\n"
"v_mfma_f32_16x16x32_bf16 v[166:169], v[70:73], a[144:147], v[166:169]\n"
"s_waitcnt lgkmcnt(4)\n"
"ds_read_b128 v[62:65], v2 offset:63488\n"
"buffer_store_dwordx4 v[158:161], v39, s[96:99], s87 offen sc1\n"
"v_mfma_f32_16x16x32_bf16 v[170:173], v[70:73], a[148:151], v[170:173]\n"
"buffer_load_dwordx4 v[130:133], v33, s[96:99], s81 offen offset:2048 sc1\n"
"v_mfma_f32_16x16x32_bf16 v[174:177], v[74:77], a[144:147], v[174:177]\n"
"s_waitcnt lgkmcnt(4)\n"
"ds_read_b128 v[66:69], v2 offset:64512\n"
"s_add_u32 m0, s59, 28672\n"
"v_mfma_f32_16x16x32_bf16 v[178:181], v[74:77], a[148:151], v[178:181]\n"
"buffer_load_dwordx4 v1, s[20:23], m0 offen sc1 lds\n"
"v_mfma_f32_16x16x32_bf16 v[166:169], v[78:81], a[152:155], v[166:169]\n"
"s_waitcnt lgkmcnt(4)\n"
"ds_read_b128 v[70:73], v3\n"
"v_mfma_f32_16x16x32_bf16 v[170:173], v[78:81], a[156:159], v[170:173]\n"
"v_mfma_f32_16x16x32_bf16 v[174:177], v[82:85], a[152:155], v[174:177]\n"
"s_waitcnt lgkmcnt(4)\n"
"ds_read_b128 v[74:77], v3 offset:1024\n"
"v_mfma_f32_16x16x32_bf16 v[178:181], v[82:85], a[156:159], v[178:181]\n"
"buffer_load_dwordx4 v[134:137], v32, s[96:99], s82 offen sc1\n"
"v_mfma_f32_16x16x32_bf16 v[182:185], v[54:57], a[0:3], v[182:185]\n"
"s_waitcnt lgkmcnt(4)\n"
"ds_read_b128 v[78:81], v3 offset:2048\n"
"v_mfma_f32_16x16x32_bf16 v[186:189], v[54:57], a[4:7], v[186:189]\n"
"s_add_u32 m0, s59, 32768\n"
"v_mfma_f32_16x16x32_bf16 v[190:193], v[58:61], a[0:3], v[190:193]\n"
"s_waitcnt lgkmcnt(4)\n"
"ds_read_b128 v[82:85], v3 offset:3072\n"
"v_mfma_f32_16x16x32_bf16 v[194:197], v[58:61], a[4:7], v[194:197]\n"
"buffer_load_dwordx4 v1, s[20:23], m0 offen sc1 lds\n"
"v_mul_f32 v218, v230, v166\n"
"v_exp_f32 v218, v218\n"
"v_mfma_f32_16x16x32_bf16 v[182:185], v[62:65], a[8:11], v[182:185]\n"
"s_waitcnt lgkmcnt(4)\n"
"ds_read_b128 v[54:57], v3 offset:4096\n"
"v_mul_f32 v219, v230, v167\n"
"v_mfma_f32_16x16x32_bf16 v[186:189], v[62:65], a[12:15], v[186:189]\n"
"v_exp_f32 v219, v219\n"
"v_mfma_f32_16x16x32_bf16 v[190:193], v[66:69], a[8:11], v[190:193]\n"
"s_waitcnt lgkmcnt(4)\n"
"ds_read_b128 v[58:61], v3 offset:5120\n"
"v_fma_f32 v218, v218, v228, v228\n"
"v_mul_f32 v166, v166, v174\n"
"v_mfma_f32_16x16x32_bf16 v[194:197], v[66:69], a[12:15], v[194:197]\n"
"buffer_load_dwordx4 v[138:141], v33, s[96:99], s82 offen sc1\n"
"v_mfma_f32_16x16x32_bf16 v[182:185], v[70:73], a[16:19], v[182:185]\n"
"s_waitcnt lgkmcnt(4)\n"
"ds_read_b128 v[62:65], v3 offset:6144\n"
"v_mul_f32 v220, v230, v168\n"
"v_fma_f32 v219, v219, v228, v228\n"
"v_mfma_f32_16x16x32_bf16 v[186:189], v[70:73], a[20:23], v[186:189]\n"
"s_add_u32 m0, s59, 36864\n"
"v_add_u32 v1, 49152, v1\n"
"v_mul_f32 v167, v167, v175\n"
"v_mfma_f32_16x16x32_bf16 v[190:193], v[74:77], a[16:19], v[190:193]\n"
"s_waitcnt lgkmcnt(4)\n"
"ds_read_b128 v[66:69], v3 offset:7168\n"
"v_mul_f32 v221, v230, v169\n"
"v_mul_f32 v168, v168, v176\n"
"v_mfma_f32_16x16x32_bf16 v[194:197], v[74:77], a[20:23], v[194:197]\n"
"buffer_load_dwordx4 v1, s[20:23], m0 offen sc1 lds\n"
"v_mfma_f32_16x16x32_bf16 v[182:185], v[78:81], a[24:27], v[182:185]\n"
"s_waitcnt lgkmcnt(4)\n"
"ds_read_b128 v[70:73], v2\n"
"v_mul_f32 v222, v231, v170\n"
"v_mfma_f32_16x16x32_bf16 v[186:189], v[78:81], a[28:31], v[186:189]\n"
"v_rcp_f32 v218, v218\n"
"v_mul_f32 v169, v169, v177\n"
"v_mfma_f32_16x16x32_bf16 v[190:193], v[82:85], a[24:27], v[190:193]\n"
"s_waitcnt lgkmcnt(4)\n"
"ds_read_b128 v[74:77], v2 offset:1024\n"
"v_mul_f32 v166, v166, v218\n"
"v_mfma_f32_16x16x32_bf16 v[194:197], v[82:85], a[28:31], v[194:197]\n"
"buffer_load_dwordx4 v[142:145], v32, s[96:99], s82 offen offset:2048 sc1\n"
"v_mul_f32 v223, v231, v171\n"
"v_mfma_f32_16x16x32_bf16 v[182:185], v[54:57], a[32:35], v[182:185]\n"
"s_waitcnt lgkmcnt(4)\n"
"ds_read_b128 v[78:81], v2 offset:2048\n"
"v_mul_f32 v170, v170, v178\n"
"v_mfma_f32_16x16x32_bf16 v[186:189], v[54:57], a[36:39], v[186:189]\n"
"s_add_u32 m0, s59, 40960\n"
"v_exp_f32 v220, v220\n"
"v_mfma_f32_16x16x32_bf16 v[190:193], v[58:61], a[32:35], v[190:193]\n"
"ds_read_b128 v[82:85], v2 offset:3072\n"
"v_fma_f32 v220, v220, v228, v228\n"
"v_mul_f32 v224, v231, v172\n"
"v_mfma_f32_16x16x32_bf16 v[194:197], v[58:61], a[36:39], v[194:197]\n"
"s_barrier\n"
"s_waitcnt lgkmcnt(5)\n"
"v_mfma_f32_16x16x32_bf16 v[182:185], v[62:65], a[40:43], v[182:185]\n"
"s_waitcnt lgkmcnt(4)\n"
"ds_read_b128 v[54:57], v2 offset:4096\n"
"v_mul_f32 v171, v171, v179\n"
"v_mfma_f32_16x16x32_bf16 v[186:189], v[62:65], a[44:47], v[186:189]\n"
"buffer_load_dwordx4 v1, s[20:23], m0 offen sc1 lds\n"
"v_mfma_f32_16x16x32_bf16 v[190:193], v[66:69], a[40:43], v[190:193]\n"
"s_waitcnt lgkmcnt(4)\n"
"ds_read_b128 v[58:61], v2 offset:5120\n"
"v_mul_f32 v225, v231, v173\n"
"v_rcp_f32 v219, v219\n"
"v_mfma_f32_16x16x32_bf16 v[194:197], v[66:69], a[44:47], v[194:197]\n"
"buffer_load_dwordx4 v[146:149], v33, s[96:99], s82 offen offset:2048 sc1\n"
"v_mfma_f32_16x16x32_bf16 v[182:185], v[70:73], a[48:51], v[182:185]\n"
"s_waitcnt lgkmcnt(4)\n"
"ds_read_b128 v[62:65], v2 offset:6144\n"
"v_mul_f32 v167, v167, v219\n"
"v_mfma_f32_16x16x32_bf16 v[186:189], v[70:73], a[52:55], v[186:189]\n"
"s_add_u32 m0, s59, 45056\n"
"v_exp_f32 v221, v221\n"
"v_cvt_pk_bf16_f32 v174, v166, v167\n"
"v_mfma_f32_16x16x32_bf16 v[190:193], v[74:77], a[48:51], v[190:193]\n"
"s_waitcnt lgkmcnt(4)\n"
"ds_read_b128 v[66:69], v2 offset:7168\n"
"v_fma_f32 v221, v221, v228, v228\n"
"v_mfma_f32_16x16x32_bf16 v[194:197], v[74:77], a[52:55], v[194:197]\n"
"buffer_load_dwordx4 v1, s[20:23], m0 offen sc1 lds\n"
"v_mul_f32 v172, v172, v180\n"
"v_mfma_f32_16x16x32_bf16 v[182:185], v[78:81], a[56:59], v[182:185]\n"
"ds_read_b128 v[70:73], v2 offset:8192\n"
"v_mul_f32 v173, v173, v181\n"
"v_mfma_f32_16x16x32_bf16 v[186:189], v[78:81], a[60:63], v[186:189]\n"
"s_waitcnt vmcnt(11)\n"
"v_cmp_ne_u64 s[18:19], v[254:255], v[8:9]\n"
"s_nop 0\n"
"s_and_b32 s18, s18, 65535\n"
"s_cbranch_scc0 L29_%=\n"
"s_mov_b32 s72, 0\n"
"L30_%=:\n"
"buffer_load_dwordx2 v[254:255], v11, s[20:23], s63 offen sc1\n"
"s_add_u32 s72, s72, 1\n"
"s_waitcnt vmcnt(0)\n"
"v_cmp_ne_u64 s[18:19], v[254:255], v[8:9]\n"
"s_nop 0\n"
"s_and_b32 s18, s18, 65535\n"
"s_cbranch_scc0 L29_%=\n"
"s_cmp_lt_u32 s72, 4194304\n"
"s_cbranch_scc1 L30_%=\n"
"L29_%=:\n"
"s_waitcnt lgkmcnt(5)\n"
"v_mfma_f32_16x16x32_bf16 v[190:193], v[82:85], a[56:59], v[190:193]\n"
"s_waitcnt lgkmcnt(4)\n"
"ds_read_b128 v[74:77], v2 offset:9216\n"
"v_rcp_f32 v220, v220\n"
"v_mfma_f32_16x16x32_bf16 v[194:197], v[82:85], a[60:63], v[194:197]\n"
"v_exp_f32 v222, v222\n"
"v_mfma_f32_16x16x32_bf16 v[182:185], v[54:57], a[64:67], v[182:185]\n"
"s_waitcnt lgkmcnt(4)\n"
"ds_read_b128 v[78:81], v2 offset:10240\n"
"v_mul_f32 v168, v168, v220\n"
"v_fma_f32 v222, v222, v229, v229\n"
"v_mfma_f32_16x16x32_bf16 v[186:189], v[54:57], a[68:71], v[186:189]\n"
"buffer_load_dwordx4 a[0:3], v32, s[96:99], s83 offen sc1\n"
"v_mfma_f32_16x16x32_bf16 v[190:193], v[58:61], a[64:67], v[190:193]\n"
"s_waitcnt lgkmcnt(4)\n"
"ds_read_b128 v[82:85], v2 offset:11264\n"
"v_mfma_f32_16x16x32_bf16 v[194:197], v[58:61], a[68:71], v[194:197]\n"
"s_add_u32 m0, s59, 49152\n"
"v_rcp_f32 v221, v221\n"
"v_mfma_f32_16x16x32_bf16 v[182:185], v[62:65], a[72:75], v[182:185]\n"
"s_waitcnt lgkmcnt(4)\n"
"ds_read_b128 v[54:57], v4 offset:4096\n"
"v_mul_f32 v169, v169, v221\n"
"v_cvt_pk_bf16_f32 v175, v168, v169\n"
"v_mfma_f32_16x16x32_bf16 v[186:189], v[62:65], a[76:79], v[186:189]\n"
"buffer_load_dwordx4 v1, s[20:23], m0 offen sc1 lds\n"
"v_mfma_f32_16x16x32_bf16 v[190:193], v[66:69], a[72:75], v[190:193]\n"
"s_waitcnt lgkmcnt(4)\n"
"ds_read_b128 v[58:61], v4 offset:5120\n"
"v_exp_f32 v223, v223\n"
"v_mfma_f32_16x16x32_bf16 v[194:197], v[66:69], a[76:79], v[194:197]\n"
"s_add_u32 m0, s59, 53248\n"
"v_rcp_f32 v222, v222\n"
"v_fma_f32 v223, v223, v229, v229\n"
"v_mfma_f32_16x16x32_bf16 v[182:185], v[70:73], a[80:83], v[182:185]\n"
"s_waitcnt lgkmcnt(4)\n"
"ds_read_b128 v[62:65], v4 offset:6144\n"
"v_mul_f32 v170, v170, v222\n"
"v_mfma_f32_16x16x32_bf16 v[186:189], v[70:73], a[84:87], v[186:189]\n"
"buffer_load_dwordx4 v1, s[20:23], m0 offen sc1 lds\n"
"v_mfma_f32_16x16x32_bf16 v[190:193], v[74:77], a[80:83], v[190:193]\n"
"s_waitcnt lgkmcnt(4)\n"
"ds_read_b128 v[66:69], v4 offset:7168\n"
"v_exp_f32 v224, v224\n"
"v_mfma_f32_16x16x32_bf16 v[194:197], v[74:77], a[84:87], v[194:197]\n"
"v_rcp_f32 v223, v223\n"
"v_mfma_f32_16x16x32_bf16 v[182:185], v[78:81], a[88:91], v[182:185]\n"
"s_waitcnt lgkmcnt(4)\n"
"ds_read_b128 v[70:73], v4 offset:8192\n"
"v_fma_f32 v224, v224, v229, v229\n"
"v_mul_f32 v171, v171, v223\n"
"v_mfma_f32_16x16x32_bf16 v[186:189], v[78:81], a[92:95], v[186:189]\n"
"buffer_load_dwordx4 a[4:7], v33, s[96:99], s83 offen sc1\n"
"v_mfma_f32_16x16x32_bf16 v[190:193], v[82:85], a[88:91], v[190:193]\n"
"s_waitcnt lgkmcnt(4)\n"
"ds_read_b128 v[74:77], v4 offset:9216\n"
"v_cvt_pk_bf16_f32 v176, v170, v171\n"
"v_mfma_f32_16x16x32_bf16 v[194:197], v[82:85], a[92:95], v[194:197]\n"
"s_add_u32 m0, s59, 57344\n"
"v_exp_f32 v225, v225\n"
"v_mfma_f32_16x16x32_bf16 v[182:185], v[54:57], a[96:99], v[182:185]\n"
"s_waitcnt lgkmcnt(4)\n"
"ds_read_b128 v[78:81], v4 offset:10240\n"
"v_fma_f32 v225, v225, v229, v229\n"
"v_mfma_f32_16x16x32_bf16 v[186:189], v[54:57], a[100:103], v[186:189]\n"
"buffer_load_dwordx4 v1, s[20:23], m0 offen sc1 lds\n"
"v_rcp_f32 v224, v224\n"
"v_mfma_f32_16x16x32_bf16 v[190:193], v[58:61], a[96:99], v[190:193]\n"
"s_waitcnt lgkmcnt(4)\n"
"ds_read_b128 v[82:85], v4 offset:11264\n"
"v_mul_f32 v172, v172, v224\n"
"v_mfma_f32_16x16x32_bf16 v[194:197], v[58:61], a[100:103], v[194:197]\n"
"s_add_u32 m0, s59, 61440\n"
"v_rcp_f32 v225, v225\n"
"v_mfma_f32_16x16x32_bf16 v[182:185], v[62:65], a[104:107], v[182:185]\n"
"s_waitcnt lgkmcnt(4)\n"
"ds_read_b128 v[54:57], v4 offset:12288\n"
"v_mul_f32 v173, v173, v225\n"
"v_cvt_pk_bf16_f32 v177, v172, v173\n"
"v_mfma_f32_16x16x32_bf16 v[186:189], v[62:65], a[108:111], v[186:189]\n"
"buffer_load_dwordx4 v1, s[20:23], m0 offen sc1 lds\n"
"v_mfma_f32_16x16x32_bf16 v[190:193], v[66:69], a[104:107], v[190:193]\n"
"s_waitcnt lgkmcnt(4)\n"
"ds_read_b128 v[58:61], v4 offset:13312\n"
"v_permlane16_swap_b32 v174, v176\n"
"v_mfma_f32_16x16x32_bf16 v[194:197], v[66:69], a[108:111], v[194:197]\n"
"v_permlane16_swap_b32 v175, v177\n"
"v_mfma_f32_16x16x32_bf16 v[182:185], v[70:73], a[112:115], v[182:185]\n"
"s_waitcnt lgkmcnt(4)\n"
"ds_read_b128 v[62:65], v4 offset:14336\n"
"v_mfma_f32_16x16x32_bf16 v[186:189], v[70:73], a[116:119], v[186:189]\n"
"buffer_load_dwordx4 a[8:11], v32, s[96:99], s83 offen offset:2048 sc1\n"
"v_mfma_f32_16x16x32_bf16 v[190:193], v[74:77], a[112:115], v[190:193]\n"
"s_waitcnt lgkmcnt(4)\n"
"ds_read_b128 v[66:69], v4 offset:15360\n"
"buffer_store_dwordx4 v[174:177], v39, s[96:99], s87 offen offset:512 sc1\n"
"v_mfma_f32_16x16x32_bf16 v[194:197], v[74:77], a[116:119], v[194:197]\n"
"s_add_u32 m0, s59, 65536\n"
"v_mfma_f32_16x16x32_bf16 v[182:185], v[78:81], a[120:123], v[182:185]\n"
"s_waitcnt lgkmcnt(4)\n"
"ds_read_b128 v[70:73], v4 offset:16384\n"
"v_mfma_f32_16x16x32_bf16 v[186:189], v[78:81], a[124:127], v[186:189]\n"
"buffer_load_dwordx4 v1, s[20:23], m0 offen sc1 lds\n"
"v_mfma_f32_16x16x32_bf16 v[190:193], v[82:85], a[120:123], v[190:193]\n"
"s_waitcnt lgkmcnt(4)\n"
"ds_read_b128 v[74:77], v4 offset:17408\n"
"v_mfma_f32_16x16x32_bf16 v[194:197], v[82:85], a[124:127], v[194:197]\n"
"v_mfma_f32_16x16x32_bf16 v[182:185], v[54:57], a[128:131], v[182:185]\n"
"s_waitcnt lgkmcnt(4)\n"
"ds_read_b128 v[78:81], v4 offset:18432\n"
"v_mfma_f32_16x16x32_bf16 v[186:189], v[54:57], a[132:135], v[186:189]\n"
"v_mfma_f32_16x16x32_bf16 v[190:193], v[58:61], a[128:131], v[190:193]\n"
"s_waitcnt lgkmcnt(4)\n"
"ds_read_b128 v[82:85], v4 offset:19456\n"
"v_mfma_f32_16x16x32_bf16 v[194:197], v[58:61], a[132:135], v[194:197]\n"
"v_mfma_f32_16x16x32_bf16 v[182:185], v[62:65], a[136:139], v[182:185]\n"
"v_mfma_f32_16x16x32_bf16 v[186:189], v[62:65], a[140:143], v[186:189]\n"
"s_waitcnt lgkmcnt(4)\n"
"buffer_load_dwordx4 a[12:15], v33, s[96:99], s83 offen offset:2048 sc1\n"
"v_mfma_f32_16x16x32_bf16 v[190:193], v[66:69], a[136:139], v[190:193]\n"
"v_mfma_f32_16x16x32_bf16 v[194:197], v[66:69], a[140:143], v[194:197]\n"
"s_waitcnt lgkmcnt(3)\n"
"v_mfma_f32_16x16x32_bf16 v[182:185], v[70:73], a[144:147], v[182:185]\n"
"v_mfma_f32_16x16x32_bf16 v[186:189], v[70:73], a[148:151], v[186:189]\n"
"s_waitcnt lgkmcnt(2)\n"
"v_mfma_f32_16x16x32_bf16 v[190:193], v[74:77], a[144:147], v[190:193]\n"
"v_mfma_f32_16x16x32_bf16 v[194:197], v[74:77], a[148:151], v[194:197]\n"
"s_waitcnt lgkmcnt(1)\n"
"v_mfma_f32_16x16x32_bf16 v[182:185], v[78:81], a[152:155], v[182:185]\n"
"v_mfma_f32_16x16x32_bf16 v[186:189], v[78:81], a[156:159], v[186:189]\n"
"s_waitcnt lgkmcnt(0)\n"
"v_mfma_f32_16x16x32_bf16 v[190:193], v[82:85], a[152:155], v[190:193]\n"
"v_mfma_f32_16x16x32_bf16 v[194:197], v[82:85], a[156:159], v[194:197]\n"
"buffer_load_dwordx4 a[16:19], v32, s[96:99], s84 offen sc1\n"
"s_nop 2\n"
"v_pk_mul_f32 v[218:219], v[182:183], v[230:231] op_sel_hi:[1,0]\n"
"v_exp_f32 v218, v218\n"
"v_exp_f32 v219, v219\n"
"v_pk_mul_f32 v[182:183], v[182:183], v[190:191]\n"
"v_pk_fma_f32 v[218:219], v[218:219], v[228:229], v[228:229] op_sel_hi:[1,0,0]\n"
"v_rcp_f32 v218, v218\n"
"buffer_load_dwordx4 a[20:23], v33, s[96:99], s84 offen sc1\n"
"v_rcp_f32 v219, v219\n"
"s_nop 0\n"
"v_pk_mul_f32 v[182:183], v[182:183], v[218:219]\n"
"v_cvt_pk_bf16_f32 v190, v182, v183\n"
"v_pk_mul_f32 v[220:221], v[184:185], v[230:231] op_sel_hi:[1,0]\n"
"v_exp_f32 v220, v220\n"
"v_exp_f32 v221, v221\n"
"v_pk_mul_f32 v[184:185], v[184:185], v[192:193]\n"
"buffer_load_dwordx4 a[24:27], v32, s[96:99], s84 offen offset:2048 sc1\n"
"v_pk_fma_f32 v[220:221], v[220:221], v[228:229], v[228:229] op_sel_hi:[1,0,0]\n"
"v_rcp_f32 v220, v220\n"
"v_rcp_f32 v221, v221\n"
"s_nop 0\n"
"v_pk_mul_f32 v[184:185], v[184:185], v[220:221]\n"
"v_cvt_pk_bf16_f32 v191, v184, v185\n"
"v_pk_mul_f32 v[218:219], v[186:187], v[230:231] op_sel:[0,1]\n"
"buffer_load_dwordx4 a[28:31], v33, s[96:99], s84 offen offset:2048 sc1\n"
"v_exp_f32 v218, v218\n"
"v_exp_f32 v219, v219\n"
"v_pk_mul_f32 v[186:187], v[186:187], v[194:195]\n"
"v_pk_fma_f32 v[218:219], v[218:219], v[228:229], v[228:229] op_sel:[0,1,1]\n"
"v_rcp_f32 v218, v218\n"
"v_rcp_f32 v219, v219\n"
"s_nop 0\n"
"v_pk_mul_f32 v[186:187], v[186:187], v[218:219]\n"
"buffer_load_dwordx4 a[32:35], v32, s[96:99], s85 offen sc1\n"
"v_cvt_pk_bf16_f32 v192, v186, v187\n"
"v_pk_mul_f32 v[220:221], v[188:189], v[230:231] op_sel:[0,1]\n"
"v_exp_f32 v220, v220\n"
"v_exp_f32 v221, v221\n"
"v_pk_mul_f32 v[188:189], v[188:189], v[196:197]\n"
"v_pk_fma_f32 v[220:221], v[220:221], v[228:229], v[228:229] op_sel:[0,1,1]\n"
"buffer_load_dwordx4 a[36:39], v33, s[96:99], s85 offen sc1\n"
"v_rcp_f32 v220, v220\n"
"v_rcp_f32 v221, v221\n"
"s_nop 0\n"
"v_pk_mul_f32 v[188:189], v[188:189], v[220:221]\n"
"v_cvt_pk_bf16_f32 v193, v188, v189\n"
"v_permlane16_swap_b32 v190, v192\n"
"s_nop 0\n"
"v_permlane16_swap_b32 v191, v193\n"
"buffer_store_dwordx4 v[190:193], v42, s[96:99], 0 offen sc1\n"
"; P5 layer 1\n"
"s_barrier\n"
"ds_read_b128 v[54:57], v7 offset:20480\n"
"ds_read_b128 v[58:61], v7 offset:22528\n"
"ds_read_b128 v[62:65], v6 offset:8192\n"
"ds_read_b128 v[66:69], v6 offset:10240\n"
"ds_read_b128 v[70:73], v6 offset:12288\n"
"ds_read_b128 v[74:77], v6 offset:14336\n"
"s_waitcnt lgkmcnt(5)\n"
"v_mfma_f32_16x16x32_bf16 v[150:153], v[54:57], v[86:89], v[198:201]\n"
"s_waitcnt lgkmcnt(4)\n"
"ds_read_b128 v[78:81], v6 offset:16384\n"
"v_mfma_f32_16x16x32_bf16 v[154:157], v[54:57], v[90:93], 0\n"
"buffer_load_dwordx4 a[40:43], v32, s[96:99], s85 offen offset:2048 sc1\n"
"v_mfma_f32_16x16x32_bf16 v[158:161], v[58:61], v[86:89], v[202:205]\n"
"ds_read_b128 v[82:85], v6 offset:18432\n"
"s_mov_b32 s3, 1835008\n"
"s_waitcnt vmcnt(10)\n"
"v_mfma_f32_16x16x32_bf16 v[162:165], v[58:61], v[90:93], 0\n"
"s_waitcnt lgkmcnt(5)\n"
"v_mfma_f32_16x16x32_bf16 v[166:169], v[62:65], v[86:89], v[206:209]\n"
"s_waitcnt lgkmcnt(4)\n"
"ds_read_b128 v[54:57], v6 offset:20480\n"
"v_mfma_f32_16x16x32_bf16 v[170:173], v[62:65], v[90:93], 0\n"
"buffer_store_dwordx2 v[8:9], v14, s[20:23], s3 offen sc1\n"
"v_mfma_f32_16x16x32_bf16 v[174:177], v[66:69], v[86:89], v[210:213]\n"
"s_waitcnt lgkmcnt(4)\n"
"ds_read_b128 v[58:61], v6 offset:22528\n"
"v_mfma_f32_16x16x32_bf16 v[178:181], v[66:69], v[90:93], 0\n"
"buffer_load_dwordx4 a[44:47], v33, s[96:99], s85 offen offset:2048 sc1\n"
"v_mfma_f32_16x16x32_bf16 v[182:185], v[70:73], v[86:89], v[214:217]\n"
"s_waitcnt lgkmcnt(4)\n"
"ds_read_b128 v[62:65], v6 offset:24576\n"
"v_mfma_f32_16x16x32_bf16 v[186:189], v[70:73], v[90:93], 0\n"
"buffer_load_dwordx4 a[48:51], v32, s[96:99], s86 offen sc1\n"
"v_mfma_f32_16x16x32_bf16 v[150:153], v[74:77], v[94:97], v[150:153]\n"
"s_waitcnt lgkmcnt(4)\n"
"ds_read_b128 v[66:69], v6 offset:26624\n"
"v_mfma_f32_16x16x32_bf16 v[154:157], v[74:77], v[98:101], v[154:157]\n"
"buffer_load_dwordx4 a[52:55], v33, s[96:99], s86 offen sc1\n"
"v_mfma_f32_16x16x32_bf16 v[158:161], v[78:81], v[94:97], v[158:161]\n"
"s_waitcnt lgkmcnt(4)\n"
"ds_read_b128 v[70:73], v6 offset:28672\n"
"v_mfma_f32_16x16x32_bf16 v[162:165], v[78:81], v[98:101], v[162:165]\n"
"v_mfma_f32_16x16x32_bf16 v[166:169], v[82:85], v[94:97], v[166:169]\n"
"s_waitcnt lgkmcnt(4)\n"
"ds_read_b128 v[74:77], v6 offset:30720\n"
"v_mfma_f32_16x16x32_bf16 v[170:173], v[82:85], v[98:101], v[170:173]\n"
"buffer_load_dwordx4 a[56:59], v32, s[96:99], s86 offen offset:2048 sc1\n"
"v_mfma_f32_16x16x32_bf16 v[174:177], v[54:57], v[94:97], v[174:177]\n"
"s_waitcnt lgkmcnt(4)\n"
"ds_read_b128 v[78:81], v6 offset:32768\n"
"v_mfma_f32_16x16x32_bf16 v[178:181], v[54:57], v[98:101], v[178:181]\n"
"v_mfma_f32_16x16x32_bf16 v[182:185], v[58:61], v[94:97], v[182:185]\n"
"s_waitcnt lgkmcnt(4)\n"
"ds_read_b128 v[82:85], v6 offset:34816\n"
"v_mfma_f32_16x16x32_bf16 v[186:189], v[58:61], v[98:101], v[186:189]\n"
"buffer_load_dwordx4 a[60:63], v33, s[96:99], s86 offen offset:2048 sc1\n"
"v_mfma_f32_16x16x32_bf16 v[150:153], v[62:65], v[102:105], v[150:153]\n"
"s_waitcnt lgkmcnt(4)\n"
"ds_read_b128 v[54:57], v6 offset:36864\n"
"v_mfma_f32_16x16x32_bf16 v[154:157], v[62:65], v[106:109], v[154:157]\n"
"s_add_u32 m0, s59, 135168\n"
"v_add_u32 v1, 4294901760, v1\n"
"v_mfma_f32_16x16x32_bf16 v[158:161], v[66:69], v[102:105], v[158:161]\n"
"s_waitcnt lgkmcnt(4)\n"
"ds_read_b128 v[58:61], v6 offset:38912\n"
"v_mfma_f32_16x16x32_bf16 v[162:165], v[66:69], v[106:109], v[162:165]\n"
"buffer_load_dwordx4 v1, s[20:23], m0 offen sc1 lds\n"
"v_mfma_f32_16x16x32_bf16 v[166:169], v[70:73], v[102:105], v[166:169]\n"
"s_waitcnt lgkmcnt(4)\n"
"ds_read_b128 v[62:65], v6 offset:40960\n"
"v_mfma_f32_16x16x32_bf16 v[170:173], v[70:73], v[106:109], v[170:173]\n"
"s_add_u32 m0, s59, 139264\n"
"v_mfma_f32_16x16x32_bf16 v[174:177], v[74:77], v[102:105], v[174:177]\n"
"s_waitcnt lgkmcnt(4)\n"
"ds_read_b128 v[66:69], v6 offset:43008\n"
"v_mfma_f32_16x16x32_bf16 v[178:181], v[74:77], v[106:109], v[178:181]\n"
"buffer_load_dwordx4 v1, s[20:23], m0 offen sc1 lds\n"
"v_mfma_f32_16x16x32_bf16 v[182:185], v[78:81], v[102:105], v[182:185]\n"
"s_waitcnt lgkmcnt(4)\n"
"ds_read_b128 v[70:73], v6 offset:45056\n"
"v_mfma_f32_16x16x32_bf16 v[186:189], v[78:81], v[106:109], v[186:189]\n"
"s_add_u32 m0, s59, 143360\n"
"v_mfma_f32_16x16x32_bf16 v[150:153], v[82:85], v[110:113], v[150:153]\n"
"s_waitcnt lgkmcnt(4)\n"
"ds_read_b128 v[74:77], v6 offset:47104\n"
"v_mfma_f32_16x16x32_bf16 v[154:157], v[82:85], v[114:117], v[154:157]\n"
"buffer_load_dwordx4 v1, s[20:23], m0 offen sc1 lds\n"
"v_mfma_f32_16x16x32_bf16 v[158:161], v[54:57], v[110:113], v[158:161]\n"
"s_waitcnt lgkmcnt(4)\n"
"ds_read_b128 v[78:81], v6 offset:49152\n"
"s_add_u32 m0, s59, 147456\n"
"v_mfma_f32_16x16x32_bf16 v[162:165], v[54:57], v[114:117], v[162:165]\n"
"buffer_load_dwordx4 v1, s[20:23], m0 offen sc1 lds\n"
"v_mfma_f32_16x16x32_bf16 v[166:169], v[58:61], v[110:113], v[166:169]\n"
"ds_read_b128 v[82:85], v6 offset:51200\n"
"s_add_u32 m0, s59, 0\n"
"v_mfma_f32_16x16x32_bf16 v[170:173], v[58:61], v[114:117], v[170:173]\n"
"s_barrier\n"
"s_waitcnt lgkmcnt(5)\n"
"v_mfma_f32_16x16x32_bf16 v[174:177], v[62:65], v[110:113], v[174:177]\n"
"s_waitcnt lgkmcnt(4)\n"
"ds_read_b128 v[54:57], v6 offset:53248\n"
"v_add_u32 v1, 151552, v1\n"
"v_mfma_f32_16x16x32_bf16 v[178:181], v[62:65], v[114:117], v[178:181]\n"
"buffer_load_dwordx4 v1, s[20:23], m0 offen sc1 lds\n"
"v_mfma_f32_16x16x32_bf16 v[182:185], v[66:69], v[110:113], v[182:185]\n"
"s_waitcnt lgkmcnt(4)\n"
"ds_read_b128 v[58:61], v6 offset:55296\n"
"v_mfma_f32_16x16x32_bf16 v[186:189], v[66:69], v[114:117], v[186:189]\n"
"s_add_u32 m0, s59, 151552\n"
"v_add_u32 v1, 4294819840, v1\n"
"v_mfma_f32_16x16x32_bf16 v[150:153], v[70:73], v[118:121], v[150:153]\n"
"s_waitcnt lgkmcnt(4)\n"
"ds_read_b128 v[62:65], v6 offset:57344\n"
"v_mfma_f32_16x16x32_bf16 v[154:157], v[70:73], v[122:125], v[154:157]\n"
"buffer_load_dwordx4 v1, s[20:23], m0 offen sc1 lds\n"
"v_mfma_f32_16x16x32_bf16 v[158:161], v[74:77], v[118:121], v[158:161]\n"
"ds_read_b128 v[66:69], v6 offset:59392\n"
"s_mov_b32 s3, 1966080\n"
"s_waitcnt vmcnt(13)\n"
"v_mfma_f32_16x16x32_bf16 v[162:165], v[74:77], v[122:125], v[162:165]\n"
"s_waitcnt lgkmcnt(5)\n"
"s_add_u32 m0, s59, 4096\n"
"v_add_u32 v1, 151552, v1\n"
"v_mfma_f32_16x16x32_bf16 v[166:169], v[78:81], v[118:121], v[166:169]\n"
"s_waitcnt lgkmcnt(4)\n"
"ds_read_b128 v[70:73], v6 offset:61440\n"
"v_mfma_f32_16x16x32_bf16 v[170:173], v[78:81], v[122:125], v[170:173]\n"
"buffer_store_dwordx2 v[8:9], v14, s[20:23], s3 offen sc1\n"
"v_mfma_f32_16x16x32_bf16 v[174:177], v[82:85], v[118:121], v[174:177]\n"
"s_waitcnt lgkmcnt(4)\n"
"ds_read_b128 v[74:77], v6 offset:63488\n"
"v_mfma_f32_16x16x32_bf16 v[178:181], v[82:85], v[122:125], v[178:181]\n"
"buffer_load_dwordx4 v1, s[20:23], m0 offen sc1 lds\n"
"v_mfma_f32_16x16x32_bf16 v[182:185], v[54:57], v[118:121], v[182:185]\n"
"s_waitcnt lgkmcnt(4)\n"
"ds_read_b128 v[78:81], v7\n"
"s_add_u32 m0, s59, 8192\n"
"v_mfma_f32_16x16x32_bf16 v[186:189], v[54:57], v[122:125], v[186:189]\n"
"s_mov_b32 s63, 1835008\n"
"buffer_load_dwordx4 v1, s[20:23], m0 offen sc1 lds\n"
"v_mfma_f32_16x16x32_bf16 v[150:153], v[58:61], v[126:129], v[150:153]\n"
"s_waitcnt lgkmcnt(4)\n"
"ds_read_b128 v[82:85], v7 offset:2048\n"
"s_add_u32 m0, s59, 69632\n"
"v_mfma_f32_16x16x32_bf16 v[154:157], v[58:61], v[130:133], v[154:157]\n"
"buffer_load_dwordx2 v[254:255], v11, s[20:23], s63 offen sc1\n"
"v_mfma_f32_16x16x32_bf16 v[158:161], v[62:65], v[126:129], v[158:161]\n"
"s_waitcnt lgkmcnt(4)\n"
"ds_read_b128 v[54:57], v5 offset:12288\n"
"v_add_u32 v1, 4294909952, v1\n"
"v_mfma_f32_16x16x32_bf16 v[162:165], v[62:65], v[130:133], v[162:165]\n"
"buffer_load_dwordx4 v1, s[20:23], m0 offen sc1 lds\n"
"v_mfma_f32_16x16x32_bf16 v[166:169], v[66:69], v[126:129], v[166:169]\n"
"s_waitcnt lgkmcnt(4)\n"
"ds_read_b128 v[58:61], v5 offset:14336\n"
"s_add_u32 m0, s59, 73728\n"
"v_mfma_f32_16x16x32_bf16 v[170:173], v[66:69], v[130:133], v[170:173]\n"
"buffer_load_dwordx4 v1, s[20:23], m0 offen sc1 lds\n"
"v_mfma_f32_16x16x32_bf16 v[174:177], v[70:73], v[126:129], v[174:177]\n"
"s_waitcnt lgkmcnt(4)\n"
"ds_read_b128 v[62:65], v5 offset:16384\n"
"v_mfma_f32_16x16x32_bf16 v[178:181], v[70:73], v[130:133], v[178:181]\n"
"s_add_u32 m0, s59, 77824\n"
"v_mfma_f32_16x16x32_bf16 v[182:185], v[74:77], v[126:129], v[182:185]\n"
"s_waitcnt lgkmcnt(4)\n"
"ds_read_b128 v[66:69], v5 offset:18432\n"
"v_mfma_f32_16x16x32_bf16 v[186:189], v[74:77], v[130:133], v[186:189]\n"
"buffer_load_dwordx4 v1, s[20:23], m0 offen sc1 lds\n"
"v_mfma_f32_16x16x32_bf16 v[150:153], v[78:81], v[134:137], v[150:153]\n"
"s_waitcnt lgkmcnt(4)\n"
"ds_read_b128 v[70:73], v5 offset:20480\n"
"s_add_u32 m0, s59, 81920\n"
"v_mfma_f32_16x16x32_bf16 v[154:157], v[78:81], v[138:141], v[154:157]\n"
"buffer_load_dwordx4 v1, s[20:23], m0 offen sc1 lds\n"
"v_mfma_f32_16x16x32_bf16 v[158:161], v[82:85], v[134:137], v[158:161]\n"
"s_waitcnt lgkmcnt(4)\n"
"ds_read_b128 v[74:77], v5 offset:22528\n"
"v_mfma_f32_16x16x32_bf16 v[162:165], v[82:85], v[138:141], v[162:165]\n"
"s_add_u32 m0, s59, 86016\n"
"v_mfma_f32_16x16x32_bf16 v[166:169], v[54:57], v[134:137], v[166:169]\n"
"s_waitcnt lgkmcnt(4)\n"
"ds_read_b128 v[78:81], v5 offset:24576\n"
"v_mfma_f32_16x16x32_bf16 v[170:173], v[54:57], v[138:141], v[170:173]\n"
"buffer_load_dwordx4 v1, s[20:23], m0 offen sc1 lds\n"
"v_mfma_f32_16x16x32_bf16 v[174:177], v[58:61], v[134:137], v[174:177]\n"
"s_waitcnt lgkmcnt(4)\n"
"ds_read_b128 v[82:85], v5 offset:26624\n"
"v_mfma_f32_16x16x32_bf16 v[178:181], v[58:61], v[138:141], v[178:181]\n"
"s_add_u32 m0, s59, 90112\n"
"v_mfma_f32_16x16x32_bf16 v[182:185], v[62:65], v[134:137], v[182:185]\n"
"s_waitcnt lgkmcnt(4)\n"
"ds_read_b128 v[54:57], v5 offset:28672\n"
"v_mfma_f32_16x16x32_bf16 v[186:189], v[62:65], v[138:141], v[186:189]\n"
"buffer_load_dwordx4 v1, s[20:23], m0 offen sc1 lds\n"
"v_mfma_f32_16x16x32_bf16 v[150:153], v[66:69], v[142:145], v[150:153]\n"
"s_waitcnt lgkmcnt(4)\n"
"ds_read_b128 v[58:61], v5 offset:30720\n"
"v_mfma_f32_16x16x32_bf16 v[154:157], v[66:69], v[146:149], v[154:157]\n"
"s_add_u32 m0, s59, 94208\n"
"v_mfma_f32_16x16x32_bf16 v[158:161], v[70:73], v[142:145], v[158:161]\n"
"s_waitcnt lgkmcnt(4)\n"
"ds_read_b128 v[62:65], v5 offset:32768\n"
"v_mfma_f32_16x16x32_bf16 v[162:165], v[70:73], v[146:149], v[162:165]\n"
"buffer_load_dwordx4 v1, s[20:23], m0 offen sc1 lds\n"
"v_mfma_f32_16x16x32_bf16 v[166:169], v[74:77], v[142:145], v[166:169]\n"
"s_waitcnt lgkmcnt(4)\n"
"ds_read_b128 v[66:69], v5 offset:34816\n"
"v_mfma_f32_16x16x32_bf16 v[170:173], v[74:77], v[146:149], v[170:173]\n"
"s_add_u32 m0, s59, 98304\n"
"v_mfma_f32_16x16x32_bf16 v[174:177], v[78:81], v[142:145], v[174:177]\n"
"buffer_load_dwordx4 v1, s[20:23], m0 offen sc1 lds\n"
"v_mfma_f32_16x16x32_bf16 v[178:181], v[78:81], v[146:149], v[178:181]\n"
"s_waitcnt lgkmcnt(4)\n"
"s_add_u32 m0, s59, 102400\n"
"v_mfma_f32_16x16x32_bf16 v[182:185], v[82:85], v[142:145], v[182:185]\n"
"buffer_load_dwordx4 v1, s[20:23], m0 offen sc1 lds\n"
"v_mfma_f32_16x16x32_bf16 v[186:189], v[82:85], v[146:149], v[186:189]\n"
"s_waitcnt lgkmcnt(3)\n"
"v_mfma_f32_16x16x32_bf16 v[150:153], v[54:57], a[0:3], v[150:153]\n"
"v_mfma_f32_16x16x32_bf16 v[154:157], v[54:57], a[4:7], v[154:157]\n"
"s_waitcnt lgkmcnt(2)\n"
"v_mfma_f32_16x16x32_bf16 v[158:161], v[58:61], a[0:3], v[158:161]\n"
"v_mfma_f32_16x16x32_bf16 v[162:165], v[58:61], a[4:7], v[162:165]\n"
"s_waitcnt lgkmcnt(1)\n"
"v_mfma_f32_16x16x32_bf16 v[166:169], v[62:65], a[0:3], v[166:169]\n"
"v_mfma_f32_16x16x32_bf16 v[170:173], v[62:65], a[4:7], v[170:173]\n"
"s_waitcnt lgkmcnt(0)\n"
"v_mfma_f32_16x16x32_bf16 v[174:177], v[66:69], a[0:3], v[174:177]\n"
"v_mfma_f32_16x16x32_bf16 v[178:181], v[66:69], a[4:7], v[178:181]\n"
"v_mfma_f32_16x16x32_bf16 v[182:185], a[160:163], a[0:3], v[182:185]\n"
"v_mfma_f32_16x16x32_bf16 v[186:189], a[160:163], a[4:7], v[186:189]\n"
"s_waitcnt vmcnt(9)\n"
"v_cmp_ne_u64 s[18:19], v[254:255], v[8:9]\n"
"s_nop 0\n"
"s_and_b32 s18, s18, 65535\n"
"s_cbranch_scc0 L31_%=\n"
"s_mov_b32 s72, 0\n"
"L32_%=:\n"
"buffer_load_dwordx2 v[254:255], v11, s[20:23], s63 offen sc1\n"
"s_add_u32 s72, s72, 1\n"
"s_waitcnt vmcnt(0)\n"
"v_cmp_ne_u64 s[18:19], v[254:255], v[8:9]\n"
"s_nop 0\n"
"s_and_b32 s18, s18, 65535\n"
"s_cbranch_scc0 L31_%=\n"
"s_cmp_lt_u32 s72, 4194304\n"
"s_cbranch_scc1 L32_%=\n"
"L31_%=:\n"
"v_mfma_f32_16x16x32_bf16 v[150:153], a[164:167], a[8:11], v[150:153]\n"
"v_mfma_f32_16x16x32_bf16 v[154:157], a[164:167], a[12:15], v[154:157]\n"
"s_barrier\n"
"v_mfma_f32_16x16x32_bf16 v[158:161], a[168:171], a[8:11], v[158:161]\n"
"buffer_load_dwordx4 a[64:67], v32, s[96:99], s87 offen sc1\n"
"v_mfma_f32_16x16x32_bf16 v[162:165], a[168:171], a[12:15], v[162:165]\n"
"v_mfma_f32_16x16x32_bf16 v[166:169], a[172:175], a[8:11], v[166:169]\n"
"v_mfma_f32_16x16x32_bf16 v[170:173], a[172:175], a[12:15], v[170:173]\n"
"v_mfma_f32_16x16x32_bf16 v[174:177], a[176:179], a[8:11], v[174:177]\n"
"buffer_load_dwordx4 a[68:71], v33, s[96:99], s87 offen sc1\n"
"v_mfma_f32_16x16x32_bf16 v[178:181], a[176:179], a[12:15], v[178:181]\n"
"v_mfma_f32_16x16x32_bf16 v[182:185], a[180:183], a[8:11], v[182:185]\n"
"s_mov_b32 s63, 1966080\n"
"v_mfma_f32_16x16x32_bf16 v[186:189], a[180:183], a[12:15], v[186:189]\n"
"buffer_load_dwordx2 v[254:255], v11, s[20:23], s63 offen sc1\n"
"v_mfma_f32_16x16x32_bf16 v[150:153], a[184:187], a[16:19], v[150:153]\n"
"buffer_load_dwordx4 a[72:75], v32, s[96:99], s87 offen offset:2048 sc1\n"
"v_mfma_f32_16x16x32_bf16 v[154:157], a[184:187], a[20:23], v[154:157]\n"
"v_mfma_f32_16x16x32_bf16 v[158:161], a[188:191], a[16:19], v[158:161]\n"
"v_mfma_f32_16x16x32_bf16 v[162:165], a[188:191], a[20:23], v[162:165]\n"
"buffer_load_dwordx4 a[76:79], v33, s[96:99], s87 offen offset:2048 sc1\n"
"v_mfma_f32_16x16x32_bf16 v[166:169], a[192:195], a[16:19], v[166:169]\n"
"s_add_u32 m0, s59, 106496\n"
"v_mfma_f32_16x16x32_bf16 v[170:173], a[192:195], a[20:23], v[170:173]\n"
"v_mfma_f32_16x16x32_bf16 v[174:177], a[196:199], a[16:19], v[174:177]\n"
"buffer_load_dwordx4 v1, s[20:23], m0 offen sc1 lds\n"
"v_mfma_f32_16x16x32_bf16 v[178:181], a[196:199], a[20:23], v[178:181]\n"
"v_mfma_f32_16x16x32_bf16 v[182:185], a[200:203], a[16:19], v[182:185]\n"
"v_mfma_f32_16x16x32_bf16 v[186:189], a[200:203], a[20:23], v[186:189]\n"
"v_mfma_f32_16x16x32_bf16 v[150:153], a[204:207], a[24:27], v[150:153]\n"
"buffer_load_dwordx4 a[80:83], v32, s[96:99], s88 offen sc1\n"
"v_mfma_f32_16x16x32_bf16 v[154:157], a[204:207], a[28:31], v[154:157]\n"
"v_mfma_f32_16x16x32_bf16 v[158:161], a[208:211], a[24:27], v[158:161]\n"
"v_mfma_f32_16x16x32_bf16 v[162:165], a[208:211], a[28:31], v[162:165]\n"
"buffer_load_dwordx4 a[84:87], v33, s[96:99], s88 offen sc1\n"
"v_mfma_f32_16x16x32_bf16 v[166:169], a[212:215], a[24:27], v[166:169]\n"
"v_mfma_f32_16x16x32_bf16 v[170:173], a[212:215], a[28:31], v[170:173]\n"
"v_mfma_f32_16x16x32_bf16 v[174:177], a[216:219], a[24:27], v[174:177]\n"
"v_mfma_f32_16x16x32_bf16 v[178:181], a[216:219], a[28:31], v[178:181]\n"
"buffer_load_dwordx4 a[88:91], v32, s[96:99], s88 offen offset:2048 sc1\n"
"v_mfma_f32_16x16x32_bf16 v[182:185], a[220:223], a[24:27], v[182:185]\n"
"v_mfma_f32_16x16x32_bf16 v[186:189], a[220:223], a[28:31], v[186:189]\n"
"v_mfma_f32_16x16x32_bf16 v[150:153], a[224:227], a[32:35], v[150:153]\n"
"buffer_load_dwordx4 a[92:95], v33, s[96:99], s88 offen offset:2048 sc1\n"
"v_mfma_f32_16x16x32_bf16 v[154:157], a[224:227], a[36:39], v[154:157]\n"
"s_add_u32 m0, s59, 110592\n"
"v_mfma_f32_16x16x32_bf16 v[158:161], a[228:231], a[32:35], v[158:161]\n"
"v_mfma_f32_16x16x32_bf16 v[162:165], a[228:231], a[36:39], v[162:165]\n"
"v_mfma_f32_16x16x32_bf16 v[166:169], a[232:235], a[32:35], v[166:169]\n"
"ds_read_b128 v[70:73], v5 offset:36864\n"
"v_mfma_f32_16x16x32_bf16 v[170:173], a[232:235], a[36:39], v[170:173]\n"
"buffer_load_dwordx4 v1, s[20:23], m0 offen sc1 lds\n"
"v_mfma_f32_16x16x32_bf16 v[174:177], a[236:239], a[32:35], v[174:177]\n"
"ds_read_b128 v[74:77], v5 offset:38912\n"
"v_mfma_f32_16x16x32_bf16 v[178:181], a[236:239], a[36:39], v[178:181]\n"
"buffer_load_dwordx4 a[96:99], v32, s[96:99], s89 offen sc1\n"
"v_mfma_f32_16x16x32_bf16 v[182:185], a[240:243], a[32:35], v[182:185]\n"
"ds_read_b128 v[78:81], v5 offset:40960\n"
"v_mfma_f32_16x16x32_bf16 v[186:189], a[240:243], a[36:39], v[186:189]\n"
"v_mfma_f32_16x16x32_bf16 v[150:153], a[244:247], a[40:43], v[150:153]\n"
"ds_read_b128 v[82:85], v5 offset:43008\n"
"v_mfma_f32_16x16x32_bf16 v[154:157], a[244:247], a[44:47], v[154:157]\n"
"buffer_load_dwordx4 a[100:103], v33, s[96:99], s89 offen sc1\n"
"v_mfma_f32_16x16x32_bf16 v[158:161], a[248:251], a[40:43], v[158:161]\n"
"ds_read_b128 v[54:57], v5 offset:45056\n"
"v_mfma_f32_16x16x32_bf16 v[162:165], a[248:251], a[44:47], v[162:165]\n"
"v_mfma_f32_16x16x32_bf16 v[166:169], a[252:255], a[40:43], v[166:169]\n"
"s_waitcnt lgkmcnt(4)\n"
"ds_read_b128 v[58:61], v5 offset:47104\n"
"v_mfma_f32_16x16x32_bf16 v[170:173], a[252:255], a[44:47], v[170:173]\n"
"buffer_load_dwordx4 a[104:107], v32, s[96:99], s89 offen offset:2048 sc1\n"
"v_mfma_f32_16x16x32_bf16 v[174:177], v[70:73], a[40:43], v[174:177]\n"
"s_waitcnt lgkmcnt(4)\n"
"ds_read_b128 v[62:65], v5 offset:49152\n"
"v_mfma_f32_16x16x32_bf16 v[178:181], v[70:73], a[44:47], v[178:181]\n"
"v_mfma_f32_16x16x32_bf16 v[182:185], v[74:77], a[40:43], v[182:185]\n"
"s_waitcnt lgkmcnt(4)\n"
"ds_read_b128 v[66:69], v5 offset:51200\n"
"v_mfma_f32_16x16x32_bf16 v[186:189], v[74:77], a[44:47], v[186:189]\n"
"buffer_load_dwordx4 a[108:111], v33, s[96:99], s89 offen offset:2048 sc1\n"
"v_mfma_f32_16x16x32_bf16 v[150:153], v[78:81], a[48:51], v[150:153]\n"
"s_waitcnt lgkmcnt(4)\n"
"ds_read_b128 v[70:73], v5 offset:53248\n"
"v_mfma_f32_16x16x32_bf16 v[154:157], v[78:81], a[52:55], v[154:157]\n"
"s_add_u32 m0, s59, 114688\n"
"v_mfma_f32_16x16x32_bf16 v[158:161], v[82:85], a[48:51], v[158:161]\n"
"s_waitcnt lgkmcnt(4)\n"
"ds_read_b128 v[74:77], v5 offset:55296\n"
"v_mfma_f32_16x16x32_bf16 v[162:165], v[82:85], a[52:55], v[162:165]\n"
"buffer_load_dwordx4 v1, s[20:23], m0 offen sc1 lds\n"
"v_mfma_f32_16x16x32_bf16 v[166:169], v[54:57], a[48:51], v[166:169]\n"
"s_waitcnt lgkmcnt(4)\n"
"ds_read_b128 v[78:81], v5 offset:57344\n"
"v_mfma_f32_16x16x32_bf16 v[170:173], v[54:57], a[52:55], v[170:173]\n"
"buffer_load_dwordx4 a[112:115], v32, s[96:99], s90 offen sc1\n"
"v_mfma_f32_16x16x32_bf16 v[174:177], v[58:61], a[48:51], v[174:177]\n"
"s_waitcnt lgkmcnt(4)\n"
"ds_read_b128 v[82:85], v5 offset:59392\n"
"v_mfma_f32_16x16x32_bf16 v[178:181], v[58:61], a[52:55], v[178:181]\n"
"v_mfma_f32_16x16x32_bf16 v[182:185], v[62:65], a[48:51], v[182:185]\n"
"s_waitcnt lgkmcnt(4)\n"
"ds_read_b128 v[54:57], v5 offset:61440\n"
"v_mfma_f32_16x16x32_bf16 v[186:189], v[62:65], a[52:55], v[186:189]\n"
"buffer_load_dwordx4 a[116:119], v33, s[96:99], s90 offen sc1\n"
"v_mfma_f32_16x16x32_bf16 v[150:153], v[66:69], a[56:59], v[150:153]\n"
"s_waitcnt lgkmcnt(4)\n"
"ds_read_b128 v[58:61], v5 offset:63488\n"
"v_mfma_f32_16x16x32_bf16 v[154:157], v[66:69], a[60:63], v[154:157]\n"
"v_mfma_f32_16x16x32_bf16 v[158:161], v[70:73], a[56:59], v[158:161]\n"
"s_waitcnt lgkmcnt(4)\n"
"ds_read_b128 v[62:65], v6\n"
"v_mfma_f32_16x16x32_bf16 v[162:165], v[70:73], a[60:63], v[162:165]\n"
"buffer_load_dwordx4 a[120:123], v32, s[96:99], s90 offen offset:2048 sc1\n"
"v_mfma_f32_16x16x32_bf16 v[166:169], v[74:77], a[56:59], v[166:169]\n"
"ds_read_b128 v[66:69], v6 offset:2048\n"
"v_mfma_f32_16x16x32_bf16 v[170:173], v[74:77], a[60:63], v[170:173]\n"
"s_waitcnt vmcnt(27)\n"
"s_barrier\n"
"s_waitcnt lgkmcnt(5)\n"
"v_mfma_f32_16x16x32_bf16 v[174:177], v[78:81], a[56:59], v[174:177]\n"
"s_waitcnt lgkmcnt(4)\n"
"ds_read_b128 v[70:73], v7 offset:4096\n"
"v_mfma_f32_16x16x32_bf16 v[178:181], v[78:81], a[60:63], v[178:181]\n"
"buffer_load_dwordx4 a[124:127], v33, s[96:99], s90 offen offset:2048 sc1\n"
"v_mfma_f32_16x16x32_bf16 v[182:185], v[82:85], a[56:59], v[182:185]\n"
"ds_read_b128 v[74:77], v7 offset:6144\n"
"s_add_u32 m0, s59, 118784\n"
"v_mfma_f32_16x16x32_bf16 v[186:189], v[82:85], a[60:63], v[186:189]\n"
"s_waitcnt vmcnt(17)\n"
"v_cmp_ne_u64 s[18:19], v[254:255], v[8:9]\n"
"s_nop 0\n"
"s_and_b32 s18, s18, 65535\n"
"s_cbranch_scc0 L33_%=\n"
"s_mov_b32 s72, 0\n"
"L34_%=:\n"
"buffer_load_dwordx2 v[254:255], v11, s[20:23], s63 offen sc1\n"
"s_add_u32 s72, s72, 1\n"
"s_waitcnt vmcnt(0)\n"
"v_cmp_ne_u64 s[18:19], v[254:255], v[8:9]\n"
"s_nop 0\n"
"s_and_b32 s18, s18, 65535\n"
"s_cbranch_scc0 L33_%=\n"
"s_cmp_lt_u32 s72, 4194304\n"
"s_cbranch_scc1 L34_%=\n"
"L33_%=:\n"
"s_waitcnt lgkmcnt(5)\n"
"v_mfma_f32_16x16x32_bf16 v[150:153], v[54:57], a[64:67], v[150:153]\n"
"s_waitcnt lgkmcnt(4)\n"
"ds_read_b128 v[78:81], v7 offset:8192\n"
"v_mfma_f32_16x16x32_bf16 v[154:157], v[54:57], a[68:71], v[154:157]\n"
"buffer_load_dwordx4 v1, s[20:23], m0 offen sc1 lds\n"
"v_mfma_f32_16x16x32_bf16 v[158:161], v[58:61], a[64:67], v[158:161]\n"
"s_waitcnt lgkmcnt(4)\n"
"ds_read_b128 v[82:85], v7 offset:10240\n"
"v_mfma_f32_16x16x32_bf16 v[162:165], v[58:61], a[68:71], v[162:165]\n"
"buffer_load_dwordx4 a[128:131], v32, s[96:99], s91 offen sc1\n"
"v_mfma_f32_16x16x32_bf16 v[166:169], v[62:65], a[64:67], v[166:169]\n"
"s_waitcnt lgkmcnt(4)\n"
"ds_read_b128 v[54:57], v7 offset:12288\n"
"v_mfma_f32_16x16x32_bf16 v[170:173], v[62:65], a[68:71], v[170:173]\n"
"v_mfma_f32_16x16x32_bf16 v[174:177], v[66:69], a[64:67], v[174:177]\n"
"s_waitcnt lgkmcnt(4)\n"
"ds_read_b128 v[58:61], v7 offset:14336\n"
"v_mfma_f32_16x16x32_bf16 v[178:181], v[66:69], a[68:71], v[178:181]\n"
"buffer_load_dwordx4 a[132:135], v33, s[96:99], s91 offen sc1\n"
"v_mfma_f32_16x16x32_bf16 v[182:185], v[70:73], a[64:67], v[182:185]\n"
"s_waitcnt vmcnt(19) lgkmcnt(4)\n"
"ds_read_b128 v[62:65], v7 offset:16384\n"
"v_mfma_f32_16x16x32_bf16 v[186:189], v[70:73], a[68:71], v[186:189]\n"
"v_mfma_f32_16x16x32_bf16 v[150:153], v[74:77], a[72:75], v[150:153]\n"
"s_waitcnt vmcnt(18) lgkmcnt(4)\n"
"ds_read_b128 v[66:69], v7 offset:18432\n"
"v_mfma_f32_16x16x32_bf16 v[154:157], v[74:77], a[76:79], v[154:157]\n"
"v_mfma_f32_16x16x32_bf16 v[158:161], v[78:81], a[72:75], v[158:161]\n"
"s_waitcnt lgkmcnt(4)\n"
"ds_read_b128 v[70:73], v5\n"
"v_mfma_f32_16x16x32_bf16 v[162:165], v[78:81], a[76:79], v[162:165]\n"
"buffer_load_dwordx4 a[136:139], v32, s[96:99], s91 offen offset:2048 sc1\n"
"v_mfma_f32_16x16x32_bf16 v[166:169], v[82:85], a[72:75], v[166:169]\n"
"s_waitcnt lgkmcnt(4)\n"
"ds_read_b128 v[74:77], v5 offset:2048\n"
"v_mfma_f32_16x16x32_bf16 v[170:173], v[82:85], a[76:79], v[170:173]\n"
"v_mfma_f32_16x16x32_bf16 v[174:177], v[54:57], a[72:75], v[174:177]\n"
"s_waitcnt lgkmcnt(4)\n"
"ds_read_b128 v[78:81], v7 offset:20480\n"
"v_mfma_f32_16x16x32_bf16 v[178:181], v[54:57], a[76:79], v[178:181]\n"
"buffer_load_dwordx4 a[140:143], v33, s[96:99], s91 offen offset:2048 sc1\n"
"v_mfma_f32_16x16x32_bf16 v[182:185], v[58:61], a[72:75], v[182:185]\n"
"s_waitcnt vmcnt(18) lgkmcnt(4)\n"
"ds_read_b128 v[82:85], v7 offset:22528\n"
"v_mfma_f32_16x16x32_bf16 v[186:189], v[58:61], a[76:79], v[186:189]\n"
"v_mfma_f32_16x16x32_bf16 v[150:153], v[62:65], a[80:83], v[150:153]\n"
"s_waitcnt vmcnt(17) lgkmcnt(4)\n"
"ds_read_b128 v[54:57], v5 offset:4096\n"
"v_mfma_f32_16x16x32_bf16 v[154:157], v[62:65], a[84:87], v[154:157]\n"
"buffer_load_dwordx4 a[144:147], v32, s[96:99], s92 offen sc1\n"
"v_mfma_f32_16x16x32_bf16 v[158:161], v[66:69], a[80:83], v[158:161]\n"
"s_waitcnt lgkmcnt(4)\n"
"ds_read_b128 v[58:61], v5 offset:6144\n"
"v_mfma_f32_16x16x32_bf16 v[162:165], v[66:69], a[84:87], v[162:165]\n"
"v_mfma_f32_16x16x32_bf16 v[182:185], v[70:73], a[80:83], v[182:185]\n"
"s_waitcnt vmcnt(17) lgkmcnt(4)\n"
"ds_read_b128 v[62:65], v5 offset:8192\n"
"v_mfma_f32_16x16x32_bf16 v[186:189], v[70:73], a[84:87], v[186:189]\n"
"v_mfma_f32_16x16x32_bf16 v[150:153], v[74:77], a[88:91], v[150:153]\n"
"s_waitcnt vmcnt(16) lgkmcnt(4)\n"
"ds_read_b128 v[66:69], v5 offset:10240\n"
"v_mfma_f32_16x16x32_bf16 v[154:157], v[74:77], a[92:95], v[154:157]\n"
"buffer_load_dwordx4 a[148:151], v33, s[96:99], s92 offen sc1\n"
"v_mfma_f32_16x16x32_bf16 v[158:161], v[78:81], a[88:91], v[158:161]\n"
"s_waitcnt lgkmcnt(4)\n"
"ds_read_b128 v[70:73], v6 offset:4096\n"
"v_mfma_f32_16x16x32_bf16 v[162:165], v[78:81], a[92:95], v[162:165]\n"
"s_add_u32 m0, s59, 122880\n"
"v_mfma_f32_16x16x32_bf16 v[182:185], v[82:85], a[88:91], v[182:185]\n"
"ds_read_b128 v[74:77], v6 offset:6144\n"
"v_mfma_f32_16x16x32_bf16 v[186:189], v[82:85], a[92:95], v[186:189]\n"
"s_barrier\n"
"s_waitcnt vmcnt(15) lgkmcnt(5)\n"
"v_mfma_f32_16x16x32_bf16 v[150:153], v[54:57], a[96:99], v[150:153]\n"
"s_waitcnt vmcnt(14) lgkmcnt(4)\n"
"ds_read_b128 v[78:81], v6 offset:8192\n"
"v_mfma_f32_16x16x32_bf16 v[154:157], v[54:57], a[100:103], v[154:157]\n"
"buffer_load_dwordx4 v1, s[20:23], m0 offen sc1 lds\n"
"v_mfma_f32_16x16x32_bf16 v[158:161], v[58:61], a[96:99], v[158:161]\n"
"s_waitcnt lgkmcnt(4)\n"
"ds_read_b128 v[82:85], v6 offset:10240\n"
"v_mfma_f32_16x16x32_bf16 v[162:165], v[58:61], a[100:103], v[162:165]\n"
"s_add_u32 m0, s59, 126976\n"
"v_mfma_f32_16x16x32_bf16 v[182:185], v[62:65], a[96:99], v[182:185]\n"
"s_waitcnt vmcnt(14) lgkmcnt(4)\n"
"ds_read_b128 v[54:57], v6 offset:12288\n"
"v_mfma_f32_16x16x32_bf16 v[186:189], v[62:65], a[100:103], v[186:189]\n"
"buffer_load_dwordx4 v1, s[20:23], m0 offen sc1 lds\n"
"v_mfma_f32_16x16x32_bf16 v[150:153], v[66:69], a[104:107], v[150:153]\n"
"s_waitcnt vmcnt(14) lgkmcnt(4)\n"
"ds_read_b128 v[58:61], v6 offset:14336\n"
"v_mfma_f32_16x16x32_bf16 v[154:157], v[66:69], a[108:111], v[154:157]\n"
"s_add_u32 m0, s59, 131072\n"
"v_mfma_f32_16x16x32_bf16 v[158:161], v[70:73], a[104:107], v[158:161]\n"
"s_waitcnt lgkmcnt(4)\n"
"ds_read_b128 v[62:65], v6 offset:16384\n"
"v_mfma_f32_16x16x32_bf16 v[162:165], v[70:73], a[108:111], v[162:165]\n"
"buffer_load_dwordx4 v1, s[20:23], m0 offen sc1 lds\n"
"v_mfma_f32_16x16x32_bf16 v[182:185], v[74:77], a[104:107], v[182:185]\n"
"s_waitcnt vmcnt(13) lgkmcnt(4)\n"
"ds_read_b128 v[66:69], v6 offset:18432\n"
"v_mfma_f32_16x16x32_bf16 v[186:189], v[74:77], a[108:111], v[186:189]\n"
"s_add_u32 m0, s59, 0\n"
"v_add_u32 v1, 135168, v1\n"
"v_mfma_f32_16x16x32_bf16 v[150:153], v[78:81], a[112:115], v[150:153]\n"
"s_waitcnt vmcnt(12) lgkmcnt(4)\n"
"ds_read_b128 v[70:73], v6 offset:20480\n"
"v_mfma_f32_16x16x32_bf16 v[154:157], v[78:81], a[116:119], v[154:157]\n"
"buffer_load_dwordx4 v1, s[20:23], m0 offen sc1 lds\n"
"v_mfma_f32_16x16x32_bf16 v[158:161], v[82:85], a[112:115], v[158:161]\n"
"s_waitcnt lgkmcnt(4)\n"
"ds_read_b128 v[74:77], v6 offset:22528\n"
"v_mfma_f32_16x16x32_bf16 v[162:165], v[82:85], a[116:119], v[162:165]\n"
"s_add_u32 m0, s59, 12288\n"
"v_add_u32 v1, 4294959104, v1\n"
"v_mfma_f32_16x16x32_bf16 v[182:185], v[54:57], a[112:115], v[182:185]\n"
"s_waitcnt vmcnt(12) lgkmcnt(4)\n"
"ds_read_b128 v[78:81], v6 offset:24576\n"
"v_mfma_f32_16x16x32_bf16 v[186:189], v[54:57], a[116:119], v[186:189]\n"
"buffer_load_dwordx4 v1, s[20:23], m0 offen sc1 lds\n"
"v_mfma_f32_16x16x32_bf16 v[150:153], v[58:61], a[120:123], v[150:153]\n"
"s_waitcnt vmcnt(12) lgkmcnt(4)\n"
"ds_read_b128 v[82:85], v6 offset:26624\n"
"v_mfma_f32_16x16x32_bf16 v[154:157], v[58:61], a[124:127], v[154:157]\n"
"v_mfma_f32_16x16x32_bf16 v[158:161], v[62:65], a[120:123], v[158:161]\n"
"s_waitcnt lgkmcnt(4)\n"
"ds_read_b128 v[54:57], v6 offset:28672\n"
"v_mfma_f32_16x16x32_bf16 v[162:165], v[62:65], a[124:127], v[162:165]\n"
"v_mfma_f32_16x16x32_bf16 v[182:185], v[66:69], a[120:123], v[182:185]\n"
"s_waitcnt vmcnt(10) lgkmcnt(4)\n"
"ds_read_b128 v[58:61], v6 offset:30720\n"
"v_mfma_f32_16x16x32_bf16 v[186:189], v[66:69], a[124:127], v[186:189]\n"
"v_mfma_f32_16x16x32_bf16 v[150:153], v[70:73], a[128:131], v[150:153]\n"
"s_waitcnt vmcnt(9) lgkmcnt(4)\n"
"ds_read_b128 v[62:65], v6 offset:32768\n"
"v_mfma_f32_16x16x32_bf16 v[154:157], v[70:73], a[132:135], v[154:157]\n"
"v_mfma_f32_16x16x32_bf16 v[158:161], v[74:77], a[128:131], v[158:161]\n"
"s_waitcnt lgkmcnt(4)\n"
"ds_read_b128 v[66:69], v6 offset:34816\n"
"v_mfma_f32_16x16x32_bf16 v[162:165], v[74:77], a[132:135], v[162:165]\n"
"v_mfma_f32_16x16x32_bf16 v[182:185], v[78:81], a[128:131], v[182:185]\n"
"ds_read_b128 v[70:73], v6 offset:36864\n"
"v_mfma_f32_16x16x32_bf16 v[186:189], v[78:81], a[132:135], v[186:189]\n"
"s_waitcnt vmcnt(4)\n"
"s_barrier\n"
"s_waitcnt lgkmcnt(5)\n"
"v_mfma_f32_16x16x32_bf16 v[150:153], v[82:85], a[136:139], v[150:153]\n"
"s_waitcnt lgkmcnt(4)\n"
"ds_read_b128 v[74:77], v6 offset:38912\n"
"v_mfma_f32_16x16x32_bf16 v[154:157], v[82:85], a[140:143], v[154:157]\n"
"v_mfma_f32_16x16x32_bf16 v[158:161], v[54:57], a[136:139], v[158:161]\n"
"s_waitcnt lgkmcnt(4)\n"
"ds_read_b128 v[78:81], v6 offset:40960\n"
"v_mfma_f32_16x16x32_bf16 v[162:165], v[54:57], a[140:143], v[162:165]\n"
"v_mfma_f32_16x16x32_bf16 v[182:185], v[58:61], a[136:139], v[182:185]\n"
"s_waitcnt lgkmcnt(4)\n"
"ds_read_b128 v[82:85], v6 offset:43008\n"
"v_mfma_f32_16x16x32_bf16 v[186:189], v[58:61], a[140:143], v[186:189]\n"
"v_mfma_f32_16x16x32_bf16 v[150:153], v[62:65], a[144:147], v[150:153]\n"
"s_waitcnt lgkmcnt(4)\n"
"ds_read_b128 v[54:57], v6 offset:45056\n"
"v_mfma_f32_16x16x32_bf16 v[154:157], v[62:65], a[148:151], v[154:157]\n"
"v_mfma_f32_16x16x32_bf16 v[158:161], v[66:69], a[144:147], v[158:161]\n"
"s_waitcnt lgkmcnt(4)\n"
"ds_read_b128 v[58:61], v6 offset:47104\n"
"v_mfma_f32_16x16x32_bf16 v[162:165], v[66:69], a[148:151], v[162:165]\n"
"v_mfma_f32_16x16x32_bf16 v[182:185], v[70:73], a[144:147], v[182:185]\n"
"s_waitcnt lgkmcnt(4)\n"
"ds_read_b128 v[62:65], v6 offset:49152\n"
"v_mfma_f32_16x16x32_bf16 v[186:189], v[70:73], a[148:151], v[186:189]\n"
"v_mfma_f32_16x16x32_bf16 v[166:169], v[74:77], a[80:83], v[166:169]\n"
"s_waitcnt lgkmcnt(4)\n"
"ds_read_b128 v[66:69], v6 offset:51200\n"
"v_mfma_f32_16x16x32_bf16 v[170:173], v[74:77], a[84:87], v[170:173]\n"
"v_mfma_f32_16x16x32_bf16 v[174:177], v[78:81], a[80:83], v[174:177]\n"
"s_waitcnt lgkmcnt(4)\n"
"ds_read_b128 v[70:73], v6 offset:53248\n"
"v_mfma_f32_16x16x32_bf16 v[178:181], v[78:81], a[84:87], v[178:181]\n"
"ds_write_b128 v43, v[154:157]\n"
"v_mfma_f32_16x16x32_bf16 v[166:169], v[82:85], a[88:91], v[166:169]\n"
"s_waitcnt lgkmcnt(5)\n"
"ds_read_b128 v[74:77], v6 offset:55296\n"
"v_mfma_f32_16x16x32_bf16 v[170:173], v[82:85], a[92:95], v[170:173]\n"
"ds_write_b128 v43, v[162:165] offset:1024\n"
"v_mfma_f32_16x16x32_bf16 v[174:177], v[54:57], a[88:91], v[174:177]\n"
"ds_read_b128 v[78:81], v6 offset:57344\n"
"ds_write_b128 v43, v[186:189] offset:4096\n"
"v_mfma_f32_16x16x32_bf16 v[178:181], v[54:57], a[92:95], v[178:181]\n"
"s_waitcnt vmcnt(0) lgkmcnt(0)\n"
"s_barrier\n"
"v_mfma_f32_16x16x32_bf16 v[166:169], v[58:61], a[96:99], v[166:169]\n"
"ds_read_b128 v[82:85], v6 offset:59392\n"
"ds_read_b128 v[86:89], v44\n"
"v_mfma_f32_16x16x32_bf16 v[170:173], v[58:61], a[100:103], v[170:173]\n"
"ds_read_b128 v[90:93], v44 offset:1024\n"
"ds_read_b128 v[102:105], v44 offset:4096\n"
"v_mfma_f32_16x16x32_bf16 v[174:177], v[62:65], a[96:99], v[174:177]\n"
"ds_read_b128 v[54:57], v6 offset:61440\n"
"v_mfma_f32_16x16x32_bf16 v[178:181], v[62:65], a[100:103], v[178:181]\n"
"v_mfma_f32_16x16x32_bf16 v[166:169], v[66:69], a[104:107], v[166:169]\n"
"ds_read_b128 v[58:61], v6 offset:63488\n"
"v_mfma_f32_16x16x32_bf16 v[170:173], v[66:69], a[108:111], v[170:173]\n"
"v_mfma_f32_16x16x32_bf16 v[174:177], v[70:73], a[104:107], v[174:177]\n"
"s_waitcnt lgkmcnt(4)\n"
"ds_read_b128 v[62:65], v7\n"
"v_add_f32 v198, v150, v86\n"
"v_mfma_f32_16x16x32_bf16 v[178:181], v[70:73], a[108:111], v[178:181]\n"
"v_add_f32 v199, v151, v87\n"
"v_add_f32 v200, v152, v88\n"
"v_mfma_f32_16x16x32_bf16 v[166:169], v[74:77], a[112:115], v[166:169]\n"
"ds_read_b128 v[66:69], v7 offset:2048\n"
"v_add_f32 v201, v153, v89\n"
"v_mfma_f32_16x16x32_bf16 v[170:173], v[74:77], a[116:119], v[170:173]\n"
"v_cvt_pk_bf16_f32 v218, v198, v199\n"
"v_cvt_pk_bf16_f32 v219, v200, v201\n"
"v_mfma_f32_16x16x32_bf16 v[174:177], v[78:81], a[112:115], v[174:177]\n"
"s_waitcnt lgkmcnt(5)\n"
"ds_read_b128 v[70:73], v5\n"
"v_add_f32 v202, v158, v90\n"
"v_mfma_f32_16x16x32_bf16 v[178:181], v[78:81], a[116:119], v[178:181]\n"
"v_add_f32 v203, v159, v91\n"
"v_add_f32 v204, v160, v92\n"
"v_mfma_f32_16x16x32_bf16 v[166:169], v[82:85], a[120:123], v[166:169]\n"
"s_waitcnt lgkmcnt(4)\n"
"ds_read_b128 v[74:77], v5 offset:2048\n"
"v_add_f32 v205, v161, v93\n"
"v_mfma_f32_16x16x32_bf16 v[170:173], v[82:85], a[124:127], v[170:173]\n"
"v_cvt_pk_bf16_f32 v220, v202, v203\n"
"v_cvt_pk_bf16_f32 v221, v204, v205\n"
"v_mfma_f32_16x16x32_bf16 v[174:177], v[54:57], a[120:123], v[174:177]\n"
"s_waitcnt lgkmcnt(4)\n"
"ds_read_b128 v[78:81], v5 offset:12288\n"
"v_add_f32 v214, v182, v102\n"
"v_mfma_f32_16x16x32_bf16 v[178:181], v[54:57], a[124:127], v[178:181]\n"
"buffer_store_dwordx4 v[218:221], v51, s[52:55], 0 offen\n"
"v_mfma_f32_16x16x32_bf16 v[166:169], v[58:61], a[128:131], v[166:169]\n"
"v_add_f32 v215, v183, v103\n"
"v_add_f32 v216, v184, v104\n"
"v_mfma_f32_16x16x32_bf16 v[170:173], v[58:61], a[132:135], v[170:173]\n"
"s_waitcnt lgkmcnt(4)\n"
"v_add_f32 v217, v185, v105\n"
"v_cvt_pk_bf16_f32 v226, v214, v215\n"
"v_mfma_f32_16x16x32_bf16 v[174:177], v[62:65], a[128:131], v[174:177]\n"
"v_cvt_pk_bf16_f32 v227, v216, v217\n"
"v_mfma_f32_16x16x32_bf16 v[178:181], v[62:65], a[132:135], v[178:181]\n"
"s_waitcnt lgkmcnt(3)\n"
"buffer_store_dwordx2 v[226:227], v52, s[52:55], 0 offen\n"
"v_mfma_f32_16x16x32_bf16 v[166:169], v[66:69], a[136:139], v[166:169]\n"
"v_mfma_f32_16x16x32_bf16 v[170:173], v[66:69], a[140:143], v[170:173]\n"
"s_waitcnt lgkmcnt(2)\n"
"v_mfma_f32_16x16x32_bf16 v[174:177], v[70:73], a[136:139], v[174:177]\n"
"v_mfma_f32_16x16x32_bf16 v[178:181], v[70:73], a[140:143], v[178:181]\n"
"s_waitcnt lgkmcnt(1)\n"
"v_mfma_f32_16x16x32_bf16 v[166:169], v[74:77], a[144:147], v[166:169]\n"
"v_mfma_f32_16x16x32_bf16 v[170:173], v[74:77], a[148:151], v[170:173]\n"
"s_waitcnt lgkmcnt(0)\n"
"v_mfma_f32_16x16x32_bf16 v[174:177], v[78:81], a[144:147], v[174:177]\n"
"v_mfma_f32_16x16x32_bf16 v[178:181], v[78:81], a[148:151], v[178:181]\n"
"s_nop 4\n"
"ds_write_b128 v43, v[170:173] offset:2048\n"
"s_nop 1\n"
"ds_write_b128 v43, v[178:181] offset:3072\n"
"s_waitcnt lgkmcnt(0)\n"
"s_barrier\n"
"ds_read_b128 v[94:97], v44 offset:2048\n"
"ds_read_b128 v[98:101], v44 offset:3072\n"
"s_waitcnt lgkmcnt(1)\n"
"v_pk_add_f32 v[206:207], v[166:167], v[94:95]\n"
"v_pk_add_f32 v[208:209], v[168:169], v[96:97]\n"
"v_cvt_pk_bf16_f32 v222, v206, v207\n"
"v_cvt_pk_bf16_f32 v223, v208, v209\n"
"s_waitcnt lgkmcnt(0)\n"
"v_pk_add_f32 v[210:211], v[174:175], v[98:99]\n"
"v_pk_add_f32 v[212:213], v[176:177], v[100:101]\n"
"v_cvt_pk_bf16_f32 v224, v210, v211\n"
"v_cvt_pk_bf16_f32 v225, v212, v213\n"
"buffer_store_dwordx4 v[222:225], v51, s[52:55], 0 offen offset:64\n"
"; end\n"
"v_mov_b32 v238, 2097152\n"
"s_or_b32 s73, s56, s57\n"
"s_or_b32 s73, s73, s58\n"
"s_cmp_lg_u32 s73, 0\n"
"s_cbranch_scc1 L35_%=\n"
"s_mov_b64 exec, 1\n"
"buffer_store_dwordx2 v[8:9], v238, s[20:23], 0 offen sc1\n"
"s_mov_b64 exec, -1\n"
"L35_%=:\n"
"Lend_%=:\n"
  : "+v"(tid)
  : [ka] "s"(__builtin_amdgcn_kernarg_segment_ptr()), [b] "s"(blockIdx.x)
  : "v1", "v2", "v3", "v4", "v5", "v6", "v7", "v8", "v9", "v10", "v11", "v12", "v13", "v14", "v15", "v16", "v17", "v18", "v19", "v20", "v21", "v22", "v23", "v24", "v25", "v26", "v27", "v28", "v29", "v30", "v31", "v32", "v33", "v34", "v35", "v36", "v37", "v38", "v39", "v40", "v41", "v42", "v43", "v44", "v45", "v46", "v47", "v48", "v49", "v50", "v51", "v52", "v53", "v54", "v55", "v56", "v57", "v58", "v59", "v60", "v61", "v62", "v63", "v64", "v65", "v66", "v67", "v68", "v69", "v70", "v71", "v72", "v73", "v74", "v75", "v76", "v77", "v78", "v79", "v80", "v81", "v82", "v83", "v84", "v85", "v86", "v87", "v88", "v89", "v90", "v91", "v92", "v93", "v94", "v95", "v96", "v97", "v98", "v99", "v100", "v101", "v102", "v103", "v104", "v105", "v106", "v107", "v108", "v109", "v110", "v111", "v112", "v113", "v114", "v115", "v116", "v117", "v118", "v119", "v120", "v121", "v122", "v123", "v124", "v125", "v126", "v127", "v128", "v129", "v130", "v131", "v132", "v133", "v134", "v135", "v136", "v137", "v138", "v139", "v140", "v141", "v142", "v143", "v144", "v145", "v146", "v147", "v148", "v149", "v150", "v151", "v152", "v153", "v154", "v155", "v156", "v157", "v158", "v159", "v160", "v161", "v162", "v163", "v164", "v165", "v166", "v167", "v168", "v169", "v170", "v171", "v172", "v173", "v174", "v175", "v176", "v177", "v178", "v179", "v180", "v181", "v182", "v183", "v184", "v185", "v186", "v187", "v188", "v189", "v190", "v191", "v192", "v193", "v194", "v195", "v196", "v197", "v198", "v199", "v200", "v201", "v202", "v203", "v204", "v205", "v206", "v207", "v208", "v209", "v210", "v211", "v212", "v213", "v214", "v215", "v216", "v217", "v218", "v219", "v220", "v221", "v222", "v223", "v224", "v225", "v226", "v227", "v228", "v229", "v230", "v231", "v232", "v233", "v234", "v235", "v236", "v237", "v238", "v239", "v240", "v241", "v242", "v243", "v244", "v245", "v246", "v247", "v248", "v249", "v250", "v251", "v252", "v253", "v254", "v255", "a0", "a1", "a2", "a3", "a4", "a5", "a6", "a7", "a8", "a9", "a10", "a11", "a12", "a13", "a14", "a15", "a16", "a17", "a18", "a19", "a20", "a21", "a22", "a23", "a24", "a25", "a26", "a27", "a28", "a29", "a30", "a31", "a32", "a33", "a34", "a35", "a36", "a37", "a38", "a39", "a40", "a41", "a42", "a43", "a44", "a45", "a46", "a47", "a48", "a49", "a50", "a51", "a52", "a53", "a54", "a55", "a56", "a57", "a58", "a59", "a60", "a61", "a62", "a63", "a64", "a65", "a66", "a67", "a68", "a69", "a70", "a71", "a72", "a73", "a74", "a75", "a76", "a77", "a78", "a79", "a80", "a81", "a82", "a83", "a84", "a85", "a86", "a87", "a88", "a89", "a90", "a91", "a92", "a93", "a94", "a95", "a96", "a97", "a98", "a99", "a100", "a101", "a102", "a103", "a104", "a105", "a106", "a107", "a108", "a109", "a110", "a111", "a112", "a113", "a114", "a115", "a116", "a117", "a118", "a119", "a120", "a121", "a122", "a123", "a124", "a125", "a126", "a127", "a128", "a129", "a130", "a131", "a132", "a133", "a134", "a135", "a136", "a137", "a138", "a139", "a140", "a141", "a142", "a143", "a144", "a145", "a146", "a147", "a148", "a149", "a150", "a151", "a152", "a153", "a154", "a155", "a156", "a157", "a158", "a159", "a160", "a161", "a162", "a163", "a164", "a165", "a166", "a167", "a168", "a169", "a170", "a171", "a172", "a173", "a174", "a175", "a176", "a177", "a178", "a179", "a180", "a181", "a182", "a183", "a184", "a185", "a186", "a187", "a188", "a189", "a190", "a191", "a192", "a193", "a194", "a195", "a196", "a197", "a198", "a199", "a200", "a201", "a202", "a203", "a204", "a205", "a206", "a207", "a208", "a209", "a210", "a211", "a212", "a213", "a214", "a215", "a216", "a217", "a218", "a219", "a220", "a221", "a222", "a223", "a224", "a225", "a226", "a227", "a228", "a229", "a230", "a231", "a232", "a233", "a234", "a235", "a236", "a237", "a238", "a239", "a240", "a241", "a242", "a243", "a244", "a245", "a246", "a247", "a248", "a249", "a250", "a251", "a252", "a253", "a254", "a255", "s3", "s4", "s5", "s6", "s7", "s8", "s9", "s10", "s11", "s12", "s13", "s14", "s15", "s16", "s17", "s18", "s19", "s20", "s21", "s22", "s23", "s24", "s25", "s26", "s27", "s28", "s29", "s30", "s31", "s36", "s37", "s38", "s39", "s40", "s41", "s42", "s43", "s44", "s45", "s46", "s47", "s48", "s49", "s50", "s51", "s52", "s53", "s54", "s55", "s56", "s57", "s58", "s59", "s60", "s61", "s62", "s63", "s64", "s65", "s66", "s67", "s68", "s69", "s70", "s71", "s72", "s73", "s74", "s75", "s76", "s77", "s78", "s79", "s80", "s81", "s82", "s83", "s84", "s85", "s86", "s87", "s88", "s89", "s90", "s91", "s92", "s93", "s94", "s95", "s96", "s97", "s98", "s99", "vcc", "scc", "memory");
}

extern "C" void kernel_launch(void* const* d_in, const int* in_sizes, int n_in, void* d_out, int out_size, void* d_ws, size_t ws_size,
                              hipStream_t stream) {
  (void)in_sizes; (void)n_in; (void)out_size; (void)ws_size;
  (void)hipFuncSetAttribute((const void*)fused_block_kernel, hipFuncAttributeMaxDynamicSharedMemorySize, 163840);
  fused_block_kernel<<<256, 256, 163840, stream>>>(d_in[0], d_in[1], d_in[2], d_in[4], d_in[5], d_in[6], d_out, d_ws);
}
```
